```python
import torch

def _softmax(x, axis=-1):
    return torch.softmax(x, dim=axis)
def _gelu(x):
    return torch.nn.functional.gelu(x, approximate="tanh")
def _silu(x):
    return torch.nn.functional.silu(x)
def _sigmoid(x):
    return torch.sigmoid(x)
def _layernorm(x, axis=-1, eps=1e-5):
    m = x.mean(dim=axis, keepdim=True)
    v = x.var(dim=axis, keepdim=True, unbiased=False)
    return (x - m) / torch.sqrt(v + eps)
def _scatter_add(tgt, idx, src, axis=0):
    return tgt.clone().index_add_(axis, idx, src)
def _segment_sum(src, seg, n):
    out = torch.zeros((n,) + src.shape[1:], dtype=src.dtype)
    return out.index_add_(0, seg, src)
def _pad(x, pw):
    flat = tuple(v for pair in reversed(pw) for v in pair)
    return torch.nn.functional.pad(x, flat)

kernel_name = 'gated_mix__sliding_local_dilated__mha_block__prompt'

def setup_inputs():

    _w = (lambda a: (lambda t: t.to(torch.bfloat16) if t.is_floating_point() else t)(torch.as_tensor(a)))
    def _A_setup():
        B, S, D = 1, 2048, 640
        return {
            "x": _w((lambda *s: torch.randn(*s, dtype=torch.bfloat16) * 0.05)(B, S, D)),
            "wqkv": _w((lambda *s: torch.randn(*s, dtype=torch.bfloat16) * 1.6)(D, 3 * D)),
            "wo": _w((lambda *s: torch.randn(*s, dtype=torch.bfloat16) * 0.05)(D, D)),
        }
    _A_in = _A_setup()

    def _B_setup():
        B, S, D, H = 1, 2048, 640, 32
        return {
            "x": _w((lambda *s: torch.randn(*s, dtype=torch.bfloat16) * 0.05)(B, S, D)),
            "wqkv": _w((lambda *s: torch.randn(*s, dtype=torch.bfloat16) * 1.6)(D, 3 * D)),
            "wo": _w((lambda *s: torch.randn(*s, dtype=torch.bfloat16) * 0.1)(D, D)),
        }
    _B_in = _B_setup()

    return {
        "A_x": _A_in["x"],
        "A_wqkv": _A_in["wqkv"],
        "A_wo": _A_in["wo"],
        "B_x": _B_in["x"],
        "B_wqkv": _B_in["wqkv"],
        "B_wo": _B_in["wo"],
        "gate_w": _w((lambda *s: torch.randn(*s, dtype=torch.bfloat16) * 0.05)(640) * 16.0),
    }

def reference(A_x, A_wqkv, A_wo, B_x, B_wqkv, B_wo, gate_w):
    def _A_ref(x, wqkv, wo):
        B, S, D = x.shape
        H = 32; Dh = D // H
        W, dil = 8, 2
        qkv = (lambda x, *d: x.permute(*d))((x @ wqkv).reshape(B, S, 3, H, Dh), 2, 0, 3, 1, 4)
        q, k, v = qkv[0], qkv[1], qkv[2]
        scale = Dh ** -0.5
        pos = torch.arange(S)
        off = torch.arange(W) * dil
        idx = pos[:, None] - off[None, :]
        mask = idx >= 0
        idx_c = torch.where(mask, idx, 0).reshape(-1)
        kg = k[:, :, idx_c, :].reshape(B, H, S, W, Dh)
        vg = v[:, :, idx_c, :].reshape(B, H, S, W, Dh)
        logits = torch.einsum("bhsd,bhswd->bhsw", q, kg) * scale
        logits = torch.where(mask[None, None, :, :], logits, -1e9)
        attn = _softmax(logits, axis=-1)
        o = torch.einsum("bhsw,bhswd->bhsd", attn, vg)
        return (lambda x, *d: x.permute(*d))(o, 0, 2, 1, 3).reshape(B, S, D) @ wo

    def _B_ref(x, wqkv, wo):
        B, S, D = x.shape
        H = 32
        Dh = D // H
        qkv = (lambda x, *d: x.permute(*d))((x @ wqkv).reshape(B, S, 3, H, Dh), 2, 0, 3, 1, 4)
        q, k, v = qkv[0], qkv[1], qkv[2]
        scores = torch.einsum("bhqd,bhkd->bhqk", q, k) * (Dh ** -0.5)
        p = _softmax(scores, axis=-1)
        o = torch.einsum("bhqk,bhkd->bhqd", p, v)
        return (lambda x, *d: x.permute(*d))(o, 0, 2, 1, 3).reshape(B, S, D) @ wo

    x = A_x
    g = _sigmoid((x * gate_w).sum(-1, keepdims=True))
    a = _A_ref(x, A_wqkv, A_wo)
    b = _B_ref(x, B_wqkv, B_wo)
    return g * a + (1 - g) * b
```

```cpp
#include <hip/hip_runtime.h>
#include <hip/hip_bf16.h>

typedef __attribute__((ext_vector_type(8))) __bf16 bf16x8;
typedef __attribute__((ext_vector_type(16))) __bf16 bf16x16;
typedef __attribute__((ext_vector_type(2))) __bf16 bf16x2;
typedef __attribute__((ext_vector_type(4))) short s16x4;
typedef __attribute__((ext_vector_type(8))) short s16x8;
typedef __attribute__((ext_vector_type(2))) float f32x2;
typedef __attribute__((ext_vector_type(4))) float f32x4;
typedef __attribute__((ext_vector_type(8))) float f32x8;
typedef __attribute__((ext_vector_type(16))) float f32x16;
typedef __attribute__((ext_vector_type(2))) unsigned u32x2;
typedef __attribute__((ext_vector_type(4))) unsigned u32x4;
typedef unsigned short u16;
#define LDS_AS __attribute__((address_space(3)))
typedef LDS_AS char* lds_ptr;

constexpr int SEQ = 2048, DM = 640, NH = 32, DH = 20;
constexpr size_t KV_TILE = 2560, KV_HEAD = 64 * KV_TILE;
constexpr size_t Q_TILE = 1280, Q_HEAD = 64 * Q_TILE;
constexpr size_t WS_KV = 0;
constexpr size_t WS_Q = WS_KV + 2 * NH * KV_HEAD;
constexpr size_t WS_O = WS_Q + 2 * NH * Q_HEAD;
constexpr size_t WS_G = WS_O + (size_t)SEQ * 2 * DM * 2;
constexpr float QSCALE = 0.22360679774997896f * 1.4426950408889634f;

#define WAIT_V(n) asm volatile("s_waitcnt vmcnt(" #n ")" ::: "memory")
#define RAW_BAR() asm volatile("s_waitcnt lgkmcnt(0)\n\ts_barrier" ::: "memory")
#define MFMA16(a, b, c) __builtin_amdgcn_mfma_f32_16x16x32_bf16(a, b, c, 0, 0, 0)
#define MFMA32(a, b, c) __builtin_amdgcn_mfma_f32_32x32x16_bf16(a, b, c, 0, 0, 0)

__device__ __forceinline__ unsigned pack2(float lo, float hi) {
    f32x2 v = {lo, hi};
    return __builtin_bit_cast(unsigned, __builtin_convertvector(v, bf16x2));
}
__device__ __forceinline__ void glds16(const char* sbase, unsigned voff, lds_ptr l) {
    unsigned keep;
    asm volatile("s_mov_b32 %0, m0\n\ts_mov_b32 m0, %3\n\ts_nop 0\n\tglobal_load_lds_dwordx4 %1, %2\n\ts_mov_b32 m0, %0"
                 : "=&s"(keep) : "v"(voff), "s"(sbase), "s"((unsigned)(size_t)l) : "memory");
}
__device__ __forceinline__ bf16x8 lds_b128(lds_ptr p) { return *(const LDS_AS bf16x8*)p; }
__device__ __forceinline__ s16x4 lds_tr(lds_ptr p) { return __builtin_amdgcn_ds_read_tr16_b64_v4i16((LDS_AS s16x4*)p); }
__device__ __forceinline__ bf16x8 cat8(s16x4 a, s16x4 b) {
    return __builtin_bit_cast(bf16x8, __builtin_shufflevector(a, b, 0, 1, 2, 3, 4, 5, 6, 7));
}
__device__ __forceinline__ int kperm(int rho) { return (rho & 32) + 8 * ((rho & 15) >> 2) + 4 * ((rho >> 4) & 1) + (rho & 3); }

constexpr int K1_STAGE = 16384 + 30720 + 512;

template <int JV>
__device__ __forceinline__ void k1_main(f32x4 (&acc)[4][4], lds_ptr lds, const char* xg, const char* wg, const unsigned (&xoff)[2],
                                        const unsigned (&woff)[4], int wid, int xa, int wa) {
    auto issue = [&](int t) {
        lds_ptr st = lds + (t % 3) * K1_STAGE;
        const char* xs = xg + t * 128;
        const char* wsrc = wg + (size_t)t * 64 * 3840;
#pragma unroll
        for (int i = 0; i < 2; ++i) glds16(xs, xoff[i], st + (wid * 2 + i) * 1024);
#pragma unroll
        for (int i = 0; i < 3; ++i) glds16(wsrc, woff[i], st + 16384 + (wid + 8 * i) * 1024);
        if (wid < 6) glds16(wsrc, woff[3], st + 16384 + (wid + 24) * 1024);
    };
    issue(0);
    issue(1);
#pragma unroll
    for (int t = 0; t < 10; ++t) {
        if (t < 9) { if (wid < 6) WAIT_V(6); else WAIT_V(5); } else WAIT_V(0);
        RAW_BAR();
        if (t + 2 < 10) issue(t + 2);
        lds_ptr st = lds + (t % 3) * K1_STAGE;
#pragma unroll
        for (int kk = 0; kk < 2; ++kk) {
            bf16x8 xf[4], wf[4];
#pragma unroll
            for (int j = 0; j < 4; ++j) {
                lds_ptr p = st + 16384 + wa + kk * 15360 + j * 32;
                wf[j] = cat8(lds_tr(p), lds_tr(p + 7680));
            }
#pragma unroll
            for (int i = 0; i < 4; ++i) xf[i] = lds_b128(st + (xa ^ (kk * 64)) + i * 2048);
#pragma unroll
            for (int i = 0; i < 4; ++i)
#pragma unroll
                for (int j = 0; j < 4; ++j)
                    acc[i][j] = j < JV ? MFMA16(wf[j], xf[i], acc[i][j]) : MFMA16(xf[i], wf[j], acc[i][j]);
        }
    }
}

__global__ __launch_bounds__(512, 2) void k1_qkv(const u16* __restrict__ x, const u16* __restrict__ wA, const u16* __restrict__ wB,
                                                  char* __restrict__ ws) {
    __shared__ __attribute__((aligned(16))) char smem[3 * K1_STAGE];
    lds_ptr lds = (lds_ptr)smem;
    const int tid = threadIdx.x, lane = tid & 63, wid = __builtin_amdgcn_readfirstlane(tid >> 6);
    const int xcd = blockIdx.x & 7, idx = blockIdx.x >> 3;
    const int mt = (xcd & 1) * 8 + (idx & 7), nt = (xcd >> 1) * 4 + (idx >> 3);
    const int br = nt >> 3, ncol0 = (nt & 7) * 240, m0 = mt * 128;
    const int wm = wid >> 2, wn = wid & 3;
    const char* xg = (const char*)(x + (size_t)m0 * DM);
    const char* wg = (const char*)((br ? wB : wA) + ncol0);
    unsigned xoff[2], woff[4];
#pragma unroll
    for (int i = 0; i < 2; ++i) {
        const int row = (wid * 2 + i) * 8 + (lane >> 3);
        xoff[i] = row * (DM * 2) + (((lane & 7) ^ (row & 7)) << 4);
    }
#pragma unroll
    for (int i = 0; i < 4; ++i) {
        int ci = (wid + 8 * i) * 64 + lane;
        if (ci >= 1920) ci = 0;
        const int rho = ci / 30, cc = ci - rho * 30;
        woff[i] = kperm(rho) * 3840 + cc * 16;
    }
    const int g = lane >> 4, r16 = lane & 15;
    const int xa = (wm * 64 + r16) * 128 + ((g ^ (lane & 7)) << 4);
    const int wa = (4 * g + (r16 >> 2)) * 480 + (wn * 64 + 4 * (lane & 3)) * 2;
    f32x4 acc[4][4];
#pragma unroll
    for (int i = 0; i < 4; ++i)
#pragma unroll
        for (int j = 0; j < 4; ++j) acc[i][j] = f32x4{0.f, 0.f, 0.f, 0.f};
    const int c0 = ncol0 + wn * 64;
    const int jv = c0 >= 1280 ? 0 : (c0 + 64 <= 1280 + 15 ? 4 : (1280 - c0) >> 4);
    if (jv == 0) k1_main<0>(acc, lds, xg, wg, xoff, woff, wid, xa, wa);
    else if (jv == 1) k1_main<1>(acc, lds, xg, wg, xoff, woff, wid, xa, wa);
    else k1_main<4>(acc, lds, xg, wg, xoff, woff, wid, xa, wa);

    char* kv = ws + WS_KV + (size_t)br * NH * KV_HEAD;
    char* qi = ws + WS_Q + (size_t)br * NH * Q_HEAD;
#pragma unroll
    for (int j = 0; j < 4; ++j) {
        if (wn == 3 && j == 3) break;
        const int cs = c0 + j * 16;
        if (cs < 1280) {
            const bool isq = cs < 640;
            const int c = cs - (isq ? 0 : 640) + 4 * g, head = c / 20, d0 = c - head * 20;
            char* base = isq ? qi + (size_t)head * Q_HEAD : kv + (size_t)head * KV_HEAD;
            const int tstride = isq ? (int)Q_TILE : (int)KV_TILE;
            const float sc = isq ? QSCALE : 1.f;
#pragma unroll
            for (int i = 0; i < 4; ++i) {
                const int tok = m0 + wm * 64 + i * 16 + r16, tr = tok & 31;
                const int off = (tok >> 5) * tstride + (d0 < 16 ? (d0 >> 3) * 512 + tr * 16 + (d0 & 7) * 2 : 1024 + tr * 8);
                u32x2 v = {pack2(acc[i][j][0] * sc, acc[i][j][1] * sc), pack2(acc[i][j][2] * sc, acc[i][j][3] * sc)};
                *(u32x2*)(base + off) = v;
            }
        } else {
            const int c = cs - 1280 + r16, head = c / 20, d = c - head * 20;
            char* base = kv + (size_t)head * KV_HEAD + 1280 + d * 16 + (g >> 1) * 320 + (g & 1) * 8;
#pragma unroll
            for (int i = 0; i < 4; ++i) {
                const int tok = m0 + wm * 64 + i * 16;
                u32x2 v = {pack2(acc[i][j][0], acc[i][j][1]), pack2(acc[i][j][2], acc[i][j][3])};
                *(u32x2*)(base + (tok >> 5) * (int)KV_TILE + ((tok >> 4) & 1) * 640) = v;
            }
        }
    }
}

#define SMFMAC(a, b8, c) __builtin_amdgcn_smfmac_f32_32x32x32_bf16(a, __builtin_bit_cast(bf16x16, b8), c, (int)0xDDDDDDDD, 0, 0)
#define LO8(s) __builtin_shufflevector(s, s, 0, 1, 2, 3, 4, 5, 6, 7)
#define HI8(s) __builtin_shufflevector(s, s, 8, 9, 10, 11, 12, 13, 14, 15)
constexpr unsigned ONE2 = 0x3F803F80u;

__device__ __forceinline__ float xhalf_max(float m) {
    auto r = __builtin_amdgcn_permlane32_swap(__float_as_uint(m), __float_as_uint(m), false, false);
    return fmaxf(__uint_as_float(r[0]), __uint_as_float(r[1]));
}
__device__ __forceinline__ float max16(const f32x16& s, float m) {
#pragma unroll
    for (int r = 0; r < 16; ++r) m = fmaxf(m, s[r]);
    return m;
}
__device__ __forceinline__ bool store_o(const f32x16& o, u16* dst, int hh, bool force) {
    auto r = __builtin_amdgcn_permlane32_swap(__float_as_uint(o[8]), __float_as_uint(o[8]), false, false);
    const float l = __uint_as_float(r[1]);
    float amax = 0.f;
#pragma unroll
    for (int i = 0; i < 8; ++i) amax = fmaxf(amax, fabsf(o[i]));
    if (!hh) {
#pragma unroll
        for (int i = 8; i < 12; ++i) amax = fmaxf(amax, fabsf(o[i]));
    }
    const bool ok = l >= 0x1p-64f && l <= 0x1p64f && amax <= 0x1p126f;
    if (!force && !__all(ok)) return false;
    const float inv = 1.f / l;
    u32x2 v0 = {pack2(o[0] * inv, o[1] * inv), pack2(o[2] * inv, o[3] * inv)};
    u32x2 v1 = {pack2(o[4] * inv, o[5] * inv), pack2(o[6] * inv, o[7] * inv)};
    u32x2 v2 = {pack2(o[8] * inv, o[9] * inv), pack2(o[10] * inv, o[11] * inv)};
    *(u32x2*)(dst + hh * 4) = v0;
    *(u32x2*)(dst + 8 + hh * 4) = v1;
    if (!hh) *(u32x2*)(dst + 16) = v2;
    return true;
}

#define S2(x) #x
#define STR(x) S2(x)
#define EXP4(C, a, b, c, d) "v_exp_f32 v[" #C "+" #a "], v[" #C "+" #a "]\n\tv_exp_f32 v[" #C "+" #b "], v[" #C "+" #b "]\n\t" \
                            "v_exp_f32 v[" #C "+" #c "], v[" #C "+" #c "]\n\tv_exp_f32 v[" #C "+" #d "], v[" #C "+" #d "]\n\t"
#define STEP(C, P, T)                                                                              \
    "s_waitcnt lgkmcnt(3)\n\t"                                                                     \
    "v_smfmac_f32_32x32x32_bf16 v[80:95], v[36:39], v[" #P "+8:" #P "+15], v40\n\t"                \
    "s_mov_b64 exec, %[vm]\n\t"                                                                    \
    "ds_read_b128 v[36:39], v43 offset:" #T "*2560+1920\n\t"                                       \
    "s_mov_b64 exec, -1\n\t"                                                                       \
    EXP4(C, 0, 1, 2, 3)                                                                            \
    "s_waitcnt lgkmcnt(3)\n\t"                                                                     \
    "v_mfma_f32_32x32x16_bf16 v[" #P ":" #P "+15], v[24:27], v[16:19], 0\n\t"                      \
    "ds_read_b128 v[24:27], v41 offset:(" #T "+2)*2560\n\t"                                        \
    EXP4(C, 4, 5, 6, 7)                                                                            \
    "s_waitcnt lgkmcnt(3)\n\t"                                                                     \
    "v_mfma_f32_32x32x16_bf16 v[" #P ":" #P "+15], v[28:31], v[20:23], v[" #P ":" #P "+15]\n\t"    \
    "ds_read_b64 v[28:29], v42 offset:(" #T "+2)*2560+1024\n\t"                                    \
    EXP4(C, 8, 9, 10, 11)                                                                          \
    "s_waitcnt lgkmcnt(3)\n\t"                                                                     \
    "v_smfmac_f32_32x32x32_bf16 v[80:95], v[32:35], v[" #C ":" #C "+7], v40\n\t"                   \
    EXP4(C, 12, 13, 14, 15)                                                                        \
    "s_mov_b64 exec, %[vm]\n\t"                                                                    \
    "ds_read_b128 v[32:35], v43 offset:(" #T "+1)*2560+1280\n\t"                                   \
    "s_mov_b64 exec, -1\n\t"
#define STEP_62(C, P, T)                                                                           \
    "s_waitcnt lgkmcnt(3)\n\t"                                                                     \
    "v_smfmac_f32_32x32x32_bf16 v[80:95], v[36:39], v[" #P "+8:" #P "+15], v40\n\t"                \
    "s_mov_b64 exec, %[vm]\n\t"                                                                    \
    "ds_read_b128 v[36:39], v43 offset:" #T "*2560+1920\n\t"                                       \
    "s_mov_b64 exec, -1\n\t"                                                                       \
    EXP4(C, 0, 1, 2, 3)                                                                            \
    "s_waitcnt lgkmcnt(1)\n\t"                                                                     \
    "v_mfma_f32_32x32x16_bf16 v[" #P ":" #P "+15], v[24:27], v[16:19], 0\n\t"                      \
    EXP4(C, 4, 5, 6, 7)                                                                            \
    "v_mfma_f32_32x32x16_bf16 v[" #P ":" #P "+15], v[28:31], v[20:23], v[" #P ":" #P "+15]\n\t"    \
    EXP4(C, 8, 9, 10, 11)                                                                          \
    "v_smfmac_f32_32x32x32_bf16 v[80:95], v[32:35], v[" #C ":" #C "+7], v40\n\t"                   \
    EXP4(C, 12, 13, 14, 15)                                                                        \
    "s_mov_b64 exec, %[vm]\n\t"                                                                    \
    "ds_read_b128 v[32:35], v43 offset:(" #T "+1)*2560+1280\n\t"                                   \
    "s_mov_b64 exec, -1\n\t"
#define STEP_63(C, P, T)                                                                           \
    "s_waitcnt lgkmcnt(1)\n\t"                                                                     \
    "v_smfmac_f32_32x32x32_bf16 v[80:95], v[36:39], v[" #P "+8:" #P "+15], v40\n\t"                \
    "s_mov_b64 exec, %[vm]\n\t"                                                                    \
    "ds_read_b128 v[36:39], v43 offset:" #T "*2560+1920\n\t"                                       \
    "s_mov_b64 exec, -1\n\t"                                                                       \
    EXP4(C, 0, 1, 2, 3)                                                                            \
    EXP4(C, 4, 5, 6, 7)                                                                            \
    EXP4(C, 8, 9, 10, 11)                                                                          \
    "s_waitcnt lgkmcnt(1)\n\t"                                                                     \
    "v_smfmac_f32_32x32x32_bf16 v[80:95], v[32:35], v[" #C ":" #C "+7], v40\n\t"                   \
    EXP4(C, 12, 13, 14, 15)                                                                        \
    "s_waitcnt lgkmcnt(0)\n\t"                                                                     \
    "s_nop 1\n\t"                                                                                  \
    "v_smfmac_f32_32x32x32_bf16 v[80:95], v[36:39], v[" #C "+8:" #C "+15], v40\n\t"                \
    "s_nop 15\n\t"

__global__ __launch_bounds__(768, 3) void k2_attn(const u16* __restrict__ x, const u16* __restrict__ gate_w, char* __restrict__ ws) {
    __shared__ __attribute__((aligned(16))) char smem[KV_HEAD];
    lds_ptr lds = (lds_ptr)smem;
    const int tid = threadIdx.x, lane = tid & 63, wid = __builtin_amdgcn_readfirstlane(tid >> 6);
    const int head = (blockIdx.x & 7) * 4 + ((blockIdx.x >> 3) & 3), qb = blockIdx.x >> 5;
    if (wid >= 8) {
        const int hw = wid - 8;
        const char* src = ws + WS_KV + (size_t)(NH + head) * KV_HEAD + hw * 1024;
#pragma unroll
        for (int i = 0; i < 40; ++i) glds16(src + i * 4096, lane * 16, lds + hw * 1024 + i * 4096);
        WAIT_V(33); RAW_BAR();
        WAIT_V(28); RAW_BAR();
        WAIT_V(23); RAW_BAR();
        WAIT_V(0); RAW_BAR();
#pragma unroll
        for (int i = 0; i < 2; ++i) {
            const int tok = blockIdx.x * 8 + hw * 2 + i;
            float s = 0.f;
#pragma unroll
            for (int c = 0; c < 2; ++c) {
                const int ch = lane + 64 * c;
                if (ch < 80) {
                    const u32x4 xv = *(const u32x4*)(x + (size_t)tok * DM + ch * 8), gv = *(const u32x4*)(gate_w + ch * 8);
#pragma unroll
                    for (int e = 0; e < 4; ++e) {
                        s = fmaf(__uint_as_float(xv[e] << 16), __uint_as_float(gv[e] << 16), s);
                        s = fmaf(__uint_as_float(xv[e] & 0xffff0000u), __uint_as_float(gv[e] & 0xffff0000u), s);
                    }
                }
            }
#pragma unroll
            for (int o = 32; o; o >>= 1) s += __shfl_xor(s, o);
            if (lane == 0) ((float*)(ws + WS_G))[tok] = 1.f / (1.f + __expf(-s));
        }
        return;
    }
    const int r32 = lane & 31, hh = lane >> 5, qt = qb * 8 + wid;
    const u32x4 vfill = r32 == 20 ? u32x4{ONE2, ONE2, ONE2, ONE2} : u32x4{0u, 0u, 0u, 0u};
    u16* orow = (u16*)(ws + WS_O) + (size_t)(qt * 32 + r32) * (2 * DM) + head * DH;
    const int voff = hh * 320 + (r32 < 20 ? r32 : 0) * 16;
    const char* qB = ws + WS_Q + (size_t)(NH + head) * Q_HEAD + qt * Q_TILE;
    const u32x4 qa = *(const u32x4*)(qB + lane * 16);
    u32x2 qb2 = *(const u32x2*)(qB + 1024 + r32 * 8);
    {
        const char* qA = ws + WS_Q + (size_t)head * Q_HEAD + qt * Q_TILE;
        const char* k1 = ws + WS_KV + (size_t)head * KV_HEAD + qt * KV_TILE;
        const char* k0 = qt ? k1 - KV_TILE : k1;
        const u32x4 aqa = *(const u32x4*)(qA + lane * 16);
        u32x2 aqb = *(const u32x2*)(qA + 1024 + r32 * 8);
        const u32x4 ka0 = *(const u32x4*)(k0 + lane * 16), ka1 = *(const u32x4*)(k1 + lane * 16);
        const u32x2 kb0 = *(const u32x2*)(k0 + 1024 + r32 * 8), kb1 = *(const u32x2*)(k1 + 1024 + r32 * 8);
        u32x4 v0h = *(const u32x4*)(k0 + 1920 + voff), v1l = *(const u32x4*)(k1 + 1280 + voff), v1h = *(const u32x4*)(k1 + 1920 + voff);
        if (r32 >= 20) { v0h = vfill; v1l = vfill; v1h = vfill; }
        if (hh) aqb = u32x2{0u, 0u};
        const bf16x8 fqa = __builtin_bit_cast(bf16x8, aqa), fqb = __builtin_bit_cast(bf16x8, u32x4{aqb[0], aqb[1], 0u, 0u});
        f32x16 s0 = MFMA32(__builtin_bit_cast(bf16x8, ka0), fqa, f32x16{});
        f32x16 s1 = MFMA32(__builtin_bit_cast(bf16x8, ka1), fqa, f32x16{});
        s0 = MFMA32(__builtin_bit_cast(bf16x8, u32x4{kb0[0], kb0[1], 0u, 0u}), fqb, s0);
        s1 = MFMA32(__builtin_bit_cast(bf16x8, u32x4{kb1[0], kb1[1], 0u, 0u}), fqb, s1);
        unsigned long long msk = 0x15554ull << r32;
        if (qt == 0) msk &= ~0xffffull;
        msk >>= 4 * hh;
        const unsigned mlo = (unsigned)msk, mhi = (unsigned)(msk >> 32);
        float m = -INFINITY;
#pragma unroll
        for (int r = 8; r < 16; ++r) {
            const int u = (r & 3) + 8 * (r >> 2) - 16;
            s0[r] = (mlo >> u) & 1 ? s0[r] : -INFINITY;
            m = fmaxf(m, s0[r]);
        }
#pragma unroll
        for (int r = 0; r < 16; ++r) {
            const int u = (r & 3) + 8 * (r >> 2) + 16;
            s1[r] = ((u < 32 ? mlo >> u : mhi >> (u - 32)) & 1) ? s1[r] : -INFINITY;
            m = fmaxf(m, s1[r]);
        }
        m = xhalf_max(m);
#pragma unroll
        for (int r = 8; r < 16; ++r) s0[r] = __builtin_amdgcn_exp2f(s0[r] - m);
#pragma unroll
        for (int r = 0; r < 16; ++r) s1[r] = __builtin_amdgcn_exp2f(s1[r] - m);
        f32x16 o = SMFMAC(__builtin_bit_cast(bf16x8, v0h), HI8(s0), f32x16{});
        o = SMFMAC(__builtin_bit_cast(bf16x8, v1l), LO8(s1), o);
        o = SMFMAC(__builtin_bit_cast(bf16x8, v1h), HI8(s1), o);
        store_o(o, orow, hh, true);
    }
    if (hh) qb2 = u32x2{0u, 0u};
    const bf16x8 fqa = __builtin_bit_cast(bf16x8, qa), fqb0 = __builtin_bit_cast(bf16x8, u32x4{qb2[0], qb2[1], 0u, 0u});
    lds_ptr ka_p = lds + lane * 16, kb_p = lds + 1024 + r32 * 8;
    auto tile_max = [&](int t, float m) {
        const u32x2 kb = *(const LDS_AS u32x2*)(kb_p + t * KV_TILE);
        f32x16 s = MFMA32(lds_b128(ka_p + t * KV_TILE), fqa, f32x16{});
        s = MFMA32(__builtin_bit_cast(bf16x8, u32x4{kb[0], kb[1], 0u, 0u}), fqb0, s);
        return max16(s, m);
    };
    RAW_BAR();
    float m = xhalf_max(tile_max(0, -INFINITY));
    const unsigned long long vmask = 0x000fffff000fffffull;
    for (int attempt = 0;; ++attempt) {
        const unsigned h1 = __float_as_uint(-m) & 0xffff0000u;
        const float r1 = -m - __uint_as_float(h1);
        const unsigned h2 = __float_as_uint(r1) & 0xffff0000u;
        const unsigned h3 = __float_as_uint(r1 - __uint_as_float(h2)) >> 16;
        u32x4 qbs = {qb2[0], qb2[1], hh ? 0u : (h1 | (h2 >> 16)), hh ? 0u : h3};
        f32x16 o = {}, s1 = {};
        u32x4 vlo = vfill, vhi = vfill;
        unsigned a_ka = (unsigned)(size_t)ka_p, a_kb = (unsigned)(size_t)(lds + r32 * 8), a_v = (unsigned)(size_t)(lds + voff);
        int nbar = attempt ? 0 : 3, it = 7;
        asm volatile(
            "v_mov_b32 v30, %[one]\n\tv_mov_b32 v31, %[one]\n\tv_mov_b32 v40, %[sidx]\n\t"
            "ds_read_b128 v[24:27], v41\n\t"
            "ds_read_b64 v[28:29], v42 offset:1024\n\t"
            "s_waitcnt lgkmcnt(0)\n\t"
            "v_mfma_f32_32x32x16_bf16 v[48:63], v[24:27], v[16:19], 0\n\t"
            "v_mfma_f32_32x32x16_bf16 v[48:63], v[28:31], v[20:23], v[48:63]\n\t"
            "ds_read_b128 v[24:27], v41 offset:2560\n\t"
            "ds_read_b64 v[28:29], v42 offset:2560+1024\n\t"
            "s_mov_b64 exec, %[vm]\n\t"
            "ds_read_b128 v[32:35], v43 offset:1280\n\t"
            "s_mov_b64 exec, -1\n\t"
            "s_nop 7\n\t"
            "1:\n\t"
            STEP(48, 64, 0) STEP(64, 48, 1) STEP(48, 64, 2) STEP(64, 48, 3)
            STEP(48, 64, 4) STEP(64, 48, 5) STEP(48, 64, 6) STEP(64, 48, 7)
            "v_add_u32 v41, 20480, v41\n\tv_add_u32 v42, 20480, v42\n\tv_add_u32 v43, 20480, v43\n\t"
            "s_cmp_lt_i32 %[nbar], 1\n\t"
            "s_cbranch_scc1 2f\n\t"
            "s_barrier\n\t"
            "2:\n\t"
            "s_sub_i32 %[nbar], %[nbar], 1\n\t"
            "s_sub_i32 %[it], %[it], 1\n\t"
            "s_cmp_lg_u32 %[it], 0\n\t"
            "s_cbranch_scc1 1b\n\t"
            STEP(48, 64, 0) STEP(64, 48, 1) STEP(48, 64, 2) STEP(64, 48, 3)
            STEP(48, 64, 4) STEP(64, 48, 5) STEP_62(48, 64, 6) STEP_63(64, 48, 7)
            : "+{v[80:95]}"(o), "+{v[64:79]}"(s1), "+{v[32:35]}"(vlo), "+{v[36:39]}"(vhi), "+{v41}"(a_ka), "+{v42}"(a_kb), "+{v43}"(a_v),
              [nbar] "+s"(nbar), [it] "+s"(it)
            : "{v[16:19]}"(qa), "{v[20:23]}"(qbs), [vm] "s"(vmask), [one] "s"(ONE2), [sidx] "s"(0xDDDDDDDDu)
            : "v24", "v25", "v26", "v27", "v28", "v29", "v30", "v31", "v40", "v48", "v49", "v50", "v51", "v52", "v53", "v54", "v55",
              "v56", "v57", "v58", "v59", "v60", "v61", "v62", "v63", "scc", "memory");
        if (store_o(o, orow + DM, hh, attempt != 0)) break;
        m = -INFINITY;
        for (int t = 0; t < 64; ++t) m = tile_max(t, m);
        m = xhalf_max(m);
    }
}

constexpr int K3_STAGE = 16384 + 20480;

__global__ __launch_bounds__(512, 2) void k3_out(const u16* __restrict__ woA, const u16* __restrict__ woB, const char* __restrict__ ws,
                                                  u16* __restrict__ out) {
    __shared__ __attribute__((aligned(16))) char smem[4 * K3_STAGE];
    lds_ptr lds = (lds_ptr)smem;
    const int tid = threadIdx.x, lane = tid & 63, wid = __builtin_amdgcn_readfirstlane(tid >> 6);
    const int xcd = blockIdx.x & 7, idx = blockIdx.x >> 3;
    const int mt = (xcd & 3) * 8 + (idx & 7), nt = (xcd >> 2) * 4 + (idx >> 3);
    const int m0 = mt * 64, n0 = nt * 80;
    const int wm = wid & 1, br = (wid >> 1) & 1, kk = wid >> 2;
    const char* og = ws + WS_O + (size_t)m0 * (4 * DM);
    unsigned xoff[2];
    unsigned woff[3];
    const char* wsrc[3];
#pragma unroll
    for (int i = 0; i < 2; ++i) {
        const int p = wid * 2 + i, row = (p & 7) * 8 + (lane >> 3);
        xoff[i] = row * (4 * DM) + (p >> 3) * (2 * DM) + (((lane & 7) ^ (row & 7)) << 4);
    }
#pragma unroll
    for (int i = 0; i < 3; ++i) {
        int p = wid + 8 * i;
        if (p >= 20) p = 0;
        const int b = p >= 10, ci = (p - 10 * b) * 64 + lane, rho = ci / 10, cc = ci - rho * 10;
        wsrc[i] = (const char*)((b ? woB : woA) + n0);
        woff[i] = kperm(rho) * (DM * 2) + cc * 16;
    }
    auto issue = [&](int t) {
        lds_ptr st = lds + (t & 3) * K3_STAGE;
#pragma unroll
        for (int i = 0; i < 2; ++i) glds16(og + t * 128, xoff[i], st + (wid * 2 + i) * 1024);
#pragma unroll
        for (int i = 0; i < 2; ++i) glds16(wsrc[i] + t * (64 * DM * 2), woff[i], st + 16384 + (wid + 8 * i) * 1024);
        if (wid < 4) glds16(wsrc[2] + t * (64 * DM * 2), woff[2], st + 16384 + (wid + 16) * 1024);
    };
    const int g = lane >> 4, r16 = lane & 15;
    const int xa = br * 8192 + (wm * 32 + r16) * 128 + (((kk * 4 + g) ^ (lane & 7)) << 4);
    const int wa = 16384 + br * 10240 + (kk * 32 + 4 * g + (r16 >> 2)) * 160 + 8 * (lane & 3);
    f32x4 acc[2][5];
#pragma unroll
    for (int i = 0; i < 2; ++i)
#pragma unroll
        for (int j = 0; j < 5; ++j) acc[i][j] = f32x4{0.f, 0.f, 0.f, 0.f};
    issue(0);
    issue(1);
    issue(2);
#pragma unroll
    for (int t = 0; t < 10; ++t) {
        if (t < 8) { if (wid < 4) WAIT_V(10); else WAIT_V(8); }
        else if (t == 8) { if (wid < 4) WAIT_V(5); else WAIT_V(4); }
        else WAIT_V(0);
        RAW_BAR();
        if (t + 3 < 10) issue(t + 3);
        lds_ptr st = lds + (t & 3) * K3_STAGE;
        bf16x8 xf[2], wf[5];
#pragma unroll
        for (int i = 0; i < 2; ++i) xf[i] = lds_b128(st + xa + i * 2048);
#pragma unroll
        for (int j = 0; j < 5; ++j) wf[j] = cat8(lds_tr(st + wa + j * 32), lds_tr(st + wa + j * 32 + 2560));
#pragma unroll
        for (int i = 0; i < 2; ++i)
#pragma unroll
            for (int j = 0; j < 5; ++j) acc[i][j] = MFMA16(wf[j], xf[i], acc[i][j]);
    }
    RAW_BAR();
    const float* gate = (const float*)(ws + WS_G) + m0;
    const int part = wid >> 1;
#pragma unroll
    for (int i = 0; i < 2; ++i) {
        const int row = wm * 32 + i * 16 + r16;
        const float gv = gate[row], f = br ? 1.f - gv : gv;
#pragma unroll
        for (int j = 0; j < 5; ++j) *(LDS_AS f32x4*)(lds + (part * 64 + row) * 336 + (j * 16 + 4 * g) * 4) = acc[i][j] * f;
    }
    RAW_BAR();
#pragma unroll
    for (int it = 0; it < 2; ++it) {
        const int c = tid + it * 512;
        if (c < 640) {
            const int row = c / 10, cc = c - row * 10;
            f32x4 a = {0.f, 0.f, 0.f, 0.f}, b = a;
#pragma unroll
            for (int p = 0; p < 4; ++p) {
                lds_ptr q = lds + (p * 64 + row) * 336 + cc * 32;
                a += *(const LDS_AS f32x4*)q;
                b += *(const LDS_AS f32x4*)(q + 16);
            }
            u32x4 v = {pack2(a[0], a[1]), pack2(a[2], a[3]), pack2(b[0], b[1]), pack2(b[2], b[3])};
            *(u32x4*)(out + (size_t)(m0 + row) * DM + n0 + cc * 8) = v;
        }
    }
}

extern "C" void kernel_launch(void* const* d_in, const int* in_sizes, int n_in, void* d_out, int out_size, void* d_ws, size_t ws_size,
                              hipStream_t stream) {
    const u16* x = (const u16*)d_in[0];
    const u16* wqkvA = (const u16*)d_in[1];
    const u16* woA = (const u16*)d_in[2];
    const u16* wqkvB = (const u16*)d_in[4];
    const u16* woB = (const u16*)d_in[5];
    const u16* gate_w = (const u16*)d_in[6];
    char* ws = (char*)d_ws;
    k1_qkv<<<256, 512, 0, stream>>>(x, wqkvA, wqkvB, ws);
    k2_attn<<<256, 768, 0, stream>>>(x, gate_w, ws);
    k3_out<<<256, 512, 0, stream>>>(woA, woB, ws, (u16*)d_out);
}
```

```cpp
#include <hip/hip_runtime.h>
#include <hip/hip_bf16.h>

typedef __attribute__((ext_vector_type(8))) __bf16 bf16x8;
typedef __attribute__((ext_vector_type(16))) __bf16 bf16x16;
typedef __attribute__((ext_vector_type(2))) __bf16 bf16x2;
typedef __attribute__((ext_vector_type(4))) short s16x4;
typedef __attribute__((ext_vector_type(8))) short s16x8;
typedef __attribute__((ext_vector_type(2))) float f32x2;
typedef __attribute__((ext_vector_type(4))) float f32x4;
typedef __attribute__((ext_vector_type(8))) float f32x8;
typedef __attribute__((ext_vector_type(16))) float f32x16;
typedef __attribute__((ext_vector_type(2))) unsigned u32x2;
typedef __attribute__((ext_vector_type(4))) unsigned u32x4;
typedef unsigned short u16;
#define LDS_AS __attribute__((address_space(3)))
typedef LDS_AS char* lds_ptr;

constexpr int SEQ = 2048, DM = 640, NH = 32, DH = 20;
constexpr size_t KV_TILE = 2560, KV_HEAD = 64 * KV_TILE;
constexpr size_t Q_TILE = 1280, Q_HEAD = 64 * Q_TILE;
constexpr size_t WS_KV = 0;
constexpr size_t WS_Q = WS_KV + 2 * NH * KV_HEAD;
constexpr size_t O_BLK = 80 * 256;
constexpr size_t WS_O = WS_Q + 2 * NH * Q_HEAD;
constexpr size_t WS_WO = WS_O + 2 * 128 * O_BLK;
constexpr size_t WS_G = WS_WO + 40 * 40 * 1024;
constexpr size_t WS_FA = WS_G + SEQ * 4;
constexpr size_t WS_FB = WS_FA + 8 * 32 * 4;
constexpr size_t WS_END = WS_FB + 8 * 32 * 4;
#define STAMP_DECL
#define STAMP(k, i)
#define FLUSH(k)
#define DIAG_WAIT()
constexpr float QSCALE = 0.22360679774997896f * 1.4426950408889634f;

#define K1_PF 3
#define PF_DONE() asm volatile("s_waitcnt vmcnt(0)" : "+v"(d0) :: "memory")
#define PRIO_SW 5
#define WAIT_V(n) asm volatile("s_waitcnt vmcnt(" #n ")" ::: "memory")
#define RAW_BAR() asm volatile("s_waitcnt lgkmcnt(0)\n\ts_barrier" ::: "memory")
#define MFMA16(a, b, c) __builtin_amdgcn_mfma_f32_16x16x32_bf16(a, b, c, 0, 0, 0)
#define MFMA32(a, b, c) __builtin_amdgcn_mfma_f32_32x32x16_bf16(a, b, c, 0, 0, 0)

__device__ __forceinline__ unsigned pack2(float lo, float hi) {
    f32x2 v = {lo, hi};
    return __builtin_bit_cast(unsigned, __builtin_convertvector(v, bf16x2));
}
__device__ __forceinline__ void glds16(const char* sbase, unsigned voff, lds_ptr l) {
    unsigned keep;
    asm volatile("s_mov_b32 %0, m0\n\ts_mov_b32 m0, %3\n\ts_nop 0\n\tglobal_load_lds_dwordx4 %1, %2\n\ts_mov_b32 m0, %0"
                 : "=&s"(keep) : "v"(voff), "s"(sbase), "s"((unsigned)(size_t)l) : "memory");
}
__device__ __forceinline__ void glds16v(const char* g, lds_ptr l) {
    unsigned keep;
    asm volatile("s_mov_b32 %0, m0\n\ts_mov_b32 m0, %2\n\ts_nop 0\n\tglobal_load_lds_dwordx4 %1, off\n\ts_mov_b32 m0, %0"
                 : "=&s"(keep) : "v"(g), "s"((unsigned)(size_t)l) : "memory");
}
__device__ __forceinline__ bf16x8 lds_b128(lds_ptr p) { return *(const LDS_AS bf16x8*)p; }
__device__ __forceinline__ s16x4 lds_tr(lds_ptr p) { return __builtin_amdgcn_ds_read_tr16_b64_v4i16((LDS_AS s16x4*)p); }
__device__ __forceinline__ bf16x8 cat8(s16x4 a, s16x4 b) {
    return __builtin_bit_cast(bf16x8, __builtin_shufflevector(a, b, 0, 1, 2, 3, 4, 5, 6, 7));
}
__device__ __forceinline__ int kperm(int rho) { return (rho & 32) + 8 * ((rho & 15) >> 2) + 4 * ((rho >> 4) & 1) + (rho & 3); }

constexpr int K1_WP = 1024 + 32;
constexpr int K1_STAGE = 16384 + 32 * K1_WP;
#define SB() __builtin_amdgcn_sched_barrier(0)

template <bool VT>
__device__ __forceinline__ void k1_main(f32x4 (&acc)[4][4], lds_ptr lds, int xa, int wa, const char* tp, int tstep, unsigned& d0) {
#define TOUCH() asm volatile("global_load_dword %0, %1, off" : "+v"(d0) : "v"(tp) : "memory")
    bf16x8 xf[2][4], wf[2][4];
    auto rd = [&](int so, int kk) {
#pragma unroll
        for (int j = 0; j < 4; ++j) {
            lds_ptr p = lds + so + wa + kk * (16 * K1_WP) + j * 32;
            wf[kk][j] = cat8(lds_tr(p), lds_tr(p + 512));
        }
#pragma unroll
        for (int i = 0; i < 4; ++i) xf[kk][i] = lds_b128(lds + so + (xa ^ (kk * 64)) + i * 2048);
    };
    auto mm = [&](int kk) {
#pragma unroll
        for (int i = 0; i < 4; ++i)
#pragma unroll
            for (int j = 0; j < 4; ++j)
                acc[i][j] = VT ? MFMA16(xf[kk][i], wf[kk][j], acc[i][j]) : MFMA16(wf[kk][j], xf[kk][i], acc[i][j]);
    };
#define ILV() do { _Pragma("unroll") for (int q_ = 0; q_ < 12; ++q_) { __builtin_amdgcn_sched_group_barrier(0x100, 1, 0); __builtin_amdgcn_sched_group_barrier(0x8, 1, 0); } __builtin_amdgcn_sched_group_barrier(0x8, 4, 0); SB(); } while (0)
    if (K1_PF && tstep < 0) TOUCH();
    RAW_BAR();
    if (K1_PF && tstep == 0) TOUCH();
    rd(0, 0); SB();
    rd(0, 1);
    mm(0); ILV();
    int so = K1_STAGE;
#pragma unroll 1
    for (int t = 1; t < 10; ++t) {
        RAW_BAR();
        if (K1_PF && tstep == t) TOUCH();
        rd(so, 0);
        mm(1); ILV();
        rd(so, 1);
        mm(0); ILV();
        so = so == 2 * K1_STAGE ? 0 : so + K1_STAGE;
    }
    mm(1);
}

__global__ __launch_bounds__(768, 3) void k1_qkv(const u16* __restrict__ x, const u16* __restrict__ wA, const u16* __restrict__ wB,
                                                  const u16* __restrict__ woA, const u16* __restrict__ woB,
                                                  const u16* __restrict__ gate_w, char* __restrict__ ws) {
    __shared__ __attribute__((aligned(16))) char smem[3 * K1_STAGE];
    lds_ptr lds = (lds_ptr)smem;
    const int tid = threadIdx.x, lane = tid & 63, wid = __builtin_amdgcn_readfirstlane(tid >> 6);
    STAMP_DECL STAMP(0, 0);
    const int xcd = blockIdx.x & 7, idx = blockIdx.x >> 3;
    const int mt = (xcd & 1) * 8 + (idx & 7), nt = (xcd >> 1) * 4 + (idx >> 3), m0 = mt * 128;
    if (nt == 15) {
        if (wid < 10) {
            const int task = mt * 10 + wid, ks = task >> 2, cg = task & 3;
            const char* wsrc = (const char*)((ks >= 20 ? woB : woA) + cg * 160);
            lds_ptr st = lds + wid * 10240;
#pragma unroll
            for (int i = 0; i < 10; ++i) {
                const int ci = i * 64 + lane, slot = ci / 20, cc = ci - slot * 20;
                const int c = (ks >= 20 ? ks - 20 : ks) * 4 + (slot >> 3), e = slot & 7, p = c / 5, r = c - p * 5;
                const int k = r < 4 ? (2 * p + (r >> 1)) * 20 + 4 * (r & 1) + (e & 3) + 8 * (e >> 2) : (2 * p + (e >> 2)) * 20 + 16 + (e & 3);
                glds16(wsrc, k * (DM * 2) + cc * 16, st + i * 1024);
            }
            WAIT_V(0);
            lds_ptr rp = st + (8 * (lane >> 4) + ((lane & 15) >> 2)) * 320 + (lane & 3) * 8;
            char* dst = ws + WS_WO + (size_t)(ks * 40 + cg * 10) * 1024 + lane * 16;
#pragma unroll
            for (int j = 0; j < 10; ++j) *(bf16x8*)(dst + j * 1024) = cat8(lds_tr(rp + j * 32), lds_tr(rp + j * 32 + 4 * 320));
        } else if (mt == 0) {
            for (int i = (wid - 10) * 64 + lane; i < (int)((WS_END - WS_FA) / 16); i += 128) *(u32x4*)(ws + WS_FA + i * 16) = u32x4{0u, 0u, 0u, 0u};
        }
        STAMP(0, 4);
        u32x4 gv[2];
        gv[0] = *(const u32x4*)(gate_w + lane * 8);
        gv[1] = lane < 16 ? *(const u32x4*)(gate_w + 512 + lane * 8) : u32x4{0u, 0u, 0u, 0u};
#pragma unroll 1
        for (int r0 = wid; r0 < 128; r0 += 48) {
            float s[4];
#pragma unroll
            for (int u = 0; u < 4; ++u) {
                const int row = r0 + 12 * u < 128 ? r0 + 12 * u : r0;
                const u16* xr = x + (size_t)(m0 + row) * DM;
                const u32x4 xv[2] = {*(const u32x4*)(xr + lane * 8), *(const u32x4*)(xr + 512 + (lane & 15) * 8)};
                float a = 0.f;
#pragma unroll
                for (int c = 0; c < 2; ++c)
#pragma unroll
                    for (int e = 0; e < 4; ++e) {
                        a = fmaf(__uint_as_float(xv[c][e] << 16), __uint_as_float(gv[c][e] << 16), a);
                        a = fmaf(__uint_as_float(xv[c][e] & 0xffff0000u), __uint_as_float(gv[c][e] & 0xffff0000u), a);
                    }
                s[u] = a;
            }
#pragma unroll
            for (int o = 32; o; o >>= 1)
#pragma unroll
                for (int u = 0; u < 4; ++u) s[u] += __shfl_xor(s[u], o);
#pragma unroll
            for (int u = 0; u < 4; ++u)
                if (lane == 0 && r0 + 12 * u < 128) ((float*)(ws + WS_G))[m0 + r0 + 12 * u] = 1.f / (1.f + __expf(-s[u]));
        }
        DIAG_WAIT(); STAMP(0, 5); FLUSH(0);
        return;
    }
    if (wid >= 8) {
        const int lw = wid - 8;
        const char* xg = (const char*)(x + (size_t)m0 * DM);
        const int gc = nt * 256 + (lane & 31) * 8;
        const bool b = gc < 2560 ? gc >= 1280 : gc >= 3200;
        const int col = gc < 2560 ? gc - (b ? 1280 : 0) : gc - (b ? 3200 : 2560) + 1280;
        const char* wl = (const char*)(b ? wB : wA) + col * 2 + (size_t)(lw + 4 * (lane >> 5)) * 3840;
        const unsigned xoff = (lw * 8 + (lane >> 3)) * (DM * 2) + (((lane & 7) ^ ((lane >> 3) & 7)) << 4);
        auto issue = [&](int t, int so) {
            lds_ptr st = lds + so;
#pragma unroll
            for (int i = 0; i < 4; ++i) glds16(xg + t * 128 + i * (32 * DM * 2), xoff, st + (lw + 4 * i) * 1024);
#pragma unroll
            for (int i = 0; i < 8; ++i) glds16v(wl + (size_t)(t * 64 + i * 8) * 3840, st + 16384 + (lw + 4 * i) * K1_WP);
        };
        issue(0, 0);
        issue(1, K1_STAGE);
        int so = 2 * K1_STAGE;
#pragma unroll 1
        for (int t = 0; t < 9; ++t) {
            WAIT_V(12);
            if (t == 0) STAMP(0, 1);
            RAW_BAR();
            if (t < 8) issue(t + 2, so);
            so = so == 2 * K1_STAGE ? 0 : so + K1_STAGE;
        }
        WAIT_V(0);
        STAMP(0, 2);
        RAW_BAR();
        FLUSH(0);
        return;
    }
    const char* tp;
    if (lane < 16) {
        const int gc = nt * 256 + (lane & 3) * 64;
        const bool b = gc < 2560 ? gc >= 1280 : gc >= 3200;
        const int col = gc < 2560 ? gc - (b ? 1280 : 0) : gc - (b ? 3200 : 2560) + 1280;
        tp = (const char*)(b ? wB : wA) + (size_t)((wid + 2) * 64 + mt * 4 + (lane >> 2)) * 3840 + col * 2;
    } else {
        const int r = nt * 9 + (lane < 25 ? lane - 16 : 0);
        tp = (const char*)(x + (size_t)(m0 + (r < 127 ? r : 127)) * DM) + (wid + 2) * 128;
    }
    unsigned d0 = 0;
    const int wm = wid >> 2, wn = wid & 3;
    const int g = lane >> 4, r16 = lane & 15;
    const int xa = (wm * 64 + r16) * 128 + ((g ^ (lane & 7)) << 4);
    const int wa = 16384 + (4 * g + (r16 >> 2)) * K1_WP + (wn * 64 + 4 * (lane & 3)) * 2;
    f32x4 acc[4][4];
#pragma unroll
    for (int i = 0; i < 4; ++i)
#pragma unroll
        for (int j = 0; j < 4; ++j) acc[i][j] = f32x4{0.f, 0.f, 0.f, 0.f};
    const int tile0 = mt * 4 + wm * 2;
    if (nt < 10) {
        k1_main<false>(acc, lds, xa, wa, tp, wid + 2 - K1_PF, d0);
        STAMP(0, 1);
        PF_DONE();
        const int br = nt >= 5;
        const int cw = (nt - 5 * br) * 256 + wn * 64;
        const bool isq = cw < 640;
        const int tstride = isq ? (int)Q_TILE : (int)KV_TILE;
        char* base = ws + (isq ? WS_Q : WS_KV) + (size_t)(br * NH * 64 + tile0) * tstride;
        const float sc = isq ? QSCALE : 1.f;
#pragma unroll
        for (int j = 0; j < 4; ++j) {
            const int c4 = (cw - (isq ? 0 : 640)) / 4 + j * 4 + g, head = c4 / 5, d4 = c4 - head * 5;
            const int rs = d4 < 4 ? 16 : 8;
            const unsigned off = head * 64 * tstride + (d4 < 4 ? (d4 >> 1) * 512 + (d4 & 1) * 8 : 1024) + r16 * rs;
#pragma unroll
            for (int i = 0; i < 4; ++i) {
                u32x2 v = {pack2(acc[i][j][0] * sc, acc[i][j][1] * sc), pack2(acc[i][j][2] * sc, acc[i][j][3] * sc)};
                *(u32x2*)(base + (i >> 1) * tstride + (off + (i & 1) * 16 * rs)) = v;
            }
        }
    } else {
        k1_main<true>(acc, lds, xa, wa, tp, wid + 2 - K1_PF, d0);
        STAMP(0, 1);
        PF_DONE();
        char* base = ws + WS_KV + (size_t)tile0 * KV_TILE + 1280;
#pragma unroll
        for (int j = 0; j < 4; ++j) {
            const int c = (nt - 10) * 256 + wn * 64 + j * 16 + r16, head = c / 20, d = c - head * 20;
            const unsigned off = head * (int)KV_HEAD + d * 16 + (g >> 1) * 320 + (g & 1) * 8;
#pragma unroll
            for (int i = 0; i < 4; ++i) {
                u32x2 v = {pack2(acc[i][j][0], acc[i][j][1]), pack2(acc[i][j][2], acc[i][j][3])};
                *(u32x2*)(base + (i >> 1) * (int)KV_TILE + (i & 1) * 640 + off) = v;
            }
        }
    }
    STAMP(0, 2);
    DIAG_WAIT();
    STAMP(0, 3); FLUSH(0);
}

#define SMFMAC(a, b8, c) __builtin_amdgcn_smfmac_f32_32x32x32_bf16(a, __builtin_bit_cast(bf16x16, b8), c, (int)0xDDDDDDDD, 0, 0)
#define LO8(s) __builtin_shufflevector(s, s, 0, 1, 2, 3, 4, 5, 6, 7)
#define HI8(s) __builtin_shufflevector(s, s, 8, 9, 10, 11, 12, 13, 14, 15)
constexpr unsigned ONE2 = 0x3F803F80u;

__device__ __forceinline__ float xhalf_max(float m) {
    auto r = __builtin_amdgcn_permlane32_swap(__float_as_uint(m), __float_as_uint(m), false, false);
    return fmaxf(__uint_as_float(r[0]), __uint_as_float(r[1]));
}
__device__ __forceinline__ float max3f(float a, float b, float c) { return __builtin_fmaxf(__builtin_fmaxf(a, b), c); }
__device__ __forceinline__ float max16(const f32x16& s, float m) {
#pragma unroll
    for (int r = 0; r < 16; r += 2) m = max3f(m, s[r], s[r + 1]);
    return m;
}
typedef __amdgpu_buffer_rsrc_t rsrc_t;
typedef __attribute__((address_space(1))) unsigned gu32;
#define RLX_AGENT __ATOMIC_RELAXED, __HIP_MEMORY_SCOPE_AGENT
constexpr int SC1 = 16;
__device__ __forceinline__ float row_sum(const f32x16& o) {
    auto r = __builtin_amdgcn_permlane32_swap(__float_as_uint(o[8]), __float_as_uint(o[8]), false, false);
    return __uint_as_float(r[1]);
}
__device__ __forceinline__ void store_o(const f32x16& o, float f, rsrc_t rs, unsigned off, int head, int hh) {
    u32x4 v = {pack2(o[0] * f, o[1] * f), pack2(o[2] * f, o[3] * f), pack2(o[4] * f, o[5] * f), pack2(o[6] * f, o[7] * f)};
    u32x2 t = {pack2(o[8] * f, o[9] * f), pack2(o[10] * f, o[11] * f)};
    const int c0 = (head >> 1) * 5;
    __builtin_amdgcn_raw_buffer_store_b128(v, rs, off + hh * 256, (c0 + (head & 1) * 2) * 256, SC1);
    if (!hh) __builtin_amdgcn_raw_buffer_store_b64(t, rs, off, (c0 + 4) * 256 + (head & 1) * 8, SC1);
}
__device__ __forceinline__ void wait_flags(gu32* w) {
#pragma unroll 1
    for (unsigned spins = 0; spins < (1u << 20); ++spins) {
        if (__all(__hip_atomic_load(w, RLX_AGENT) != 0u)) break;
        __builtin_amdgcn_s_sleep(2);
    }
}

#define S2(x) #x
#define STR(x) S2(x)
#define EXP4(C, a, b, c, d) "v_exp_f32 v[" #C "+" #a "], v[" #C "+" #a "]\n\tv_exp_f32 v[" #C "+" #b "], v[" #C "+" #b "]\n\t" \
                            "v_exp_f32 v[" #C "+" #c "], v[" #C "+" #c "]\n\tv_exp_f32 v[" #C "+" #d "], v[" #C "+" #d "]\n\t"
#define VAR 0
#define STEP(C, P, T)                                                                              \
    "s_waitcnt lgkmcnt(3)\n\t"                                                                     \
    "v_smfmac_f32_32x32x32_bf16 v[80:95], v[36:39], v[" #P "+8:" #P "+15], v40\n\t"                \
    "s_mov_b64 exec, %[vm]\n\t"                                                                    \
    "ds_read_b128 v[36:39], v43 offset:" #T "*2560+1920\n\t"                                       \
    "s_mov_b64 exec, -1\n\t"                                                                       \
    EXP4(C, 0, 1, 2, 3)                                                                            \
    "s_waitcnt lgkmcnt(3)\n\t"                                                                     \
    "v_mfma_f32_32x32x16_bf16 v[" #P ":" #P "+15], v[24:27], v[16:19], 0\n\t"                      \
    "ds_read_b128 v[24:27], v41 offset:(" #T "+2)*2560\n\t"                                        \
    EXP4(C, 4, 5, 6, 7)                                                                            \
    "s_waitcnt lgkmcnt(3)\n\t"                                                                     \
    "v_mfma_f32_32x32x16_bf16 v[" #P ":" #P "+15], v[28:31], v[20:23], v[" #P ":" #P "+15]\n\t"    \
    "ds_read_b64 v[28:29], v42 offset:(" #T "+2)*2560+1024\n\t"                                    \
    EXP4(C, 8, 9, 10, 11)                                                                          \
    "s_waitcnt lgkmcnt(3)\n\t"                                                                     \
    "v_smfmac_f32_32x32x32_bf16 v[80:95], v[32:35], v[" #C ":" #C "+7], v40\n\t"                   \
    EXP4(C, 12, 13, 14, 15)                                                                        \
    "s_mov_b64 exec, %[vm]\n\t"                                                                    \
    "ds_read_b128 v[32:35], v43 offset:(" #T "+1)*2560+1280\n\t"                                   \
    "s_mov_b64 exec, -1\n\t"
#define PRIO(n, L) "s_cmp_ge_u32 %[psw], 4\n\ts_cbranch_scc1 " #L "f\n\ts_setprio " #n "\n\t" #L ":\n\t"
#define PRIO_A
#define PRIO_B PRIO(0, 4)
#define STEP_62(C, P, T)                                                                           \
    "s_waitcnt lgkmcnt(3)\n\t"                                                                     \
    "v_smfmac_f32_32x32x32_bf16 v[80:95], v[36:39], v[" #P "+8:" #P "+15], v40\n\t"                \
    "s_mov_b64 exec, %[vm]\n\t"                                                                    \
    "ds_read_b128 v[36:39], v43 offset:" #T "*2560+1920\n\t"                                       \
    "s_mov_b64 exec, -1\n\t"                                                                       \
    EXP4(C, 0, 1, 2, 3)                                                                            \
    "s_waitcnt lgkmcnt(1)\n\t"                                                                     \
    "v_mfma_f32_32x32x16_bf16 v[" #P ":" #P "+15], v[24:27], v[16:19], 0\n\t"                      \
    EXP4(C, 4, 5, 6, 7)                                                                            \
    "v_mfma_f32_32x32x16_bf16 v[" #P ":" #P "+15], v[28:31], v[20:23], v[" #P ":" #P "+15]\n\t"    \
    EXP4(C, 8, 9, 10, 11)                                                                          \
    "v_smfmac_f32_32x32x32_bf16 v[80:95], v[32:35], v[" #C ":" #C "+7], v40\n\t"                   \
    EXP4(C, 12, 13, 14, 15)                                                                        \
    "s_mov_b64 exec, %[vm]\n\t"                                                                    \
    "ds_read_b128 v[32:35], v43 offset:(" #T "+1)*2560+1280\n\t"                                   \
    "s_mov_b64 exec, -1\n\t"
#define STEP_63(C, P, T)                                                                           \
    "s_waitcnt lgkmcnt(1)\n\t"                                                                     \
    "v_smfmac_f32_32x32x32_bf16 v[80:95], v[36:39], v[" #P "+8:" #P "+15], v40\n\t"                \
    "s_mov_b64 exec, %[vm]\n\t"                                                                    \
    "ds_read_b128 v[36:39], v43 offset:" #T "*2560+1920\n\t"                                       \
    "s_mov_b64 exec, -1\n\t"                                                                       \
    EXP4(C, 0, 1, 2, 3)                                                                            \
    EXP4(C, 4, 5, 6, 7)                                                                            \
    EXP4(C, 8, 9, 10, 11)                                                                          \
    "s_waitcnt lgkmcnt(1)\n\t"                                                                     \
    "v_smfmac_f32_32x32x32_bf16 v[80:95], v[32:35], v[" #C ":" #C "+7], v40\n\t"                   \
    EXP4(C, 12, 13, 14, 15)                                                                        \
    "s_waitcnt lgkmcnt(0)\n\t"                                                                     \
    "s_nop 1\n\t"                                                                                  \
    "v_smfmac_f32_32x32x32_bf16 v[80:95], v[36:39], v[" #C "+8:" #C "+15], v40\n\t"                \
    "s_nop 15\n\t"

__global__ __launch_bounds__(768, 3) void k2_attn(char* __restrict__ ws, u16* __restrict__ out) {
    __shared__ __attribute__((aligned(16))) char smem[KV_HEAD];
    lds_ptr lds = (lds_ptr)smem;
    const int tid = threadIdx.x, lane = tid & 63, wid = __builtin_amdgcn_readfirstlane(tid >> 6);
    const int head = (blockIdx.x & 7) * 4 + ((blockIdx.x >> 3) & 3), qb = blockIdx.x >> 5;
    const int omt = head, ont = qb;
    const int r32 = lane & 31, hh = lane >> 5;
    const u32x4 vfill = r32 == 20 ? u32x4{ONE2, ONE2, ONE2, ONE2} : u32x4{0u, 0u, 0u, 0u};
    const int voff = hh * 320 + (r32 < 20 ? r32 : 0) * 16;
    const rsrc_t ors = __builtin_amdgcn_make_buffer_rsrc(ws + WS_O, 0, (int)(2 * 128 * O_BLK), 0x00020000);
    const unsigned olane = (r32 >> 4) * (int)O_BLK + (r32 & 15) * 16;
    const float* gate = (const float*)(ws + WS_G);
    const char* wimg = ws + WS_WO + ont * 5120 + lane * 16;
    const int g = lane >> 4, r16 = lane & 15;
    STAMP_DECL STAMP(1, 0);
    if (wid >= 8) {
        const int hw = wid - 8;
        __builtin_amdgcn_s_setprio(3);
        const char* src = ws + WS_KV + (size_t)(NH + head) * KV_HEAD + hw * 1024;
#define LOADS(a, b) _Pragma("unroll 1") for (int i = a; i < b; ++i) glds16(src + i * 4096, lane * 16, lds + hw * 1024 + i * 4096)
        LOADS(0, 12);
        WAIT_V(5); RAW_BAR();
        LOADS(12, 40);
        f32x16 c0, c1;
        {
            const unsigned long long msk = ~(0x15554ull << r32) >> (4 * hh);
            const unsigned mlo = (unsigned)msk, mhi = (unsigned)(msk >> 32);
#pragma unroll
            for (int r = 0; r < 16; ++r) {
                const int u = (r & 3) + 8 * (r >> 2);
                c0[r] = r < 8 ? 0.f : __uint_as_float(__builtin_amdgcn_sbfe(mlo, (u - 16) & 31, 1) & 0xff800000u);
                c1[r] = __uint_as_float(__builtin_amdgcn_sbfe(u < 16 ? mlo : mhi, (u + 16) & 31, 1) & 0xff800000u);
            }
        }
        WAIT_V(28); RAW_BAR();
        WAIT_V(0); RAW_BAR();
        STAMP(1, 1);
#pragma unroll 1
        for (int it = 0; it < 2; ++it) {
            const int qt = qb * 8 + hw * 2 + 1 - it;
            const char* qA = ws + WS_Q + (size_t)head * Q_HEAD + qt * Q_TILE;
            const char* k1 = ws + WS_KV + (size_t)head * KV_HEAD + qt * KV_TILE;
            const char* k0 = qt ? k1 - KV_TILE : k1;
            const u32x4 aqa = *(const u32x4*)(qA + lane * 16);
            u32x2 aqb = {0u, 0u};
            if (!hh) aqb = *(const u32x2*)(qA + 1024 + r32 * 8);
            const u32x4 ka0 = *(const u32x4*)(k0 + lane * 16), ka1 = *(const u32x4*)(k1 + lane * 16);
            const u32x2 kb0 = *(const u32x2*)(k0 + 1024 + r32 * 8), kb1 = *(const u32x2*)(k1 + 1024 + r32 * 8);
            u32x4 v0h = vfill, v1l = vfill, v1h = vfill;
            if (r32 < 20) { v0h = *(const u32x4*)(k0 + 1920 + voff); v1l = *(const u32x4*)(k1 + 1280 + voff); v1h = *(const u32x4*)(k1 + 1920 + voff); }
            const float gv = gate[qt * 32 + r32];
            if (qt == 0) {
#pragma unroll
                for (int r = 8; r < 16; ++r) c0[r] = -INFINITY;
            }
            const bf16x8 fqa = __builtin_bit_cast(bf16x8, aqa), fqb = __builtin_bit_cast(bf16x8, u32x4{aqb[0], aqb[1], 0u, 0u});
            f32x16 s0 = MFMA32(__builtin_bit_cast(bf16x8, ka0), fqa, c0);
            f32x16 s1 = MFMA32(__builtin_bit_cast(bf16x8, ka1), fqa, c1);
            s0 = MFMA32(__builtin_bit_cast(bf16x8, u32x4{kb0[0], kb0[1], 0u, 0u}), fqb, s0);
            s1 = MFMA32(__builtin_bit_cast(bf16x8, u32x4{kb1[0], kb1[1], 0u, 0u}), fqb, s1);
            float m = max3f(s0[8], s0[9], s0[10]);
            m = max3f(m, s0[11], s0[12]); m = max3f(m, s0[13], s0[14]);
            m = xhalf_max(max16(s1, fmaxf(m, s0[15])));
#pragma unroll
            for (int r = 8; r < 16; ++r) s0[r] = __builtin_amdgcn_exp2f(s0[r] - m);
#pragma unroll
            for (int r = 0; r < 16; ++r) s1[r] = __builtin_amdgcn_exp2f(s1[r] - m);
            f32x16 o = SMFMAC(__builtin_bit_cast(bf16x8, v0h), HI8(s0), f32x16{});
            o = SMFMAC(__builtin_bit_cast(bf16x8, v1l), LO8(s1), o);
            o = SMFMAC(__builtin_bit_cast(bf16x8, v1h), HI8(s1), o);
            store_o(o, gv * __builtin_amdgcn_rcpf(row_sum(o)), ors, qt * 2 * (int)O_BLK + olane, head, hh);
        }
        WAIT_V(0);
        STAMP(1, 2);
        RAW_BAR();
        if (hw == 0 && lane == 0) __hip_atomic_store((gu32*)(ws + WS_FA) + qb * 32 + head, 1u, RLX_AGENT);
        wait_flags((gu32*)(ws + WS_FA) + (omt >> 2) * 32 + r32);
        __builtin_amdgcn_fence(__ATOMIC_ACQUIRE, "wavefront");
        STAMP(1, 3);
        f32x4 acc[4][5];
#pragma unroll
        for (int i = 0; i < 4; ++i)
#pragma unroll
            for (int j = 0; j < 5; ++j) acc[i][j] = f32x4{0.f, 0.f, 0.f, 0.f};
#pragma unroll 1
        for (int ks = hw * 5; ks < hw * 5 + 5; ++ks) {
            bf16x8 of[4], wf[5];
#pragma unroll
            for (int i = 0; i < 4; ++i)
                of[i] = __builtin_bit_cast(bf16x8, __builtin_amdgcn_raw_buffer_load_b128(ors, lane * 16, (omt * 4 + i) * (int)O_BLK + ks * 1024, SC1));
#pragma unroll
            for (int j = 0; j < 5; ++j) wf[j] = *(const bf16x8*)(wimg + ks * 40960 + j * 1024);
#pragma unroll
            for (int i = 0; i < 4; ++i)
#pragma unroll
                for (int j = 0; j < 5; ++j) acc[i][j] = MFMA16(wf[j], of[i], acc[i][j]);
        }
        STAMP(1, 4);
        RAW_BAR();
        if (hw == 0 && lane == 0) __hip_atomic_store((gu32*)(ws + WS_FB) + qb * 32 + head, 1u, RLX_AGENT);
        STAMP(1, 5);
#pragma unroll
        for (int i = 0; i < 4; ++i)
#pragma unroll
            for (int j = 0; j < 5; ++j)
                *(LDS_AS f32x4*)(lds + hw * 20480 + (i * 5 + j) * 1024 + g * 256 + ((r16 + 2 * j + (g >> 1)) & 15) * 16) = acc[i][j];
        if (hw == 0) wait_flags((gu32*)(ws + WS_FB) + (omt >> 2) * 32 + r32);
        STAMP(1, 6);
        RAW_BAR();
    } else {
    const int qt = qb * 8 + wid;
    const char* qB = ws + WS_Q + (size_t)(NH + head) * Q_HEAD + qt * Q_TILE;
    const u32x4 qa = *(const u32x4*)(qB + lane * 16);
    u32x2 qb2 = *(const u32x2*)(qB + 1024 + r32 * 8);
    const float gB = 1.f - gate[qt * 32 + r32];
    if (hh) qb2 = u32x2{0u, 0u};
    const bf16x8 fqa = __builtin_bit_cast(bf16x8, qa), fqb0 = __builtin_bit_cast(bf16x8, u32x4{qb2[0], qb2[1], 0u, 0u});
    lds_ptr ka_p = lds + lane * 16, kb_p = lds + 1024 + r32 * 8;
    auto tile_max = [&](int t, float m) {
        const u32x2 kb = *(const LDS_AS u32x2*)(kb_p + t * KV_TILE);
        f32x16 s = MFMA32(lds_b128(ka_p + t * KV_TILE), fqa, f32x16{});
        s = MFMA32(__builtin_bit_cast(bf16x8, u32x4{kb[0], kb[1], 0u, 0u}), fqb0, s);
        return max16(s, m);
    };
    RAW_BAR();
    STAMP(1, 1);
    float m = xhalf_max(tile_max(0, -INFINITY));
    const unsigned long long vmask = 0x000fffff000fffffull;
    int attempt = 0;
    asm volatile("" : "+s"(attempt));
#pragma unroll 1
    for (;; ++attempt) {
        const unsigned h1 = __float_as_uint(-m) & 0xffff0000u;
        const float r1 = -m - __uint_as_float(h1);
        const unsigned h2 = __float_as_uint(r1) & 0xffff0000u;
        const unsigned h3 = __float_as_uint(r1 - __uint_as_float(h2)) >> 16;
        u32x4 qbs = {qb2[0], qb2[1], hh ? 0u : (h1 | (h2 >> 16)), hh ? 0u : h3};
        f32x16 o = {}, s1 = {};
        u32x4 vlo = vfill, vhi = vfill;
        unsigned a_ka = (unsigned)(size_t)ka_p, a_kb = (unsigned)(size_t)(lds + r32 * 8), a_v = (unsigned)(size_t)(lds + voff);
        int nbar = attempt ? 0 : 0xB, it = 7;
        const int psw = PRIO_SW ? wid : 4;
        __builtin_amdgcn_s_setprio(1);
        asm volatile(
            "v_mov_b32 v30, %[one]\n\tv_mov_b32 v31, %[one]\n\tv_mov_b32 v40, %[sidx]\n\t"
            "ds_read_b128 v[24:27], v41\n\t"
            "ds_read_b64 v[28:29], v42 offset:1024\n\t"
            "s_waitcnt lgkmcnt(0)\n\t"
            "v_mfma_f32_32x32x16_bf16 v[48:63], v[24:27], v[16:19], 0\n\t"
            "v_mfma_f32_32x32x16_bf16 v[48:63], v[28:31], v[20:23], v[48:63]\n\t"
            "ds_read_b128 v[24:27], v41 offset:2560\n\t"
            "ds_read_b64 v[28:29], v42 offset:2560+1024\n\t"
            "s_mov_b64 exec, %[vm]\n\t"
            "ds_read_b128 v[32:35], v43 offset:1280\n\t"
            "s_mov_b64 exec, -1\n\t"
            "s_nop 7\n\t"
            "1:\n\t"
            PRIO(2, 3) STEP(48, 64, 0) STEP(64, 48, 1) STEP(48, 64, 2) STEP(64, 48, 3) PRIO_A
            STEP(48, 64, 4) PRIO_B STEP(64, 48, 5) STEP(48, 64, 6) STEP(64, 48, 7)
            "v_add_u32 v41, 20480, v41\n\tv_add_u32 v42, 20480, v42\n\tv_add_u32 v43, 20480, v43\n\t"
            "s_bitcmp0_b32 %[nbar], 0\n\t"
            "s_cbranch_scc1 2f\n\t"
            "s_barrier\n\t"
            "2:\n\t"
            "s_lshr_b32 %[nbar], %[nbar], 1\n\t"
            "s_sub_i32 %[it], %[it], 1\n\t"
            "s_cmp_lg_u32 %[it], 0\n\t"
            "s_cbranch_scc1 1b\n\t"
            PRIO(2, 3) STEP(48, 64, 0) STEP(64, 48, 1) STEP(48, 64, 2) STEP(64, 48, 3) PRIO_A
            STEP(48, 64, 4) PRIO_B STEP(64, 48, 5) STEP_62(48, 64, 6) STEP_63(64, 48, 7)
            : "+{v[80:95]}"(o), "+{v[64:79]}"(s1), "+{v[32:35]}"(vlo), "+{v[36:39]}"(vhi), "+{v41}"(a_ka), "+{v42}"(a_kb), "+{v43}"(a_v),
              [nbar] "+s"(nbar), [it] "+s"(it)
            : "{v[16:19]}"(qa), "{v[20:23]}"(qbs), [vm] "s"(vmask), [one] "s"(ONE2), [sidx] "s"(0xDDDDDDDDu), [psw] "s"(psw)
            : "v24", "v25", "v26", "v27", "v28", "v29", "v30", "v31", "v40", "v48", "v49", "v50", "v51", "v52", "v53", "v54", "v55",
              "v96", "v97", "v98", "v99", "v100", "v101", "v102", "v103", "v56", "v57", "v58", "v59", "v60", "v61", "v62", "v63", "scc", "memory");
        STAMP(1, 2);
        const float l = row_sum(o);
        float amax = hh ? 0.f : fmaxf(fmaxf(fabsf(o[8]), fabsf(o[9])), fmaxf(fabsf(o[10]), fabsf(o[11])));
#pragma unroll
        for (int i = 0; i < 8; ++i) amax = fmaxf(amax, fabsf(o[i]));
        if (attempt || __all(l >= 0x1p-64f && l <= 0x1p64f && amax <= 0x1p126f)) {
            store_o(o, gB * __builtin_amdgcn_rcpf(l), ors, (128 + qt * 2) * (int)O_BLK + olane, head, hh);
            break;
        }
        m = -INFINITY;
#pragma unroll 1
        for (int t = 0; t < 64; ++t) m = tile_max(t, m);
        m = xhalf_max(m);
    }
    WAIT_V(0);
    STAMP(1, 3);
    RAW_BAR();
    const int wm = wid & 1, kq = wid >> 1;
    const char* wsrc = wimg + (20 + kq * 5) * 40960;
    bf16x8 wf[5][5], of[5][2];
#pragma unroll
    for (int s = 0; s < 3; ++s)
#pragma unroll
        for (int j = 0; j < 5; ++j) wf[s][j] = *(const bf16x8*)(wsrc + s * 40960 + j * 1024);
    SB();
    RAW_BAR();
    STAMP(1, 4);
#pragma unroll
    for (int s = 0; s < 5; ++s)
#pragma unroll
        for (int i = 0; i < 2; ++i)
            of[s][i] = __builtin_bit_cast(bf16x8, __builtin_amdgcn_raw_buffer_load_b128(ors, lane * 16, (128 + omt * 4 + wm * 2 + i) * (int)O_BLK + (kq * 5 + s) * 1024, SC1));
    f32x4 acc[2][5];
    lds_ptr pp = lds + kq * 20480 + wm * 10240 + g * 256;
#pragma unroll
    for (int i = 0; i < 2; ++i)
#pragma unroll
        for (int j = 0; j < 5; ++j) acc[i][j] = *(const LDS_AS f32x4*)(pp + (i * 5 + j) * 1024 + ((r16 + 2 * j + (g >> 1)) & 15) * 16);
    SB();
#pragma unroll
    for (int s = 0; s < 5; ++s) {
#pragma unroll
        for (int i = 0; i < 2; ++i)
#pragma unroll
            for (int j = 0; j < 5; ++j) acc[i][j] = MFMA16(wf[s][j], of[s][i], acc[i][j]);
        if (s < 2) {
#pragma unroll
            for (int j = 0; j < 5; ++j) wf[s + 3][j] = *(const bf16x8*)(wsrc + (s + 3) * 40960 + j * 1024);
        }
        SB();
    }
#pragma unroll
    for (int i = 0; i < 2; ++i)
#pragma unroll
        for (int j = 0; j < 5; ++j) *(LDS_AS f32x4*)(pp + (i * 5 + j) * 1024 + ((r16 + 2 * j + (g >> 1)) & 15) * 16) = acc[i][j];
    STAMP(1, 5);
    }
    RAW_BAR();
    if (tid < 640) {
        const int row = tid / 10, c = tid - row * 10;
        lds_ptr q = lds + ((row >> 4) * 5 + (c >> 1)) * 1024 + (c & 1) * 512 + (((row & 15) + c) & 15) * 16;
        f32x4 a = {0.f, 0.f, 0.f, 0.f}, b = a;
#pragma unroll
        for (int p = 0; p < 4; ++p) {
            a += *(const LDS_AS f32x4*)(q + p * 20480);
            b += *(const LDS_AS f32x4*)(q + p * 20480 + 256);
        }
        u32x4 v = {pack2(a[0], a[1]), pack2(a[2], a[3]), pack2(b[0], b[1]), pack2(b[2], b[3])};
        *(u32x4*)(out + (size_t)(omt * 64 + row) * DM + ont * 80 + c * 8) = v;
    }
    DIAG_WAIT();
    STAMP(1, 7); FLUSH(1);
}

extern "C" void kernel_launch(void* const* d_in, const int* in_sizes, int n_in, void* d_out, int out_size, void* d_ws, size_t ws_size,
                              hipStream_t stream) {
    const u16* x = (const u16*)d_in[0];
    const u16* wqkvA = (const u16*)d_in[1];
    const u16* woA = (const u16*)d_in[2];
    const u16* wqkvB = (const u16*)d_in[4];
    const u16* woB = (const u16*)d_in[5];
    const u16* gate_w = (const u16*)d_in[6];
    char* ws = (char*)d_ws;
    k1_qkv<<<256, 768, 0, stream>>>(x, wqkvA, wqkvB, woA, woB, gate_w, ws);
    k2_attn<<<256, 768, 0, stream>>>(ws, (u16*)d_out);
}
```

```cpp
#include <hip/hip_runtime.h>
#include <hip/hip_bf16.h>

typedef __attribute__((ext_vector_type(8))) __bf16 bf16x8;
typedef __attribute__((ext_vector_type(16))) __bf16 bf16x16;
typedef __attribute__((ext_vector_type(2))) __bf16 bf16x2;
typedef __attribute__((ext_vector_type(4))) short s16x4;
typedef __attribute__((ext_vector_type(8))) short s16x8;
typedef __attribute__((ext_vector_type(2))) float f32x2;
typedef __attribute__((ext_vector_type(4))) float f32x4;
typedef __attribute__((ext_vector_type(8))) float f32x8;
typedef __attribute__((ext_vector_type(16))) float f32x16;
typedef __attribute__((ext_vector_type(2))) unsigned u32x2;
typedef __attribute__((ext_vector_type(4))) unsigned u32x4;
typedef unsigned short u16;
#define LDS_AS __attribute__((address_space(3)))
typedef LDS_AS char* lds_ptr;

constexpr int SEQ = 2048, DM = 640, NH = 32, DH = 20;
constexpr size_t KV_TILE = 2560, KV_HEAD = 64 * KV_TILE;
constexpr size_t Q_TILE = 1280, Q_HEAD = 64 * Q_TILE;
constexpr size_t WS_KV = 0;
constexpr size_t WS_Q = WS_KV + 2 * NH * KV_HEAD;
constexpr size_t O_BLK = 80 * 256;
constexpr size_t WS_O = WS_Q + 2 * NH * Q_HEAD;
constexpr size_t WS_WO = WS_O + 2 * 128 * O_BLK;
constexpr size_t WS_G = WS_WO + 40 * 40 * 1024;
constexpr size_t WS_F1 = WS_G + SEQ * 4;
constexpr size_t WS_FA = WS_F1 + 256 * 8;
constexpr size_t WS_FB = WS_FA + 256 * 8;
constexpr size_t WS_EPOCH = WS_FB + 256 * 8;
constexpr size_t WS_END = WS_EPOCH + 128;
#define STAMP_DECL
#define STAMP(k, i)
#define FLUSH(k)
#define DIAG_WAIT()
constexpr float QSCALE = 0.22360679774997896f * 1.4426950408889634f;

#define K1_PF 3
#define PF_DONE() asm volatile("s_waitcnt vmcnt(0)" : "+v"(d0) :: "memory")
#define PRIO_SW 5
#define WAIT_V(n) asm volatile("s_waitcnt vmcnt(" #n ")" ::: "memory")
#define RAW_BAR() asm volatile("s_waitcnt lgkmcnt(0)\n\ts_barrier" ::: "memory")
#define MFMA16(a, b, c) __builtin_amdgcn_mfma_f32_16x16x32_bf16(a, b, c, 0, 0, 0)
#define MFMA32(a, b, c) __builtin_amdgcn_mfma_f32_32x32x16_bf16(a, b, c, 0, 0, 0)

__device__ __forceinline__ unsigned pack2(float lo, float hi) {
    f32x2 v = {lo, hi};
    return __builtin_bit_cast(unsigned, __builtin_convertvector(v, bf16x2));
}
__device__ __forceinline__ void glds16(const char* sbase, unsigned voff, lds_ptr l) {
    unsigned keep;
    asm volatile("s_mov_b32 %0, m0\n\ts_mov_b32 m0, %3\n\ts_nop 0\n\tglobal_load_lds_dwordx4 %1, %2\n\ts_mov_b32 m0, %0"
                 : "=&s"(keep) : "v"(voff), "s"(sbase), "s"((unsigned)(size_t)l) : "memory");
}
__device__ __forceinline__ void glds16v(const char* g, lds_ptr l) {
    unsigned keep;
    asm volatile("s_mov_b32 %0, m0\n\ts_mov_b32 m0, %2\n\ts_nop 0\n\tglobal_load_lds_dwordx4 %1, off\n\ts_mov_b32 m0, %0"
                 : "=&s"(keep) : "v"(g), "s"((unsigned)(size_t)l) : "memory");
}
__device__ __forceinline__ bf16x8 lds_b128(lds_ptr p) { return *(const LDS_AS bf16x8*)p; }
__device__ __forceinline__ s16x4 lds_tr(lds_ptr p) { return __builtin_amdgcn_ds_read_tr16_b64_v4i16((LDS_AS s16x4*)p); }
__device__ __forceinline__ bf16x8 cat8(s16x4 a, s16x4 b) {
    return __builtin_bit_cast(bf16x8, __builtin_shufflevector(a, b, 0, 1, 2, 3, 4, 5, 6, 7));
}
typedef __amdgpu_buffer_rsrc_t rsrc_t;
typedef unsigned long long u64;
typedef __attribute__((address_space(1))) u64 gu64;
#define RLX_AGENT __ATOMIC_RELAXED, __HIP_MEMORY_SCOPE_AGENT
constexpr int SC1 = 16;
#define LD16(voff, soff) __builtin_amdgcn_raw_buffer_load_b128(wrs, voff, soff, SC1)
#define LD8(voff, soff) __builtin_amdgcn_raw_buffer_load_b64(wrs, voff, soff, SC1)
#define LD4(voff, soff) __builtin_amdgcn_raw_buffer_load_b32(wrs, voff, soff, SC1)
#define ST16(v, voff, soff) __builtin_amdgcn_raw_buffer_store_b128(v, wrs, (voff) + (soff), 0, SC1)
#define ST8(v, voff, soff) __builtin_amdgcn_raw_buffer_store_b64(v, wrs, voff, soff, SC1)
#define ST4(v, voff, soff) __builtin_amdgcn_raw_buffer_store_b32(v, wrs, voff, soff, SC1)
#define BF8(x) __builtin_bit_cast(bf16x8, x)
__device__ __forceinline__ void glds16_dev(const char* sbase, unsigned voff, lds_ptr l) {
    unsigned keep;
    asm volatile("s_mov_b32 %0, m0\n\ts_mov_b32 m0, %3\n\ts_nop 0\n\tglobal_load_lds_dwordx4 %1, %2 sc1\n\ts_mov_b32 m0, %0"
                 : "=&s"(keep) : "v"(voff), "s"(sbase), "s"((unsigned)(size_t)l) : "memory");
}
template <int N>
__device__ __forceinline__ void wait_flags(gu64* const (&w)[N], u64 tag) {
#pragma unroll 1
    for (unsigned spins = 0; spins < (1u << 20); ++spins) {
        bool ok = true;
#pragma unroll
        for (int i = 0; i < N; ++i) ok &= __hip_atomic_load(w[i], RLX_AGENT) == tag;
        if (__all(ok)) break;
        __builtin_amdgcn_s_sleep(2);
    }
    __builtin_amdgcn_fence(__ATOMIC_ACQUIRE, "wavefront");
}
__device__ __forceinline__ u32x4 swap16(u32x2 a, u32x2 b) {
    auto x = __builtin_amdgcn_permlane16_swap(a[0], b[0], false, false);
    auto y = __builtin_amdgcn_permlane16_swap(a[1], b[1], false, false);
    return u32x4{x[0], y[0], x[1], y[1]};
}

constexpr int K1_WP = 1024 + 32;
constexpr int K1_STAGE = 16384 + 32 * K1_WP;
#define SB() __builtin_amdgcn_sched_barrier(0)

template <bool VT>
__device__ __forceinline__ void k1_main(f32x4 (&acc)[4][4], lds_ptr lds, int xa, int wa, const char* tp, int tstep, unsigned& d0) {
#define TOUCH() asm volatile("global_load_dword %0, %1, off" : "+v"(d0) : "v"(tp) : "memory")
    bf16x8 xf[2][4], wf[2][4];
    auto rd = [&](int so, int kk) {
#pragma unroll
        for (int j = 0; j < 4; ++j) {
            lds_ptr p = lds + so + wa + kk * (16 * K1_WP) + j * 32;
            wf[kk][j] = cat8(lds_tr(p), lds_tr(p + 512));
        }
#pragma unroll
        for (int i = 0; i < 4; ++i) xf[kk][i] = lds_b128(lds + so + (xa ^ (kk * 64)) + i * 2048);
    };
    auto mm = [&](int kk) {
#pragma unroll
        for (int i = 0; i < 4; ++i)
#pragma unroll
            for (int j = 0; j < 4; ++j)
                acc[i][j] = VT ? MFMA16(xf[kk][i], wf[kk][j], acc[i][j]) : MFMA16(wf[kk][j], xf[kk][i], acc[i][j]);
    };
#define ILV() do { _Pragma("unroll") for (int q_ = 0; q_ < 12; ++q_) { __builtin_amdgcn_sched_group_barrier(0x100, 1, 0); __builtin_amdgcn_sched_group_barrier(0x8, 1, 0); } __builtin_amdgcn_sched_group_barrier(0x8, 4, 0); SB(); } while (0)
    if (K1_PF && tstep < 0) TOUCH();
    RAW_BAR();
    if (K1_PF && tstep == 0) TOUCH();
    rd(0, 0); SB();
    rd(0, 1);
    mm(0); ILV();
    int so = K1_STAGE;
#pragma unroll 1
    for (int t = 1; t < 10; ++t) {
        RAW_BAR();
        if (K1_PF && tstep == t) TOUCH();
        rd(so, 0);
        mm(1); ILV();
        rd(so, 1);
        mm(0); ILV();
        so = so == 2 * K1_STAGE ? 0 : so + K1_STAGE;
    }
    mm(1);
}

#define SMFMAC(a, b8, c) __builtin_amdgcn_smfmac_f32_32x32x32_bf16(a, __builtin_bit_cast(bf16x16, b8), c, (int)0xDDDDDDDD, 0, 0)
#define LO8(s) __builtin_shufflevector(s, s, 0, 1, 2, 3, 4, 5, 6, 7)
#define HI8(s) __builtin_shufflevector(s, s, 8, 9, 10, 11, 12, 13, 14, 15)
constexpr unsigned ONE2 = 0x3F803F80u;

__device__ __forceinline__ float xhalf_max(float m) {
    auto r = __builtin_amdgcn_permlane32_swap(__float_as_uint(m), __float_as_uint(m), false, false);
    return fmaxf(__uint_as_float(r[0]), __uint_as_float(r[1]));
}
__device__ __forceinline__ float max3f(float a, float b, float c) { return __builtin_fmaxf(__builtin_fmaxf(a, b), c); }
__device__ __forceinline__ float max16(const f32x16& s, float m) {
#pragma unroll
    for (int r = 0; r < 16; r += 2) m = max3f(m, s[r], s[r + 1]);
    return m;
}
__device__ __forceinline__ float row_sum(const f32x16& o) {
    auto r = __builtin_amdgcn_permlane32_swap(__float_as_uint(o[8]), __float_as_uint(o[8]), false, false);
    return __uint_as_float(r[1]);
}
__device__ __forceinline__ void store_o(const f32x16& o, float f, rsrc_t wrs, unsigned voff, int soff, int head, int hh) {
    u32x4 v = {pack2(o[0] * f, o[1] * f), pack2(o[2] * f, o[3] * f), pack2(o[4] * f, o[5] * f), pack2(o[6] * f, o[7] * f)};
    u32x2 t = {pack2(o[8] * f, o[9] * f), pack2(o[10] * f, o[11] * f)};
    const int c0 = (head >> 1) * 5;
    ST16(v, voff + hh * 256, soff + (c0 + (head & 1) * 2) * 256);
    if (!hh) ST8(t, voff, soff + (c0 + 4) * 256 + (head & 1) * 8);
}

#define S2(x) #x
#define STR(x) S2(x)
#define EXP4(C, a, b, c, d) "v_exp_f32 v[" #C "+" #a "], v[" #C "+" #a "]\n\tv_exp_f32 v[" #C "+" #b "], v[" #C "+" #b "]\n\t" \
                            "v_exp_f32 v[" #C "+" #c "], v[" #C "+" #c "]\n\tv_exp_f32 v[" #C "+" #d "], v[" #C "+" #d "]\n\t"
#define STEP(C, P, T)                                                                              \
    "s_waitcnt lgkmcnt(3)\n\t"                                                                     \
    "v_smfmac_f32_32x32x32_bf16 v[80:95], v[36:39], v[" #P "+8:" #P "+15], v40\n\t"                \
    "s_mov_b64 exec, %[vm]\n\t"                                                                    \
    "ds_read_b128 v[36:39], v43 offset:" #T "*2560+1920\n\t"                                       \
    "s_mov_b64 exec, -1\n\t"                                                                       \
    EXP4(C, 0, 1, 2, 3)                                                                            \
    "s_waitcnt lgkmcnt(3)\n\t"                                                                     \
    "v_mfma_f32_32x32x16_bf16 v[" #P ":" #P "+15], v[24:27], v[16:19], 0\n\t"                      \
    "ds_read_b128 v[24:27], v41 offset:(" #T "+2)*2560\n\t"                                        \
    EXP4(C, 4, 5, 6, 7)                                                                            \
    "s_waitcnt lgkmcnt(3)\n\t"                                                                     \
    "v_mfma_f32_32x32x16_bf16 v[" #P ":" #P "+15], v[28:31], v[20:23], v[" #P ":" #P "+15]\n\t"    \
    "ds_read_b64 v[28:29], v42 offset:(" #T "+2)*2560+1024\n\t"                                    \
    EXP4(C, 8, 9, 10, 11)                                                                          \
    "s_waitcnt lgkmcnt(3)\n\t"                                                                     \
    "v_smfmac_f32_32x32x32_bf16 v[80:95], v[32:35], v[" #C ":" #C "+7], v40\n\t"                   \
    EXP4(C, 12, 13, 14, 15)                                                                        \
    "s_mov_b64 exec, %[vm]\n\t"                                                                    \
    "ds_read_b128 v[32:35], v43 offset:(" #T "+1)*2560+1280\n\t"                                   \
    "s_mov_b64 exec, -1\n\t"
#define PRIO(n, L) "s_cmp_ge_u32 %[psw], 4\n\ts_cbranch_scc1 " #L "f\n\ts_setprio " #n "\n\t" #L ":\n\t"
#define PRIO_A
#define PRIO_B PRIO(0, 4)
#define STEP_62(C, P, T)                                                                           \
    "s_waitcnt lgkmcnt(3)\n\t"                                                                     \
    "v_smfmac_f32_32x32x32_bf16 v[80:95], v[36:39], v[" #P "+8:" #P "+15], v40\n\t"                \
    "s_mov_b64 exec, %[vm]\n\t"                                                                    \
    "ds_read_b128 v[36:39], v43 offset:" #T "*2560+1920\n\t"                                       \
    "s_mov_b64 exec, -1\n\t"                                                                       \
    EXP4(C, 0, 1, 2, 3)                                                                            \
    "s_waitcnt lgkmcnt(1)\n\t"                                                                     \
    "v_mfma_f32_32x32x16_bf16 v[" #P ":" #P "+15], v[24:27], v[16:19], 0\n\t"                      \
    EXP4(C, 4, 5, 6, 7)                                                                            \
    "v_mfma_f32_32x32x16_bf16 v[" #P ":" #P "+15], v[28:31], v[20:23], v[" #P ":" #P "+15]\n\t"    \
    EXP4(C, 8, 9, 10, 11)                                                                          \
    "v_smfmac_f32_32x32x32_bf16 v[80:95], v[32:35], v[" #C ":" #C "+7], v40\n\t"                   \
    EXP4(C, 12, 13, 14, 15)                                                                        \
    "s_mov_b64 exec, %[vm]\n\t"                                                                    \
    "ds_read_b128 v[32:35], v43 offset:(" #T "+1)*2560+1280\n\t"                                   \
    "s_mov_b64 exec, -1\n\t"
#define STEP_63(C, P, T)                                                                           \
    "s_waitcnt lgkmcnt(1)\n\t"                                                                     \
    "v_smfmac_f32_32x32x32_bf16 v[80:95], v[36:39], v[" #P "+8:" #P "+15], v40\n\t"                \
    "s_mov_b64 exec, %[vm]\n\t"                                                                    \
    "ds_read_b128 v[36:39], v43 offset:" #T "*2560+1920\n\t"                                       \
    "s_mov_b64 exec, -1\n\t"                                                                       \
    EXP4(C, 0, 1, 2, 3)                                                                            \
    EXP4(C, 4, 5, 6, 7)                                                                            \
    EXP4(C, 8, 9, 10, 11)                                                                          \
    "s_waitcnt lgkmcnt(1)\n\t"                                                                     \
    "v_smfmac_f32_32x32x32_bf16 v[80:95], v[32:35], v[" #C ":" #C "+7], v40\n\t"                   \
    EXP4(C, 12, 13, 14, 15)                                                                        \
    "s_waitcnt lgkmcnt(0)\n\t"                                                                     \
    "s_nop 1\n\t"                                                                                  \
    "v_smfmac_f32_32x32x32_bf16 v[80:95], v[36:39], v[" #C "+8:" #C "+15], v40\n\t"                \
    "s_nop 15\n\t"

__global__ __launch_bounds__(768, 3) void gated_mix_kernel(const u16* __restrict__ x, const u16* __restrict__ wA, const u16* __restrict__ wB,
                                                            const u16* __restrict__ woA, const u16* __restrict__ woB,
                                                            const u16* __restrict__ gate_w, char* __restrict__ ws, u16* __restrict__ out) {
    __shared__ __attribute__((aligned(16))) char smem[KV_HEAD];
    lds_ptr lds = (lds_ptr)smem;
    const int tid = threadIdx.x, lane = tid & 63, wid = __builtin_amdgcn_readfirstlane(tid >> 6);
    const int g = lane >> 4, r16 = lane & 15;
    const rsrc_t wrs = __builtin_amdgcn_make_buffer_rsrc(ws, 0, (int)WS_END, 0x00020000);
    STAMP_DECL STAMP(0, 0);
    u64 tag = 0;
    {
        const int xcd = blockIdx.x & 7, idx = blockIdx.x >> 3;
        const int mt = (xcd & 1) * 8 + (idx & 7), nt = (xcd >> 1) * 4 + (idx >> 3), m0 = mt * 128;
        if (nt == 15) {
            if (wid >= 8) tag = __hip_atomic_load((gu64*)(ws + WS_EPOCH), RLX_AGENT) + 1;
            const int task = mt * 10 + wid, ks = task >> 2, cg = task & 3;
            lds_ptr st = lds + wid * 10240;
            if (wid < 10) {
                const char* wsrc = (const char*)((ks >= 20 ? woB : woA) + cg * 160);
#pragma unroll
                for (int i = 0; i < 10; ++i) {
                    const int ci = i * 64 + lane, slot = ci / 20, cc = ci - slot * 20;
                    const int c = (ks >= 20 ? ks - 20 : ks) * 4 + (slot >> 3), e = slot & 7, p = c / 5, r = c - p * 5;
                    const int k = r < 4 ? (2 * p + (r >> 1)) * 20 + 4 * (r & 1) + (e & 3) + 8 * (e >> 2) : (2 * p + (e >> 2)) * 20 + 16 + (e & 3);
                    glds16(wsrc, k * (DM * 2) + cc * 16, st + i * 1024);
                }
            }
            u32x4 gv[2];
            gv[0] = *(const u32x4*)(gate_w + lane * 8);
            gv[1] = lane < 16 ? *(const u32x4*)(gate_w + 512 + lane * 8) : u32x4{0u, 0u, 0u, 0u};
            float s[11];
#pragma unroll
            for (int u = 0; u < 11; ++u) {
                const int row = wid + 12 * u < 128 ? wid + 12 * u : wid;
                const u16* xr = x + (size_t)(m0 + row) * DM;
                const u32x4 xv[2] = {*(const u32x4*)(xr + lane * 8), *(const u32x4*)(xr + 512 + r16 * 8)};
                float a = 0.f;
#pragma unroll
                for (int c = 0; c < 2; ++c)
#pragma unroll
                    for (int e = 0; e < 4; ++e) {
                        a = fmaf(__uint_as_float(xv[c][e] << 16), __uint_as_float(gv[c][e] << 16), a);
                        a = fmaf(__uint_as_float(xv[c][e] & 0xffff0000u), __uint_as_float(gv[c][e] & 0xffff0000u), a);
                    }
                s[u] = a;
            }
#pragma unroll
            for (int o = 32; o; o >>= 1)
#pragma unroll
                for (int u = 0; u < 11; ++u) s[u] += __shfl_xor(s[u], o);
            float z = s[0];
#pragma unroll
            for (int u = 1; u < 11; ++u) z = lane == u ? s[u] : z;
            if (lane < 11 && wid + 12 * lane < 128) ST4(__float_as_uint(1.f / (1.f + __expf(-z))), (wid + 12 * lane) * 4, (int)WS_G + m0 * 4);
            WAIT_V(0);
            if (wid < 10) {
                lds_ptr rp = st + (8 * g + (r16 >> 2)) * 320 + (lane & 3) * 8;
#pragma unroll
                for (int j = 0; j < 10; ++j)
                    ST16(__builtin_bit_cast(u32x4, cat8(lds_tr(rp + j * 32), lds_tr(rp + j * 32 + 4 * 320))), lane * 16, (int)WS_WO + (ks * 40 + cg * 10 + j) * 1024);
            }
            STAMP(0, 4);
        } else if (wid >= 8) {
            const int lw = wid - 8;
            const char* xg = (const char*)(x + (size_t)m0 * DM);
            const int gc = nt * 256 + (lane & 31) * 8;
            const bool b = gc < 2560 ? gc >= 1280 : gc >= 3200;
            const int col = gc < 2560 ? gc - (b ? 1280 : 0) : gc - (b ? 3200 : 2560) + 1280;
            const char* wl = (const char*)(b ? wB : wA) + col * 2 + (size_t)(lw + 4 * (lane >> 5)) * 3840;
            const unsigned xoff = (lw * 8 + (lane >> 3)) * (DM * 2) + (((lane & 7) ^ ((lane >> 3) & 7)) << 4);
            auto issue = [&](int t, int so) {
                lds_ptr st = lds + so;
#pragma unroll
                for (int i = 0; i < 4; ++i) glds16(xg + t * 128 + i * (32 * DM * 2), xoff, st + (lw + 4 * i) * 1024);
#pragma unroll
                for (int i = 0; i < 8; ++i) glds16v(wl + (size_t)(t * 64 + i * 8) * 3840, st + 16384 + (lw + 4 * i) * K1_WP);
            };
            int so = 0;
#pragma unroll 1
            for (int t = -2; t < 9; ++t) {
                if (t >= 0) {
                    WAIT_V(12);
                    if (t == 0) STAMP(0, 1);
                    RAW_BAR();
                }
                if (t < 8) issue(t + 2, so);
                so = so == 2 * K1_STAGE ? 0 : so + K1_STAGE;
            }
            WAIT_V(0);
            STAMP(0, 2);
            RAW_BAR();
            tag = __hip_atomic_load((gu64*)(ws + WS_EPOCH), RLX_AGENT) + 1;
        } else {
            const char* tp;
            if (lane < 16) {
                const int gc = nt * 256 + (lane & 3) * 64;
                const bool b = gc < 2560 ? gc >= 1280 : gc >= 3200;
                const int col = gc < 2560 ? gc - (b ? 1280 : 0) : gc - (b ? 3200 : 2560) + 1280;
                tp = (const char*)(b ? wB : wA) + (size_t)((wid + 2) * 64 + mt * 4 + (lane >> 2)) * 3840 + col * 2;
            } else {
                const int r = nt * 9 + (lane < 25 ? lane - 16 : 0);
                tp = (const char*)(x + (size_t)(m0 + (r < 127 ? r : 127)) * DM) + (wid + 2) * 128;
            }
            unsigned d0 = 0;
            const int wm = wid >> 2, wn = wid & 3;
            const int xa = (wm * 64 + r16) * 128 + ((g ^ (lane & 7)) << 4);
            const int wa = 16384 + (4 * g + (r16 >> 2)) * K1_WP + (wn * 64 + 4 * (lane & 3)) * 2;
            f32x4 acc[4][4];
#pragma unroll
            for (int i = 0; i < 4; ++i)
#pragma unroll
                for (int j = 0; j < 4; ++j) acc[i][j] = f32x4{0.f, 0.f, 0.f, 0.f};
            const int tile0 = mt * 4 + wm * 2;
            if (nt < 10) {
                k1_main<false>(acc, lds, xa, wa, tp, wid + 2 - K1_PF, d0);
                STAMP(0, 1);
                PF_DONE();
                const int br = nt >= 5;
                const int cw = (nt - 5 * br) * 256 + wn * 64;
                const bool isq = cw < 640;
                const int tstride = __builtin_amdgcn_readfirstlane(isq ? (int)Q_TILE : (int)KV_TILE);
                const int sbase = __builtin_amdgcn_readfirstlane((int)(isq ? WS_Q : WS_KV) + (br * NH * 64 + tile0) * tstride);
                const float sc = isq ? QSCALE : 1.f;
                const int tk = (g & 1) * 16 + r16;
#pragma unroll
                for (int j = 0; j < 4; ++j) {
                    const int cp = (cw - (isq ? 0 : 640)) / 8 + j * 2 + (g >> 1), p = cp / 5, r = cp - p * 5;
                    const unsigned hoff = 2 * p * 64 * tstride;
                    const unsigned off = r == 2 ? hoff + 1024 + tk * 8 : hoff + (r > 2 ? 64 * tstride + (r - 3) * 512 : r * 512) + tk * 16;
#pragma unroll
                    for (int i = 0; i < 2; ++i) {
                        const f32x4 a = acc[2 * i][j] * sc, b = acc[2 * i + 1][j] * sc;
                        const u32x4 v = swap16(u32x2{pack2(a[0], a[1]), pack2(a[2], a[3])}, u32x2{pack2(b[0], b[1]), pack2(b[2], b[3])});
                        if (r != 2) {
                            ST16(v, off, sbase + i * tstride);
                        } else {
                            ST8((u32x2{v[0], v[1]}), off, sbase + i * tstride);
                            ST8((u32x2{v[2], v[3]}), off, sbase + (i + 64) * tstride);
                        }
                    }
                }
            } else {
                k1_main<true>(acc, lds, xa, wa, tp, wid + 2 - K1_PF, d0);
                STAMP(0, 1);
                PF_DONE();
                const int sbase = __builtin_amdgcn_readfirstlane((int)WS_KV + tile0 * (int)KV_TILE + 1280);
#pragma unroll
                for (int j = 0; j < 4; ++j) {
                    const int c = (nt - 10) * 256 + wn * 64 + j * 16 + r16, head = c / 20, d = c - head * 20;
                    const unsigned off = head * (int)KV_HEAD + (g & 1) * 640 + (g >> 1) * 320 + d * 16;
#pragma unroll
                    for (int i = 0; i < 2; ++i) {
                        const f32x4 a = acc[2 * i][j], b = acc[2 * i + 1][j];
                        ST16(swap16(u32x2{pack2(a[0], a[1]), pack2(a[2], a[3])}, u32x2{pack2(b[0], b[1]), pack2(b[2], b[3])}), off, sbase + i * (int)KV_TILE);
                    }
                }
            }
            STAMP(0, 2);
        }
        asm volatile("s_waitcnt vmcnt(0)" : "+v"(tag) :: "memory");
        STAMP(0, 3);
        RAW_BAR();
        if (wid == 8 && lane == 0) __hip_atomic_store((gu64*)(ws + WS_F1) + nt * 16 + mt, tag, RLX_AGENT);
        FLUSH(0);
    }
    const int head = (blockIdx.x & 7) * 4 + ((blockIdx.x >> 3) & 3), qb = blockIdx.x >> 5;
    const int omt = head, ont = qb;
    const int r32 = lane & 31, hh = lane >> 5;
    const u32x4 vfill = r32 == 20 ? u32x4{ONE2, ONE2, ONE2, ONE2} : u32x4{0u, 0u, 0u, 0u};
    const int voff = hh * 320 + (r32 < 20 ? r32 : 0) * 16;
    const unsigned olane = (r32 >> 4) * (int)O_BLK + (r32 & 15) * 16;
    const int wimg = (int)WS_WO + ont * 5120;
    gu64* const f1 = (gu64*)(ws + WS_F1);
    STAMP(1, 0);
    if (wid >= 8) {
        const int hw = wid - 8;
        __builtin_amdgcn_s_setprio(3);
        if (hw == 0) {
            const int c = head * 20 + (g & 1) * 19;
            gu64* const w[2] = {f1 + ((g < 2 ? 5 : 10) + ((640 + c) >> 8)) * 16 + r16, f1 + (5 + (c >> 8)) * 16 + qb * 2 + (lane & 1)};
            wait_flags(w, tag);
        }
        RAW_BAR();
        STAMP(1, 1);
        const char* src = ws + WS_KV + (size_t)(NH + head) * KV_HEAD + hw * 1024;
#define LOADS(a, b) _Pragma("unroll 1") for (int i = a; i < b; ++i) glds16_dev(src + i * 4096, lane * 16, lds + hw * 1024 + i * 4096)
        LOADS(0, 12);
        WAIT_V(5); RAW_BAR();
        LOADS(12, 40);
        f32x16 c0, c1;
        {
            const unsigned long long msk = ~(0x15554ull << r32) >> (4 * hh);
            const unsigned mlo = (unsigned)msk, mhi = (unsigned)(msk >> 32);
#pragma unroll
            for (int r = 0; r < 16; ++r) {
                const int u = (r & 3) + 8 * (r >> 2);
                c0[r] = r < 8 ? 0.f : __uint_as_float(__builtin_amdgcn_sbfe(mlo, (u - 16) & 31, 1) & 0xff800000u);
                c1[r] = __uint_as_float(__builtin_amdgcn_sbfe(u < 16 ? mlo : mhi, (u + 16) & 31, 1) & 0xff800000u);
            }
        }
        WAIT_V(28); RAW_BAR();
        WAIT_V(0); RAW_BAR();
        {
            gu64* const w[4] = {f1 + lane, f1 + 64 + lane, f1 + 128 + lane, f1 + 192 + lane};
            wait_flags(w, tag);
            if (blockIdx.x == 0 && hw == 0 && lane == 0) __hip_atomic_store((gu64*)(ws + WS_EPOCH), tag, RLX_AGENT);
        }
        STAMP(1, 2);
#pragma unroll 1
        for (int it = 0; it < 2; ++it) {
            const int qt = qb * 8 + hw * 2 + 1 - it;
            const int qA = (int)WS_Q + (head * 64 + qt) * (int)Q_TILE;
            const int k1 = (int)WS_KV + (head * 64 + qt) * (int)KV_TILE, k0 = qt ? k1 - (int)KV_TILE : k1;
            const u32x4 aqa = LD16(lane * 16, qA);
            u32x2 aqb = {0u, 0u};
            if (!hh) aqb = LD8(r32 * 8, qA + 1024);
            const u32x4 ka0 = LD16(lane * 16, k0), ka1 = LD16(lane * 16, k1);
            const u32x2 kb0 = LD8(r32 * 8, k0 + 1024), kb1 = LD8(r32 * 8, k1 + 1024);
            u32x4 v0h = vfill, v1l = vfill, v1h = vfill;
            if (r32 < 20) { v0h = LD16(voff, k0 + 1920); v1l = LD16(voff, k1 + 1280); v1h = LD16(voff, k1 + 1920); }
            const float gv = __uint_as_float(LD4(r32 * 4, (int)WS_G + qt * 128));
            if (qt == 0) {
#pragma unroll
                for (int r = 8; r < 16; ++r) c0[r] = -INFINITY;
            }
            const bf16x8 fqa = BF8(aqa), fqb = BF8((u32x4{aqb[0], aqb[1], 0u, 0u}));
            f32x16 s0 = MFMA32(BF8(ka0), fqa, c0);
            f32x16 s1 = MFMA32(BF8(ka1), fqa, c1);
            s0 = MFMA32(BF8((u32x4{kb0[0], kb0[1], 0u, 0u})), fqb, s0);
            s1 = MFMA32(BF8((u32x4{kb1[0], kb1[1], 0u, 0u})), fqb, s1);
            float m = max3f(s0[8], s0[9], s0[10]);
            m = max3f(m, s0[11], s0[12]); m = max3f(m, s0[13], s0[14]);
            m = xhalf_max(max16(s1, fmaxf(m, s0[15])));
#pragma unroll
            for (int r = 8; r < 16; ++r) s0[r] = __builtin_amdgcn_exp2f(s0[r] - m);
#pragma unroll
            for (int r = 0; r < 16; ++r) s1[r] = __builtin_amdgcn_exp2f(s1[r] - m);
            f32x16 o = SMFMAC(BF8(v0h), HI8(s0), f32x16{});
            o = SMFMAC(BF8(v1l), LO8(s1), o);
            o = SMFMAC(BF8(v1h), HI8(s1), o);
            store_o(o, gv * __builtin_amdgcn_rcpf(row_sum(o)), wrs, olane, (int)WS_O + qt * 2 * (int)O_BLK, head, hh);
        }
        WAIT_V(0);
        STAMP(1, 3);
        RAW_BAR();
        if (hw == 0 && lane == 0) __hip_atomic_store((gu64*)(ws + WS_FA) + qb * 32 + head, tag, RLX_AGENT);
        {
            gu64* const w[1] = {(gu64*)(ws + WS_FA) + (omt >> 2) * 32 + r32};
            wait_flags(w, tag);
        }
        f32x4 acc[4][5];
#pragma unroll
        for (int i = 0; i < 4; ++i)
#pragma unroll
            for (int j = 0; j < 5; ++j) acc[i][j] = f32x4{0.f, 0.f, 0.f, 0.f};
#pragma unroll 1
        for (int ks = hw * 5; ks < hw * 5 + 5; ++ks) {
            bf16x8 of[4], wf[5];
#pragma unroll
            for (int i = 0; i < 4; ++i) of[i] = BF8(LD16(lane * 16, (int)WS_O + (omt * 4 + i) * (int)O_BLK + ks * 1024));
#pragma unroll
            for (int j = 0; j < 5; ++j) wf[j] = BF8(LD16(lane * 16, wimg + ks * 40960 + j * 1024));
#pragma unroll
            for (int i = 0; i < 4; ++i)
#pragma unroll
                for (int j = 0; j < 5; ++j) acc[i][j] = MFMA16(wf[j], of[i], acc[i][j]);
        }
        STAMP(1, 4);
        RAW_BAR();
        if (hw == 0 && lane == 0) __hip_atomic_store((gu64*)(ws + WS_FB) + qb * 32 + head, tag, RLX_AGENT);
        STAMP(1, 5);
#pragma unroll
        for (int i = 0; i < 4; ++i)
#pragma unroll
            for (int j = 0; j < 5; ++j)
                *(LDS_AS f32x4*)(lds + hw * 20480 + (i * 5 + j) * 1024 + g * 256 + ((r16 + 2 * j + (g >> 1)) & 15) * 16) = acc[i][j];
        if (hw == 0) {
            gu64* const w[1] = {(gu64*)(ws + WS_FB) + (omt >> 2) * 32 + r32};
            wait_flags(w, tag);
        }
        STAMP(1, 6);
        RAW_BAR();
    } else {
        const int qt = qb * 8 + wid;
        RAW_BAR();
        const int qB = (int)WS_Q + ((NH + head) * 64 + qt) * (int)Q_TILE;
        const u32x4 qa = LD16(lane * 16, qB);
        u32x2 qb2 = {0u, 0u};
        if (!hh) qb2 = LD8(r32 * 8, qB + 1024);
        const bf16x8 fqa = BF8(qa), fqb0 = BF8((u32x4{qb2[0], qb2[1], 0u, 0u}));
        lds_ptr ka_p = lds + lane * 16, kb_p = lds + 1024 + r32 * 8;
        auto tile_max = [&](int t, float m) {
            const u32x2 kb = *(const LDS_AS u32x2*)(kb_p + t * KV_TILE);
            f32x16 s = MFMA32(lds_b128(ka_p + t * KV_TILE), fqa, f32x16{});
            s = MFMA32(BF8((u32x4{kb[0], kb[1], 0u, 0u})), fqb0, s);
            return max16(s, m);
        };
        RAW_BAR();
        STAMP(1, 1);
        float m = xhalf_max(tile_max(0, -INFINITY));
        const unsigned long long vmask = 0x000fffff000fffffull;
        int attempt = 0;
        asm volatile("" : "+s"(attempt));
#pragma unroll 1
        for (;; ++attempt) {
        const unsigned h1 = __float_as_uint(-m) & 0xffff0000u;
        const float r1 = -m - __uint_as_float(h1);
        const unsigned h2 = __float_as_uint(r1) & 0xffff0000u;
        const unsigned h3 = __float_as_uint(r1 - __uint_as_float(h2)) >> 16;
        u32x4 qbs = {qb2[0], qb2[1], hh ? 0u : (h1 | (h2 >> 16)), hh ? 0u : h3};
        f32x16 o = {}, s1 = {};
        u32x4 vlo = vfill, vhi = vfill;
        unsigned a_ka = (unsigned)(size_t)ka_p, a_kb = (unsigned)(size_t)(lds + r32 * 8), a_v = (unsigned)(size_t)(lds + voff);
        int nbar = attempt ? 0 : 0xB, it = 7;
        const int psw = PRIO_SW ? wid : 4;
        __builtin_amdgcn_s_setprio(1);
        asm volatile(
            "v_mov_b32 v30, %[one]\n\tv_mov_b32 v31, %[one]\n\tv_mov_b32 v40, %[sidx]\n\t"
            "ds_read_b128 v[24:27], v41\n\t"
            "ds_read_b64 v[28:29], v42 offset:1024\n\t"
            "s_waitcnt lgkmcnt(0)\n\t"
            "v_mfma_f32_32x32x16_bf16 v[48:63], v[24:27], v[16:19], 0\n\t"
            "v_mfma_f32_32x32x16_bf16 v[48:63], v[28:31], v[20:23], v[48:63]\n\t"
            "ds_read_b128 v[24:27], v41 offset:2560\n\t"
            "ds_read_b64 v[28:29], v42 offset:2560+1024\n\t"
            "s_mov_b64 exec, %[vm]\n\t"
            "ds_read_b128 v[32:35], v43 offset:1280\n\t"
            "s_mov_b64 exec, -1\n\t"
            "s_nop 7\n\t"
            "1:\n\t"
            PRIO(2, 3) STEP(48, 64, 0) STEP(64, 48, 1) STEP(48, 64, 2) STEP(64, 48, 3) PRIO_A
            STEP(48, 64, 4) PRIO_B STEP(64, 48, 5) STEP(48, 64, 6) STEP(64, 48, 7)
            "v_add_u32 v41, 20480, v41\n\tv_add_u32 v42, 20480, v42\n\tv_add_u32 v43, 20480, v43\n\t"
            "s_bitcmp0_b32 %[nbar], 0\n\t"
            "s_cbranch_scc1 2f\n\t"
            "s_barrier\n\t"
            "2:\n\t"
            "s_lshr_b32 %[nbar], %[nbar], 1\n\t"
            "s_sub_i32 %[it], %[it], 1\n\t"
            "s_cmp_lg_u32 %[it], 0\n\t"
            "s_cbranch_scc1 1b\n\t"
            PRIO(2, 3) STEP(48, 64, 0) STEP(64, 48, 1) STEP(48, 64, 2) STEP(64, 48, 3) PRIO_A
            STEP(48, 64, 4) PRIO_B STEP(64, 48, 5) STEP_62(48, 64, 6) STEP_63(64, 48, 7)
            : "+{v[80:95]}"(o), "+{v[64:79]}"(s1), "+{v[32:35]}"(vlo), "+{v[36:39]}"(vhi), "+{v41}"(a_ka), "+{v42}"(a_kb), "+{v43}"(a_v),
              [nbar] "+s"(nbar), [it] "+s"(it)
            : "{v[16:19]}"(qa), "{v[20:23]}"(qbs), [vm] "s"(vmask), [one] "s"(ONE2), [sidx] "s"(0xDDDDDDDDu), [psw] "s"(psw)
            : "v24", "v25", "v26", "v27", "v28", "v29", "v30", "v31", "v40", "v48", "v49", "v50", "v51", "v52", "v53", "v54", "v55",
              "v56", "v57", "v58", "v59", "v60", "v61", "v62", "v63", "scc", "memory");
        STAMP(1, 2);
        const float gB = 1.f - __uint_as_float(LD4(r32 * 4, (int)WS_G + qt * 128));
        const float l = row_sum(o);
        float amax = hh ? 0.f : fmaxf(fmaxf(fabsf(o[8]), fabsf(o[9])), fmaxf(fabsf(o[10]), fabsf(o[11])));
#pragma unroll
        for (int i = 0; i < 8; ++i) amax = fmaxf(amax, fabsf(o[i]));
        if (attempt || __all(l >= 0x1p-64f && l <= 0x1p64f && amax <= 0x1p126f)) {
            store_o(o, gB * __builtin_amdgcn_rcpf(l), wrs, olane, (int)WS_O + (128 + qt * 2) * (int)O_BLK, head, hh);
            break;
        }
        m = -INFINITY;
#pragma unroll 1
        for (int t = 0; t < 64; ++t) m = tile_max(t, m);
        m = xhalf_max(m);
        }
        WAIT_V(0);
        STAMP(1, 3);
        RAW_BAR();
        const int wm = wid & 1, kq = wid >> 1;
        const int wsrc = wimg + (20 + kq * 5) * 40960;
        bf16x8 wf[5][5], of[5][2];
#pragma unroll
        for (int s = 0; s < 3; ++s)
#pragma unroll
            for (int j = 0; j < 5; ++j) wf[s][j] = BF8(LD16(lane * 16, wsrc + s * 40960 + j * 1024));
        SB();
        RAW_BAR();
        STAMP(1, 4);
#pragma unroll
        for (int s = 0; s < 5; ++s)
#pragma unroll
            for (int i = 0; i < 2; ++i) of[s][i] = BF8(LD16(lane * 16, (int)WS_O + (128 + omt * 4 + wm * 2 + i) * (int)O_BLK + (kq * 5 + s) * 1024));
        f32x4 acc[2][5];
        lds_ptr pp = lds + kq * 20480 + wm * 10240 + g * 256;
#pragma unroll
        for (int i = 0; i < 2; ++i)
#pragma unroll
            for (int j = 0; j < 5; ++j) acc[i][j] = *(const LDS_AS f32x4*)(pp + (i * 5 + j) * 1024 + ((r16 + 2 * j + (g >> 1)) & 15) * 16);
        SB();
#pragma unroll
        for (int s = 0; s < 5; ++s) {
#pragma unroll
            for (int i = 0; i < 2; ++i)
#pragma unroll
                for (int j = 0; j < 5; ++j) acc[i][j] = MFMA16(wf[s][j], of[s][i], acc[i][j]);
            if (s < 2) {
#pragma unroll
                for (int j = 0; j < 5; ++j) wf[s + 3][j] = BF8(LD16(lane * 16, wsrc + (s + 3) * 40960 + j * 1024));
            }
            SB();
        }
#pragma unroll
        for (int i = 0; i < 2; ++i)
#pragma unroll
            for (int j = 0; j < 5; ++j) *(LDS_AS f32x4*)(pp + (i * 5 + j) * 1024 + ((r16 + 2 * j + (g >> 1)) & 15) * 16) = acc[i][j];
        STAMP(1, 5);
    }
    RAW_BAR();
    if (tid < 640) {
        const int row = tid / 10, c = tid - row * 10;
        lds_ptr q = lds + ((row >> 4) * 5 + (c >> 1)) * 1024 + (c & 1) * 512 + (((row & 15) + c) & 15) * 16;
        f32x4 a = {0.f, 0.f, 0.f, 0.f}, b = a;
#pragma unroll
        for (int p = 0; p < 4; ++p) {
            a += *(const LDS_AS f32x4*)(q + p * 20480);
            b += *(const LDS_AS f32x4*)(q + p * 20480 + 256);
        }
        u32x4 v = {pack2(a[0], a[1]), pack2(a[2], a[3]), pack2(b[0], b[1]), pack2(b[2], b[3])};
        *(u32x4*)(out + (size_t)(omt * 64 + row) * DM + ont * 80 + c * 8) = v;
    }
    DIAG_WAIT();
    STAMP(1, 7); FLUSH(1);
}

extern "C" void kernel_launch(void* const* d_in, const int* in_sizes, int n_in, void* d_out, int out_size, void* d_ws, size_t ws_size,
                              hipStream_t stream) {
    gated_mix_kernel<<<256, 768, 0, stream>>>((const u16*)d_in[0], (const u16*)d_in[1], (const u16*)d_in[4], (const u16*)d_in[2],
                                              (const u16*)d_in[5], (const u16*)d_in[6], (char*)d_ws, (u16*)d_out);
}
```

```cpp
#include <hip/hip_runtime.h>
#include <hip/hip_bf16.h>

typedef __attribute__((ext_vector_type(8))) __bf16 bf16x8;
typedef __attribute__((ext_vector_type(16))) __bf16 bf16x16;
typedef __attribute__((ext_vector_type(2))) __bf16 bf16x2;
typedef __attribute__((ext_vector_type(4))) short s16x4;
typedef __attribute__((ext_vector_type(8))) short s16x8;
typedef __attribute__((ext_vector_type(2))) float f32x2;
typedef __attribute__((ext_vector_type(4))) float f32x4;
typedef __attribute__((ext_vector_type(8))) float f32x8;
typedef __attribute__((ext_vector_type(16))) float f32x16;
typedef __attribute__((ext_vector_type(2))) unsigned u32x2;
typedef __attribute__((ext_vector_type(4))) unsigned u32x4;
typedef unsigned short u16;
#define LDS_AS __attribute__((address_space(3)))
typedef LDS_AS char* lds_ptr;

constexpr int SEQ = 2048, DM = 640, NH = 32, DH = 20;
constexpr size_t KV_TILE = 2560, KV_HEAD = 64 * KV_TILE;
constexpr size_t Q_TILE = 1280, Q_HEAD = 64 * Q_TILE;
constexpr size_t WS_KV = 0;
constexpr size_t WS_Q = WS_KV + 2 * NH * KV_HEAD;
constexpr size_t O_BLK = 80 * 256;
constexpr size_t WS_O = WS_Q + 2 * NH * Q_HEAD;
constexpr size_t WS_WO = WS_O + 2 * 128 * O_BLK;
constexpr size_t WS_G = WS_WO + 40 * 40 * 1024;
constexpr size_t WS_F1 = WS_G + SEQ * 4;
constexpr size_t WS_FA = WS_F1 + 512 * 8;
constexpr size_t WS_EPOCH = WS_FA + 256 * 8;
constexpr size_t WS_END = WS_EPOCH + 128;
#define STAMP_DECL
#define STAMP(k, i)
#define FLUSH(k)
#define DIAG_WAIT()
constexpr float QSCALE = 0.22360679774997896f * 1.4426950408889634f;

#define K1_PF 3
#define PF_DONE() asm volatile("s_waitcnt vmcnt(0)" : "+v"(d0) :: "memory")
#define PRIO_SW 5
#define ABL 0
#define THIN 1
#define FORCE_FALLBACK 0
#define WAIT_V(n) asm volatile("s_waitcnt vmcnt(" #n ")" ::: "memory")
#define RAW_BAR() asm volatile("s_waitcnt lgkmcnt(0)\n\ts_barrier" ::: "memory")
#define MFMA16(a, b, c) __builtin_amdgcn_mfma_f32_16x16x32_bf16(a, b, c, 0, 0, 0)
#define MFMA32(a, b, c) __builtin_amdgcn_mfma_f32_32x32x16_bf16(a, b, c, 0, 0, 0)

__device__ __forceinline__ unsigned pack2(float lo, float hi) {
    f32x2 v = {lo, hi};
    return __builtin_bit_cast(unsigned, __builtin_convertvector(v, bf16x2));
}
__device__ __forceinline__ void glds16(const char* sbase, unsigned voff, lds_ptr l) {
    unsigned keep;
    asm volatile("s_mov_b32 %0, m0\n\ts_mov_b32 m0, %3\n\ts_nop 0\n\tglobal_load_lds_dwordx4 %1, %2\n\ts_mov_b32 m0, %0"
                 : "=&s"(keep) : "v"(voff), "s"(sbase), "s"((unsigned)(size_t)l) : "memory");
}
__device__ __forceinline__ void glds16v(const char* g, lds_ptr l) {
    unsigned keep;
    asm volatile("s_mov_b32 %0, m0\n\ts_mov_b32 m0, %2\n\ts_nop 0\n\tglobal_load_lds_dwordx4 %1, off\n\ts_mov_b32 m0, %0"
                 : "=&s"(keep) : "v"(g), "s"((unsigned)(size_t)l) : "memory");
}
__device__ __forceinline__ bf16x8 lds_b128(lds_ptr p) { return *(const LDS_AS bf16x8*)p; }
__device__ __forceinline__ s16x4 lds_tr(lds_ptr p) { return __builtin_amdgcn_ds_read_tr16_b64_v4i16((LDS_AS s16x4*)p); }
__device__ __forceinline__ bf16x8 cat8(s16x4 a, s16x4 b) {
    return __builtin_bit_cast(bf16x8, __builtin_shufflevector(a, b, 0, 1, 2, 3, 4, 5, 6, 7));
}
typedef __amdgpu_buffer_rsrc_t rsrc_t;
typedef unsigned long long u64;
typedef __attribute__((address_space(1))) u64 gu64;
#define RLX_AGENT __ATOMIC_RELAXED, __HIP_MEMORY_SCOPE_AGENT
constexpr int SC1 = 16;
#define LD16(voff, soff) __builtin_amdgcn_raw_buffer_load_b128(wrs, voff, soff, SC1)
#define LD8(voff, soff) __builtin_amdgcn_raw_buffer_load_b64(wrs, voff, soff, SC1)
#define LD4(voff, soff) __builtin_amdgcn_raw_buffer_load_b32(wrs, voff, soff, SC1)
#define ST16(v, voff, soff) __builtin_amdgcn_raw_buffer_store_b128(v, wrs, (voff) + (soff), 0, SC1)
#define ST8(v, voff, soff) __builtin_amdgcn_raw_buffer_store_b64(v, wrs, voff, soff, SC1)
#define ST4(v, voff, soff) __builtin_amdgcn_raw_buffer_store_b32(v, wrs, voff, soff, SC1)
#define BF8(x) __builtin_bit_cast(bf16x8, x)
__device__ __forceinline__ void glds16_dev(const char* sbase, unsigned voff, lds_ptr l) {
    unsigned keep;
    asm volatile("s_mov_b32 %0, m0\n\ts_mov_b32 m0, %3\n\ts_nop 0\n\tglobal_load_lds_dwordx4 %1, %2 sc1\n\ts_mov_b32 m0, %0"
                 : "=&s"(keep) : "v"(voff), "s"(sbase), "s"((unsigned)(size_t)l) : "memory");
}
template <int N>
__device__ __forceinline__ void wait_flags(gu64* const (&w)[N], u64 tag) {
#pragma unroll 1
    for (unsigned spins = 0; spins < (1u << 20); ++spins) {
        bool ok = true;
#pragma unroll
        for (int i = 0; i < N; ++i) ok &= __hip_atomic_load(w[i], RLX_AGENT) == tag;
        if (__all(ok)) break;
        __builtin_amdgcn_s_sleep(2);
    }
    __builtin_amdgcn_fence(__ATOMIC_ACQUIRE, "wavefront");
}
__device__ __forceinline__ u32x4 swap16(u32x2 a, u32x2 b) {
    auto x = __builtin_amdgcn_permlane16_swap(a[0], b[0], false, false);
    auto y = __builtin_amdgcn_permlane16_swap(a[1], b[1], false, false);
    return u32x4{x[0], y[0], x[1], y[1]};
}

constexpr int K1_WP = 1024 + 32;
constexpr int K1_STAGE = 16384 + 32 * K1_WP;
#define SB() __builtin_amdgcn_sched_barrier(0)

template <bool VT>
__device__ __forceinline__ void k1_main(f32x4 (&acc)[4][4], lds_ptr lds, int xa, int wa, const char* tp, int tstep, unsigned& d0) {
#define TOUCH() asm volatile("global_load_dword %0, %1, off" : "+v"(d0) : "v"(tp) : "memory")
    bf16x8 xf[2][4], wf[2][4];
    auto rd = [&](int so, int kk) {
#pragma unroll
        for (int j = 0; j < 4; ++j) {
            lds_ptr p = lds + so + wa + kk * (16 * K1_WP) + j * 32;
            wf[kk][j] = cat8(lds_tr(p), lds_tr(p + 512));
        }
#pragma unroll
        for (int i = 0; i < 4; ++i) xf[kk][i] = lds_b128(lds + so + (xa ^ (kk * 64)) + i * 2048);
    };
    auto mm = [&](int kk) {
#pragma unroll
        for (int i = 0; i < 4; ++i)
#pragma unroll
            for (int j = 0; j < 4; ++j)
                acc[i][j] = VT ? MFMA16(xf[kk][i], wf[kk][j], acc[i][j]) : MFMA16(wf[kk][j], xf[kk][i], acc[i][j]);
    };
#define ILV() do { _Pragma("unroll") for (int q_ = 0; q_ < 12; ++q_) { __builtin_amdgcn_sched_group_barrier(0x100, 1, 0); __builtin_amdgcn_sched_group_barrier(0x8, 1, 0); } __builtin_amdgcn_sched_group_barrier(0x8, 4, 0); SB(); } while (0)
    if (K1_PF && tstep < 0) TOUCH();
    RAW_BAR();
    if (K1_PF && tstep == 0) TOUCH();
    rd(0, 0); SB();
    rd(0, 1);
    mm(0); ILV();
    int so = K1_STAGE;
#pragma unroll 1
    for (int t = 1; t < 10; ++t) {
        RAW_BAR();
        if (K1_PF && tstep == t) TOUCH();
        rd(so, 0);
        mm(1); ILV();
        rd(so, 1);
        mm(0); ILV();
        so = so == 2 * K1_STAGE ? 0 : so + K1_STAGE;
    }
    mm(1);
}

#define SMFMAC(a, b8, c) __builtin_amdgcn_smfmac_f32_32x32x32_bf16(a, __builtin_bit_cast(bf16x16, b8), c, (int)0xDDDDDDDD, 0, 0)
#define LO8(s) __builtin_shufflevector(s, s, 0, 1, 2, 3, 4, 5, 6, 7)
#define HI8(s) __builtin_shufflevector(s, s, 8, 9, 10, 11, 12, 13, 14, 15)
constexpr unsigned ONE2 = 0x3F803F80u;

__device__ __forceinline__ float xhalf_max(float m) {
    auto r = __builtin_amdgcn_permlane32_swap(__float_as_uint(m), __float_as_uint(m), false, false);
    return fmaxf(__uint_as_float(r[0]), __uint_as_float(r[1]));
}
__device__ __forceinline__ float max3f(float a, float b, float c) { return __builtin_fmaxf(__builtin_fmaxf(a, b), c); }
__device__ __forceinline__ float max16(const f32x16& s, float m) {
#pragma unroll
    for (int r = 0; r < 16; r += 2) m = max3f(m, s[r], s[r + 1]);
    return m;
}
__device__ __forceinline__ float row_sum(const f32x16& o) {
    auto r = __builtin_amdgcn_permlane32_swap(__float_as_uint(o[8]), __float_as_uint(o[8]), false, false);
    return __uint_as_float(r[1]);
}
__device__ __forceinline__ u32x4 with_ref(u32x2 q, float m, int hh) {
    const unsigned h1 = __float_as_uint(-m) & 0xffff0000u;
    const float r1 = -m - __uint_as_float(h1);
    const unsigned h2 = __float_as_uint(r1) & 0xffff0000u;
    const unsigned h3 = __float_as_uint(r1 - __uint_as_float(h2)) >> 16;
    return u32x4{q[0], q[1], hh ? 0u : (h1 | (h2 >> 16)), hh ? 0u : h3};
}
__device__ __forceinline__ void put_o(u32x4 v, u32x2 t, rsrc_t wrs, unsigned voff, int soff, int head, int hh) {
    const int c0 = (head >> 1) * 5;
    ST16(v, voff + hh * 256, soff + (c0 + (head & 1) * 2) * 256);
    if (!hh) ST8(t, voff, soff + (c0 + 4) * 256 + (head & 1) * 8);
}
__device__ __forceinline__ unsigned pack_o(float a, float b) { return min(pack2(a, b), 0xFFFFFFFEu); }
__device__ __forceinline__ void store_o(const f32x16& o, float f, rsrc_t wrs, unsigned voff, int soff, int head, int hh) {
    put_o(u32x4{pack_o(o[0] * f, o[1] * f), pack_o(o[2] * f, o[3] * f), pack_o(o[4] * f, o[5] * f), pack_o(o[6] * f, o[7] * f)},
          u32x2{pack_o(o[8] * f, o[9] * f), pack_o(o[10] * f, o[11] * f)}, wrs, voff, soff, head, hh);
}

#define S2(x) #x
#define STR(x) S2(x)
#define EXP4(C, a, b, c, d) "v_exp_f32 v[" #C "+" #a "], v[" #C "+" #a "]\n\tv_exp_f32 v[" #C "+" #b "], v[" #C "+" #b "]\n\t" \
                            "v_exp_f32 v[" #C "+" #c "], v[" #C "+" #c "]\n\tv_exp_f32 v[" #C "+" #d "], v[" #C "+" #d "]\n\t"
#define STEP(C, P, T)                                                                              \
    "s_waitcnt lgkmcnt(3)\n\t"                                                                     \
    "v_smfmac_f32_32x32x32_bf16 v[80:95], v[36:39], v[" #P "+8:" #P "+15], v40\n\t"                \
    "s_mov_b64 exec, %[vm]\n\t"                                                                    \
    "ds_read_b128 v[36:39], v43 offset:" #T "*2560+1920\n\t"                                       \
    "s_mov_b64 exec, -1\n\t"                                                                       \
    EXP4(C, 0, 1, 2, 3)                                                                            \
    "s_waitcnt lgkmcnt(3)\n\t"                                                                     \
    "v_mfma_f32_32x32x16_bf16 v[" #P ":" #P "+15], v[24:27], v[16:19], 0\n\t"                      \
    "ds_read_b128 v[24:27], v41 offset:(" #T "+2)*2560\n\t"                                        \
    EXP4(C, 4, 5, 6, 7)                                                                            \
    "s_waitcnt lgkmcnt(3)\n\t"                                                                     \
    "v_mfma_f32_32x32x16_bf16 v[" #P ":" #P "+15], v[28:31], v[20:23], v[" #P ":" #P "+15]\n\t"    \
    "ds_read_b64 v[28:29], v42 offset:(" #T "+2)*2560+1024\n\t"                                    \
    EXP4(C, 8, 9, 10, 11)                                                                          \
    "s_waitcnt lgkmcnt(3)\n\t"                                                                     \
    "v_smfmac_f32_32x32x32_bf16 v[80:95], v[32:35], v[" #C ":" #C "+7], v40\n\t"                   \
    EXP4(C, 12, 13, 14, 15)                                                                        \
    "s_mov_b64 exec, %[vm]\n\t"                                                                    \
    "ds_read_b128 v[32:35], v43 offset:(" #T "+1)*2560+1280\n\t"                                   \
    "s_mov_b64 exec, -1\n\t"
#define PRIO(n, L) "s_cmp_ge_u32 %[psw], 4\n\ts_cbranch_scc1 " #L "f\n\ts_setprio " #n "\n\t" #L ":\n\t"
#define PRIO_A
#define PRIO_B PRIO(0, 4)
#define STEP_62(C, P, T)                                                                           \
    "s_waitcnt lgkmcnt(3)\n\t"                                                                     \
    "v_smfmac_f32_32x32x32_bf16 v[80:95], v[36:39], v[" #P "+8:" #P "+15], v40\n\t"                \
    "s_mov_b64 exec, %[vm]\n\t"                                                                    \
    "ds_read_b128 v[36:39], v43 offset:" #T "*2560+1920\n\t"                                       \
    "s_mov_b64 exec, -1\n\t"                                                                       \
    EXP4(C, 0, 1, 2, 3)                                                                            \
    "s_waitcnt lgkmcnt(1)\n\t"                                                                     \
    "v_mfma_f32_32x32x16_bf16 v[" #P ":" #P "+15], v[24:27], v[16:19], 0\n\t"                      \
    EXP4(C, 4, 5, 6, 7)                                                                            \
    "v_mfma_f32_32x32x16_bf16 v[" #P ":" #P "+15], v[28:31], v[20:23], v[" #P ":" #P "+15]\n\t"    \
    EXP4(C, 8, 9, 10, 11)                                                                          \
    "v_smfmac_f32_32x32x32_bf16 v[80:95], v[32:35], v[" #C ":" #C "+7], v40\n\t"                   \
    EXP4(C, 12, 13, 14, 15)                                                                        \
    "s_mov_b64 exec, %[vm]\n\t"                                                                    \
    "ds_read_b128 v[32:35], v43 offset:(" #T "+1)*2560+1280\n\t"                                   \
    "s_mov_b64 exec, -1\n\t"
#define STEP_63(C, P, T)                                                                           \
    "s_waitcnt lgkmcnt(1)\n\t"                                                                     \
    "v_smfmac_f32_32x32x32_bf16 v[80:95], v[36:39], v[" #P "+8:" #P "+15], v40\n\t"                \
    "s_mov_b64 exec, %[vm]\n\t"                                                                    \
    "ds_read_b128 v[36:39], v43 offset:" #T "*2560+1920\n\t"                                       \
    "s_mov_b64 exec, -1\n\t"                                                                       \
    EXP4(C, 0, 1, 2, 3)                                                                            \
    EXP4(C, 4, 5, 6, 7)                                                                            \
    EXP4(C, 8, 9, 10, 11)                                                                          \
    "s_waitcnt lgkmcnt(1)\n\t"                                                                     \
    "v_smfmac_f32_32x32x32_bf16 v[80:95], v[32:35], v[" #C ":" #C "+7], v40\n\t"                   \
    EXP4(C, 12, 13, 14, 15)                                                                        \
    "s_waitcnt lgkmcnt(0)\n\t"                                                                     \
    "s_nop 1\n\t"                                                                                  \
    "v_smfmac_f32_32x32x32_bf16 v[80:95], v[36:39], v[" #C "+8:" #C "+15], v40\n\t"                \
    "s_nop 15\n\t"

__global__ __launch_bounds__(768, 3) void gated_mix_kernel(const u16* __restrict__ x, const u16* __restrict__ wA, const u16* __restrict__ wB,
                                                            const u16* __restrict__ woA, const u16* __restrict__ woB,
                                                            const u16* __restrict__ gate_w, char* __restrict__ ws, u16* __restrict__ out) {
    __shared__ __attribute__((aligned(16))) char smem[KV_HEAD];
    lds_ptr lds = (lds_ptr)smem;
    const int tid = threadIdx.x, lane = tid & 63, wid = __builtin_amdgcn_readfirstlane(tid >> 6);
    const int g = lane >> 4, r16 = lane & 15;
    const rsrc_t wrs = __builtin_amdgcn_make_buffer_rsrc(ws, 0, (int)WS_END, 0x00020000);
    STAMP_DECL STAMP(0, 0);
    u64 tag = 0;
    const int xcd = blockIdx.x & 7, idx = blockIdx.x >> 3;
    const int mt = (xcd & 1) * 8 + (idx & 7), nt = (xcd >> 1) * 4 + (idx >> 3), m0 = mt * 128;
    const bool late = nt < 15 && (wid & 2);
    u32x4 pv[4][2];
    unsigned poff[4];
    int pstride = 0;
    auto publish = [&]() {
        const int ps = __builtin_amdgcn_readfirstlane(pstride);
#pragma unroll
        for (int j = 0; j < 4; ++j) {
            if (!(poff[j] & 1)) {
#pragma unroll
                for (int i = 0; i < 2; ++i) ST16(pv[j][i], poff[j], i * ps);
            } else {
#pragma unroll
                for (int i = 0; i < 2; ++i) {
                    ST8((u32x2{pv[j][i][0], pv[j][i][1]}), poff[j] - 1, i * ps);
                    ST8((u32x2{pv[j][i][2], pv[j][i][3]}), poff[j] - 1, (i + 64) * ps);
                }
            }
        }
    };
    {
        if (nt == 15) {
            if (wid >= 8) tag = __hip_atomic_load((gu64*)(ws + WS_EPOCH), RLX_AGENT) + 1;
            const int task = mt * 10 + wid, ks = task >> 2, cg = task & 3;
            lds_ptr st = lds + wid * 10240;
            if (wid < 10) {
                const char* wsrc = (const char*)((ks >= 20 ? woB : woA) + cg * 160);
#pragma unroll
                for (int i = 0; i < 10; ++i) {
                    const int ci = i * 64 + lane, slot = ci / 20, cc = ci - slot * 20;
                    const int c = (ks >= 20 ? ks - 20 : ks) * 4 + (slot >> 3), e = slot & 7, p = c / 5, r = c - p * 5;
                    const int k = r < 4 ? (2 * p + (r >> 1)) * 20 + 4 * (r & 1) + (e & 3) + 8 * (e >> 2) : (2 * p + (e >> 2)) * 20 + 16 + (e & 3);
                    glds16(wsrc, k * (DM * 2) + cc * 16, st + i * 1024);
                }
            }
            u32x4 gv[2];
            gv[0] = *(const u32x4*)(gate_w + lane * 8);
            gv[1] = lane < 16 ? *(const u32x4*)(gate_w + 512 + lane * 8) : u32x4{0u, 0u, 0u, 0u};
            float s[11];
#pragma unroll
            for (int u = 0; u < 11; ++u) {
                const int row = wid + 12 * u < 128 ? wid + 12 * u : wid;
                const u16* xr = x + (size_t)(m0 + row) * DM;
                const u32x4 xv[2] = {*(const u32x4*)(xr + lane * 8), *(const u32x4*)(xr + 512 + r16 * 8)};
                float a = 0.f;
#pragma unroll
                for (int c = 0; c < 2; ++c)
#pragma unroll
                    for (int e = 0; e < 4; ++e) {
                        a = fmaf(__uint_as_float(xv[c][e] << 16), __uint_as_float(gv[c][e] << 16), a);
                        a = fmaf(__uint_as_float(xv[c][e] & 0xffff0000u), __uint_as_float(gv[c][e] & 0xffff0000u), a);
                    }
                s[u] = a;
            }
#pragma unroll
            for (int o = 32; o; o >>= 1)
#pragma unroll
                for (int u = 0; u < 11; ++u) s[u] += __shfl_xor(s[u], o);
            float z = s[0];
#pragma unroll
            for (int u = 1; u < 11; ++u) z = lane == u ? s[u] : z;
            if (lane < 11 && wid + 12 * lane < 128) ST4(__float_as_uint(1.f / (1.f + __expf(-z))), (wid + 12 * lane) * 4, (int)WS_G + m0 * 4);
            WAIT_V(0);
            if (wid < 10) {
                lds_ptr rp = st + (8 * g + (r16 >> 2)) * 320 + (lane & 3) * 8;
#pragma unroll
                for (int j = 0; j < 10; ++j)
                    ST16(__builtin_bit_cast(u32x4, cat8(lds_tr(rp + j * 32), lds_tr(rp + j * 32 + 4 * 320))), lane * 16, (int)WS_WO + (ks * 40 + cg * 10 + j) * 1024);
            }
            STAMP(0, 4);
        } else if (wid >= 8) {
            const int lw = wid - 8;
            const char* xg = (const char*)(x + (size_t)m0 * DM);
            const char* wl = (const char*)((lane & 16 ? wA : wB) + nt * 128 + r16 * 8) + (size_t)(lw + 4 * (lane >> 5)) * 3840;
            const unsigned xoff = (lw * 8 + (lane >> 3)) * (DM * 2) + (((lane & 7) ^ ((lane >> 3) & 7)) << 4);
            auto issue = [&](int t, int so) {
                lds_ptr st = lds + so;
#pragma unroll
                for (int i = 0; i < 4; ++i) glds16(xg + t * 128 + i * (32 * DM * 2), xoff, st + (lw + 4 * i) * 1024);
#pragma unroll
                for (int i = 0; i < 8; ++i) glds16v(wl + (size_t)(t * 64 + i * 8) * 3840, st + 16384 + (lw + 4 * i) * K1_WP);
            };
            int so = 0;
#pragma unroll 1
            for (int t = -2; t < 9; ++t) {
                if (t >= 0) {
                    WAIT_V(12);
                    if (t == 0) STAMP(0, 1);
                    RAW_BAR();
                }
                if (t < 8) issue(t + 2, so);
                so = so == 2 * K1_STAGE ? 0 : so + K1_STAGE;
            }
            WAIT_V(0);
            STAMP(0, 2);
            RAW_BAR();
            tag = __hip_atomic_load((gu64*)(ws + WS_EPOCH), RLX_AGENT) + 1;
        } else {
            const char* tp;
            if (lane < 16) {
                tp = (const char*)((lane & 2 ? wA : wB) + nt * 128 + (lane & 1) * 64) + (size_t)((wid + 2) * 64 + mt * 4 + (lane >> 2)) * 3840;
            } else {
                const int r = nt * 9 + (lane < 25 ? lane - 16 : 0);
                tp = (const char*)(x + (size_t)(m0 + (r < 127 ? r : 127)) * DM) + (wid + 2) * 128;
            }
            unsigned d0 = 0;
            {
                unsigned long long pc;
                asm volatile("s_getpc_b64 %0" : "=s"(pc));
                asm volatile("global_load_dword %0, %1, %2" : "+v"(d0) : "v"((wid * 64 + lane) * 64 < 23500 ? (wid * 64 + lane) * 64 : 0), "s"(pc & ~63ull) : "memory");
            }
            const int wm = wid >> 2, wn = wid & 3;
            const int xa = (wm * 64 + r16) * 128 + ((g ^ (lane & 7)) << 4);
            const int wa = 16384 + (4 * g + (r16 >> 2)) * K1_WP + (wn * 64 + 4 * (lane & 3)) * 2;
            f32x4 acc[4][4];
#pragma unroll
            for (int i = 0; i < 4; ++i)
#pragma unroll
                for (int j = 0; j < 4; ++j) acc[i][j] = f32x4{0.f, 0.f, 0.f, 0.f};
            const int tile0 = mt * 4 + wm * 2, br = wn < 2;
            if (nt < 10) {
                k1_main<false>(acc, lds, xa, wa, tp, wid + 2 - K1_PF, d0);
                const int cw = nt * 128 + (wn & 1) * 64;
                const bool isq = cw < 640;
                pstride = isq ? (int)Q_TILE : (int)KV_TILE;
                const int sbase = (int)(isq ? WS_Q : WS_KV) + (br * NH * 64 + tile0) * pstride;
                if (isq) {
#pragma unroll
                    for (int i = 0; i < 4; ++i)
#pragma unroll
                        for (int j = 0; j < 4; ++j) acc[i][j] *= QSCALE;
                }
                const int tk = (g & 1) * 16 + r16;
#pragma unroll
                for (int j = 0; j < 4; ++j) {
                    const int cp = (cw - (isq ? 0 : 640)) / 8 + j * 2 + (g >> 1), p = cp / 5, r = cp - p * 5;
                    const unsigned hoff = sbase + 2 * p * 64 * pstride;
                    poff[j] = r == 2 ? hoff + 1024 + tk * 8 + 1 : hoff + (r > 2 ? 64 * pstride + (r - 3) * 512 : r * 512) + tk * 16;
                }
            } else {
                k1_main<true>(acc, lds, xa, wa, tp, wid + 2 - K1_PF, d0);
                pstride = (int)KV_TILE;
#pragma unroll
                for (int j = 0; j < 4; ++j) {
                    const int c = br * 640 + (nt - 10) * 128 + (wn & 1) * 64 + j * 16 + r16, head = c / 20, d = c - head * 20;
                    poff[j] = (int)WS_KV + tile0 * (int)KV_TILE + 1280 + head * (int)KV_HEAD + (g & 1) * 640 + (g >> 1) * 320 + d * 16;
                }
            }
            STAMP(0, 1);
            PF_DONE();
#pragma unroll
            for (int j = 0; j < 4; ++j)
#pragma unroll
                for (int i = 0; i < 2; ++i) {
                    const f32x4 a = acc[2 * i][j], b = acc[2 * i + 1][j];
                    pv[j][i] = swap16(u32x2{pack2(a[0], a[1]), pack2(a[2], a[3])}, u32x2{pack2(b[0], b[1]), pack2(b[2], b[3])});
                }
            if (!late) publish();
            STAMP(0, 2);
        }
        asm volatile("s_waitcnt vmcnt(0)" : "+v"(tag) :: "memory");
        STAMP(0, 3);
        RAW_BAR();
        if (wid == 8 && lane == 0) __hip_atomic_store((gu64*)(ws + WS_F1) + nt * 16 + mt, tag, RLX_AGENT);
        FLUSH(0);
    }
    const int head = (blockIdx.x & 7) * 4 + ((blockIdx.x >> 3) & 3), qb = blockIdx.x >> 5;
    const int omt = head, ont = qb;
    const int r32 = lane & 31, hh = lane >> 5;
    const u32x4 vfill = r32 == 20 ? u32x4{ONE2, ONE2, ONE2, ONE2} : u32x4{0u, 0u, 0u, 0u};
    const int voff = hh * 320 + (r32 < 20 ? r32 : 0) * 16;
    const unsigned olane = (r32 >> 4) * (int)O_BLK + (r32 & 15) * 16;
    const int wimg = (int)WS_WO + ont * 5120;
    gu64* const f1 = (gu64*)(ws + WS_F1);
    constexpr int P_OFF = 102400;
    const int pslot = g * 256;
    STAMP(1, 0);
    if (wid >= 8) {
        const int hw = wid - 8;
        __builtin_amdgcn_s_setprio(3);
        if (hw == 0) {
            const int c = head * 20 + (g & 1) * 19;
            gu64* const w[2] = {f1 + (((g < 2 ? 640 : 1280) + c) >> 7) * 16 + r16, f1 + (c >> 7) * 16 + qb * 2 + (lane & 1)};
            wait_flags(w, tag);
        }
        RAW_BAR();
        STAMP(1, 1);
        const char* src = ws + WS_KV + (size_t)(NH + head) * KV_HEAD + hw * 1024;
#define LOADS(a, b) _Pragma("unroll 1") for (int i = a; i < b; ++i) glds16_dev(src + i * 4096, lane * 16, lds + hw * 1024 + i * 4096)
        LOADS(0, 12);
        WAIT_V(5); RAW_BAR();
        if (hw == 0 && lane == 0) __hip_atomic_store(f1 + 256 + nt * 16 + mt, tag, RLX_AGENT);
        LOADS(12, 40);
        f32x16 c0, c1;
        {
            const unsigned long long msk = ~(0x15554ull << r32) >> (4 * hh);
            const unsigned mlo = (unsigned)msk, mhi = (unsigned)(msk >> 32);
#pragma unroll
            for (int r = 0; r < 16; ++r) {
                const int u = (r & 3) + 8 * (r >> 2);
                c0[r] = r < 8 ? 0.f : __uint_as_float(__builtin_amdgcn_sbfe(mlo, (u - 16) & 31, 1) & 0xff800000u);
                c1[r] = __uint_as_float(__builtin_amdgcn_sbfe(u < 16 ? mlo : mhi, (u + 16) & 31, 1) & 0xff800000u);
            }
        }
        WAIT_V(28); RAW_BAR();
        WAIT_V(0); RAW_BAR();
        {
            gu64* const w[4] = {f1 + 256 + lane, f1 + 320 + lane, f1 + 384 + lane, f1 + 448 + lane};
            wait_flags(w, tag);
            if (blockIdx.x == 0 && hw == 0 && lane == 0) __hip_atomic_store((gu64*)(ws + WS_EPOCH), tag, RLX_AGENT);
        }
        STAMP(1, 2);
#pragma unroll 1
        for (int it = 0; it < (ABL & 1 ? 0 : 2); ++it) {
            const int qt = qb * 8 + hw * 2 + 1 - it;
            const int qA = (int)WS_Q + (head * 64 + qt) * (int)Q_TILE;
            const int k1 = (int)WS_KV + (head * 64 + qt) * (int)KV_TILE, k0 = qt ? k1 - (int)KV_TILE : k1;
            const u32x4 aqa = LD16(lane * 16, qA);
            u32x2 aqb = {0u, 0u};
            if (!hh) aqb = LD8(r32 * 8, qA + 1024);
            const u32x4 ka0 = LD16(lane * 16, k0), ka1 = LD16(lane * 16, k1);
            const u32x2 kb0 = LD8(r32 * 8, k0 + 1024), kb1 = LD8(r32 * 8, k1 + 1024);
            u32x4 v0h = vfill, v1l = vfill, v1h = vfill;
            if (r32 < 20) { v0h = LD16(voff, k0 + 1920); v1l = LD16(voff, k1 + 1280); v1h = LD16(voff, k1 + 1920); }
            const float gv = __uint_as_float(LD4(r32 * 4, (int)WS_G + qt * 128));
            if (qt == 0) {
#pragma unroll
                for (int r = 8; r < 16; ++r) c0[r] = -INFINITY;
            }
            const bf16x8 fqa = BF8(aqa), fqb = BF8((u32x4{aqb[0], aqb[1], 0u, 0u}));
            f32x16 s0 = MFMA32(BF8(ka0), fqa, c0);
            f32x16 s1 = MFMA32(BF8(ka1), fqa, c1);
            s0 = MFMA32(BF8((u32x4{kb0[0], kb0[1], 0u, 0u})), fqb, s0);
            s1 = MFMA32(BF8((u32x4{kb1[0], kb1[1], 0u, 0u})), fqb, s1);
            float m = max3f(s0[8], s0[9], s0[10]);
            m = max3f(m, s0[11], s0[12]); m = max3f(m, s0[13], s0[14]);
            m = xhalf_max(max16(s1, fmaxf(m, s0[15])));
#pragma unroll
            for (int r = 8; r < 16; ++r) s0[r] = __builtin_amdgcn_exp2f(s0[r] - m);
#pragma unroll
            for (int r = 0; r < 16; ++r) s1[r] = __builtin_amdgcn_exp2f(s1[r] - m);
            f32x16 o = SMFMAC(BF8(v0h), HI8(s0), f32x16{});
            o = SMFMAC(BF8(v1l), LO8(s1), o);
            o = SMFMAC(BF8(v1h), HI8(s1), o);
            store_o(o, gv * __builtin_amdgcn_rcpf(row_sum(o)), wrs, olane, (int)WS_O + qt * 2 * (int)O_BLK, head, hh);
            put_o(u32x4{~0u, ~0u, ~0u, ~0u}, u32x2{~0u, ~0u}, wrs, olane, (int)WS_O + (128 + qt * 2) * (int)O_BLK, head, hh);
        }
        WAIT_V(0);
        STAMP(1, 3);
        RAW_BAR();
        if (hw == 0 && lane == 0) __hip_atomic_store((gu64*)(ws + WS_FA) + qb * 32 + head, tag, RLX_AGENT);
#define WLOAD(ks) _Pragma("unroll") for (int j = 0; j < 5; ++j) glds16_dev(ws + wimg + (20 + (ks)) * 40960 + j * 1024, lane * 16, lds + (ks) * 5120 + j * 1024)
#pragma unroll 1
        for (int i = 0; i < 4; ++i) WLOAD(hw * 4 + i);
        {
            gu64* const w[1] = {(gu64*)(ws + WS_FA) + (omt >> 2) * 32 + r32};
            wait_flags(w, tag);
        }
        f32x4 acc[2][5];
        bf16x8 of[2][2], wf[2][5];
        const int osrc = (int)WS_O + (omt * 4 + (hw & 1) * 2) * (int)O_BLK + (hw >> 1) * 10240, wsrc = wimg + (hw >> 1) * 409600;
        auto ld = [&](int b, int ks) {
#pragma unroll
            for (int i = 0; i < 2; ++i) of[b][i] = BF8(LD16(lane * 16, osrc + i * (int)O_BLK + ks * 1024));
#pragma unroll
            for (int j = 0; j < 5; ++j) wf[b][j] = BF8(LD16(lane * 16, wsrc + ks * 40960 + j * 1024));
        };
        auto mm = [&](int b) {
#pragma unroll
            for (int j = 0; j < 5; ++j) {
#pragma unroll
                for (int i = 0; i < 2; ++i) acc[i][j] = MFMA16(wf[b][j], of[b][i], acc[i][j]);
                if (THIN) { SB(); __builtin_amdgcn_s_sleep(1); SB(); }
            }
        };
#pragma unroll
        for (int i = 0; i < 2; ++i)
#pragma unroll
            for (int j = 0; j < 5; ++j) acc[i][j] = f32x4{0.f, 0.f, 0.f, 0.f};
        ld(0, 0);
#pragma unroll 1
        for (int ks = 0; ks < (ABL & 2 ? 0 : 10); ks += 2) {
            ld(1, ks + 1);
            mm(0);
            ld(0, ks < 8 ? ks + 2 : 0);
            mm(1);
        }
        STAMP(1, 4);
        RAW_BAR();
        WLOAD(16 + hw);
#pragma unroll
        for (int i = 0; i < 2; ++i)
#pragma unroll
            for (int j = 0; j < 5; ++j) *(LDS_AS f32x4*)(lds + P_OFF + (hw >> 1) * 20480 + (((hw & 1) * 2 + i) * 5 + j) * 1024 + pslot + ((r16 + 2 * j + (g >> 1)) & 15) * 16) = acc[i][j];
        WAIT_V(0);
        STAMP(1, 5);
        RAW_BAR();
    } else {
        const int qt = qb * 8 + wid;
        if (late) publish();
        RAW_BAR();
        const int qB = (int)WS_Q + ((NH + head) * 64 + qt) * (int)Q_TILE;
        const u32x4 qa = LD16(lane * 16, qB);
        u32x2 qb2 = {0u, 0u};
        if (!hh) qb2 = LD8(r32 * 8, qB + 1024);
        const bf16x8 fqa = BF8(qa), fqb0 = BF8((u32x4{qb2[0], qb2[1], 0u, 0u}));
        lds_ptr ka_p = lds + lane * 16, kb_p = lds + 1024 + r32 * 8;
        auto tile_max = [&](int t, float m) {
            const u32x2 kb = *(const LDS_AS u32x2*)(kb_p + t * KV_TILE);
            f32x16 s = MFMA32(lds_b128(ka_p + t * KV_TILE), fqa, f32x16{});
            s = MFMA32(BF8((u32x4{kb[0], kb[1], 0u, 0u})), fqb0, s);
            return max16(s, m);
        };
        WAIT_V(0);
        RAW_BAR();
        STAMP(1, 1);
        const float m = xhalf_max(tile_max(0, -INFINITY));
        const unsigned long long vmask = 0x000fffff000fffffull;
        const u32x4 qbs = with_ref(qb2, m, hh);
        f32x16 o = {}, s1 = {};
        float gate;
        u32x4 vlo = vfill, vhi = vfill;
        unsigned a_ka = (unsigned)(size_t)ka_p, a_kb = (unsigned)(size_t)(lds + r32 * 8), a_v = (unsigned)(size_t)(lds + voff);
        int nbar = 0x4B, it = 7;
        const int psw = PRIO_SW ? wid : 4;
        __builtin_amdgcn_s_setprio(1);
        asm volatile(
            "v_mov_b32 v30, %[one]\n\tv_mov_b32 v31, %[one]\n\tv_mov_b32 v40, %[sidx]\n\t"
            "ds_read_b128 v[24:27], v41\n\t"
            "ds_read_b64 v[28:29], v42 offset:1024\n\t"
            "s_waitcnt lgkmcnt(0)\n\t"
            "v_mfma_f32_32x32x16_bf16 v[48:63], v[24:27], v[16:19], 0\n\t"
            "v_mfma_f32_32x32x16_bf16 v[48:63], v[28:31], v[20:23], v[48:63]\n\t"
            "ds_read_b128 v[24:27], v41 offset:2560\n\t"
            "ds_read_b64 v[28:29], v42 offset:2560+1024\n\t"
            "s_mov_b64 exec, %[vm]\n\t"
            "ds_read_b128 v[32:35], v43 offset:1280\n\t"
            "s_mov_b64 exec, -1\n\t"
            "s_nop 7\n\t"
            "1:\n\t"
            PRIO(2, 3) STEP(48, 64, 0) STEP(64, 48, 1) STEP(48, 64, 2) STEP(64, 48, 3) PRIO_A
            STEP(48, 64, 4) PRIO_B STEP(64, 48, 5) STEP(48, 64, 6) STEP(64, 48, 7)
            "v_add_u32 v41, 20480, v41\n\tv_add_u32 v42, 20480, v42\n\tv_add_u32 v43, 20480, v43\n\t"
            "s_bitcmp0_b32 %[nbar], 0\n\t"
            "s_cbranch_scc1 2f\n\t"
            "s_waitcnt lgkmcnt(3)\n\ts_barrier\n\t"
            "2:\n\t"
            "s_lshr_b32 %[nbar], %[nbar], 1\n\t"
            "s_sub_i32 %[it], %[it], 1\n\t"
            "s_cmp_lg_u32 %[it], 0\n\t"
            "s_cbranch_scc1 1b\n\t"
            "global_load_dword v44, %[goff], %[gb] sc1\n\t"
            PRIO(2, 3) STEP(48, 64, 0) STEP(64, 48, 1) STEP(48, 64, 2) STEP(64, 48, 3) PRIO_A
            STEP(48, 64, 4) PRIO_B STEP(64, 48, 5) STEP_62(48, 64, 6) STEP_63(64, 48, 7)
            "s_waitcnt vmcnt(0)"
            : "={v44}"(gate), "+{v[80:95]}"(o), "+{v[64:79]}"(s1), "+{v[32:35]}"(vlo), "+{v[36:39]}"(vhi), "+{v41}"(a_ka), "+{v42}"(a_kb), "+{v43}"(a_v),
              [nbar] "+s"(nbar), [it] "+s"(it)
            : "{v[16:19]}"(qa), "{v[20:23]}"(qbs), [vm] "s"(vmask), [one] "s"(ONE2), [sidx] "s"(0xDDDDDDDDu), [psw] "s"(psw),
              [goff] "v"(r32 * 4), [gb] "s"(ws + WS_G + qt * 128)
            : "v24", "v25", "v26", "v27", "v28", "v29", "v30", "v31", "v40", "v48", "v49", "v50", "v51", "v52", "v53", "v54", "v55",
              "v56", "v57", "v58", "v59", "v60", "v61", "v62", "v63", "scc", "memory");
        STAMP(1, 2);
        __builtin_amdgcn_s_setprio(3);
        const float gB = 1.f - gate;
        float l = row_sum(o);
        float amax = hh ? 0.f : fmaxf(fmaxf(fabsf(o[8]), fabsf(o[9])), fmaxf(fabsf(o[10]), fabsf(o[11])));
#pragma unroll
        for (int i = 0; i < 8; ++i) amax = fmaxf(amax, fabsf(o[i]));
        if (FORCE_FALLBACK || !__all(l >= 0x1p-64f && l <= 0x1p64f && amax <= 0x1p126f)) {
            const int kv = (int)WS_KV + (NH + head) * (int)KV_HEAD;
            u32x4 qr = {qb2[0], qb2[1], 0u, 0u};
            float mt2 = -INFINITY;
            o = f32x16{};
#pragma unroll 1
            for (int t = 0; t < 128; ++t) {
                const int kt = kv + (t & 63) * (int)KV_TILE;
                const u32x2 kb = LD8(r32 * 8, kt + 1024);
                f32x16 sc = MFMA32(BF8(LD16(lane * 16, kt)), fqa, f32x16{});
                sc = MFMA32(BF8((u32x4{kb[0], kb[1], ONE2, ONE2})), BF8(qr), sc);
                if (t < 64) {
                    mt2 = max16(sc, mt2);
                    if (t == 63) qr = with_ref(qb2, xhalf_max(mt2), hh);
                } else {
                    u32x4 v0 = vfill, v1 = vfill;
                    if (r32 < 20) { v0 = LD16(voff, kt + 1280); v1 = LD16(voff, kt + 1920); }
#pragma unroll
                    for (int r = 0; r < 16; ++r) sc[r] = __builtin_amdgcn_exp2f(sc[r]);
                    o = SMFMAC(BF8(v0), LO8(sc), o);
                    o = SMFMAC(BF8(v1), HI8(sc), o);
                }
            }
            l = row_sum(o);
        }
        store_o(o, gB * __builtin_amdgcn_rcpf(l), wrs, olane, (int)WS_O + (128 + qt * 2) * (int)O_BLK, head, hh);
        STAMP(1, 6);
        const int mb = wid & 3, kh = wid >> 2;
        const int osrc = (int)WS_O + (128 + omt * 4 + mb) * (int)O_BLK + kh * 10240;
        u32x4 of[10];
#pragma unroll
        for (int s = 0; s < 10; ++s) of[s] = LD16(lane * 16, osrc + s * 1024);
        SB();
        STAMP(1, 3);
        RAW_BAR();
        f32x4 acc[5];
        lds_ptr pp = lds + P_OFF + kh * 20480 + mb * 5120 + pslot;
#pragma unroll
        for (int j = 0; j < 5; ++j) acc[j] = *(const LDS_AS f32x4*)(pp + j * 1024 + ((r16 + 2 * j + (g >> 1)) & 15) * 16);
        lds_ptr wp = lds + kh * 51200 + lane * 16;
#pragma unroll
        for (int s = 0; s < 10; s += 2) {
            bf16x8 wf[10];
#pragma unroll
            for (int j = 0; j < 10; ++j) wf[j] = lds_b128(wp + s * 5120 + j * 1024);
#pragma unroll 1
            for (unsigned spins = 0; spins < (1u << 20); ++spins) {
                const u32x4 a = __builtin_elementwise_max(of[s], of[s + 1]);
                if (!__any(max(max(a[0], a[1]), max(a[2], a[3])) == ~0u)) break;
                asm volatile("s_sleep 1" ::: "memory");
#pragma unroll
                for (int t = s; t < 10; ++t) of[t] = LD16(lane * 16, osrc + t * 1024);
            }
            if (s == 0) STAMP(1, 4);
#pragma unroll
            for (int j = 0; j < 10; ++j) acc[j % 5] = MFMA16(wf[j], BF8(of[s + j / 5]), acc[j % 5]);
        }
        STAMP(1, 5);
#pragma unroll
        for (int j = 0; j < 5; ++j) *(LDS_AS f32x4*)(pp + j * 1024 + ((r16 + 2 * j + (g >> 1)) & 15) * 16) = acc[j];
    }
    RAW_BAR();
    if (tid < 640) {
        const int row = tid / 10, c = tid - row * 10;
        lds_ptr q = lds + P_OFF + ((row >> 4) * 5 + (c >> 1)) * 1024 + (c & 1) * 512 + (((row & 15) + c) & 15) * 16;
        f32x4 a = {0.f, 0.f, 0.f, 0.f}, b = a;
#pragma unroll
        for (int p = 0; p < 2; ++p) {
            a += *(const LDS_AS f32x4*)(q + p * 20480);
            b += *(const LDS_AS f32x4*)(q + p * 20480 + 256);
        }
        u32x4 v = {pack2(a[0], a[1]), pack2(a[2], a[3]), pack2(b[0], b[1]), pack2(b[2], b[3])};
        *(u32x4*)(out + (size_t)(omt * 64 + row) * DM + ont * 80 + c * 8) = v;
    }
    DIAG_WAIT();
    STAMP(1, 7); FLUSH(1);
}

extern "C" void kernel_launch(void* const* d_in, const int* in_sizes, int n_in, void* d_out, int out_size, void* d_ws, size_t ws_size,
                              hipStream_t stream) {
    gated_mix_kernel<<<256, 768, 0, stream>>>((const u16*)d_in[0], (const u16*)d_in[1], (const u16*)d_in[4], (const u16*)d_in[2],
                                              (const u16*)d_in[5], (const u16*)d_in[6], (char*)d_ws, (u16*)d_out);
}
```

```cpp
#include <hip/hip_runtime.h>
#include <hip/hip_bf16.h>

typedef __attribute__((ext_vector_type(8))) __bf16 bf16x8;
typedef __attribute__((ext_vector_type(16))) __bf16 bf16x16;
typedef __attribute__((ext_vector_type(2))) __bf16 bf16x2;
typedef __attribute__((ext_vector_type(4))) short s16x4;
typedef __attribute__((ext_vector_type(8))) short s16x8;
typedef __attribute__((ext_vector_type(2))) float f32x2;
typedef __attribute__((ext_vector_type(4))) float f32x4;
typedef __attribute__((ext_vector_type(8))) float f32x8;
typedef __attribute__((ext_vector_type(16))) float f32x16;
typedef __attribute__((ext_vector_type(2))) unsigned u32x2;
typedef __attribute__((ext_vector_type(4))) unsigned u32x4;
typedef unsigned short u16;
#define LDS_AS __attribute__((address_space(3)))
typedef LDS_AS char* lds_ptr;

constexpr int SEQ = 2048, DM = 640, NH = 32, DH = 20;
constexpr size_t KV_TILE = 2560, KV_HEAD = 64 * KV_TILE;
constexpr size_t Q_TILE = 1280, Q_HEAD = 64 * Q_TILE;
constexpr size_t WS_KV = 0;
constexpr size_t WS_Q = WS_KV + 2 * NH * KV_HEAD;
constexpr size_t O_BLK = 80 * 256;
constexpr size_t WS_O = WS_Q + 2 * NH * Q_HEAD;
constexpr size_t WS_WO = WS_O + 2 * 128 * O_BLK;
constexpr size_t WS_G = WS_WO + 40 * 40 * 1024;
constexpr size_t WS_F1 = WS_G + SEQ * 4;
constexpr size_t WS_FA = WS_F1 + 512 * 8;
constexpr size_t WS_EPOCH = WS_FA + 256 * 8;
constexpr size_t WS_END = WS_EPOCH + 128;
#define STAMP_DECL
#define STAMP(k, i)
#define FLUSH(k)
#define DIAG_WAIT()
constexpr float QSCALE = 0.22360679774997896f * 1.4426950408889634f;

#define K1_PF 3
#define PF_DONE() asm volatile("s_waitcnt vmcnt(0)" : "+v"(d0) :: "memory")
#define PRIO_SW 5
#define ABL 0
#define PROBE 0
#define EV 0
#define THIN 1
#define FORCE_FALLBACK 0
#define WAIT_V(n) asm volatile("s_waitcnt vmcnt(" #n ")" ::: "memory")
#define RAW_BAR() asm volatile("s_waitcnt lgkmcnt(0)\n\ts_barrier" ::: "memory")
#define MFMA16(a, b, c) __builtin_amdgcn_mfma_f32_16x16x32_bf16(a, b, c, 0, 0, 0)
#define MFMA32(a, b, c) __builtin_amdgcn_mfma_f32_32x32x16_bf16(a, b, c, 0, 0, 0)

__device__ __forceinline__ unsigned pack2(float lo, float hi) {
    f32x2 v = {lo, hi};
    return __builtin_bit_cast(unsigned, __builtin_convertvector(v, bf16x2));
}
__device__ __forceinline__ void glds16(const char* sbase, unsigned voff, lds_ptr l) {
    unsigned keep;
    asm volatile("s_mov_b32 %0, m0\n\ts_mov_b32 m0, %3\n\ts_nop 0\n\tglobal_load_lds_dwordx4 %1, %2\n\ts_mov_b32 m0, %0"
                 : "=&s"(keep) : "v"(voff), "s"(sbase), "s"((unsigned)(size_t)l) : "memory");
}
__device__ __forceinline__ void glds16v(const char* g, lds_ptr l) {
    unsigned keep;
    asm volatile("s_mov_b32 %0, m0\n\ts_mov_b32 m0, %2\n\ts_nop 0\n\tglobal_load_lds_dwordx4 %1, off\n\ts_mov_b32 m0, %0"
                 : "=&s"(keep) : "v"(g), "s"((unsigned)(size_t)l) : "memory");
}
__device__ __forceinline__ bf16x8 lds_b128(lds_ptr p) { return *(const LDS_AS bf16x8*)p; }
__device__ __forceinline__ s16x4 lds_tr(lds_ptr p) { return __builtin_amdgcn_ds_read_tr16_b64_v4i16((LDS_AS s16x4*)p); }
__device__ __forceinline__ bf16x8 cat8(s16x4 a, s16x4 b) {
    return __builtin_bit_cast(bf16x8, __builtin_shufflevector(a, b, 0, 1, 2, 3, 4, 5, 6, 7));
}
typedef __amdgpu_buffer_rsrc_t rsrc_t;
typedef unsigned long long u64;
typedef __attribute__((address_space(1))) u64 gu64;
#define RLX_AGENT __ATOMIC_RELAXED, __HIP_MEMORY_SCOPE_AGENT
constexpr int SC1 = 16;
#define LD16(voff, soff) __builtin_amdgcn_raw_buffer_load_b128(wrs, voff, soff, SC1)
#define LD8(voff, soff) __builtin_amdgcn_raw_buffer_load_b64(wrs, voff, soff, SC1)
#define LD4(voff, soff) __builtin_amdgcn_raw_buffer_load_b32(wrs, voff, soff, SC1)
#define ST16(v, voff, soff) __builtin_amdgcn_raw_buffer_store_b128(v, wrs, (voff) + (soff), 0, SC1)
#define ST8(v, voff, soff) __builtin_amdgcn_raw_buffer_store_b64(v, wrs, voff, soff, SC1)
#define ST4(v, voff, soff) __builtin_amdgcn_raw_buffer_store_b32(v, wrs, voff, soff, SC1)
#define BF8(x) __builtin_bit_cast(bf16x8, x)
__device__ __forceinline__ void glds16_dev(const char* sbase, unsigned voff, lds_ptr l) {
    unsigned keep;
    asm volatile("s_mov_b32 %0, m0\n\ts_mov_b32 m0, %3\n\ts_nop 0\n\tglobal_load_lds_dwordx4 %1, %2 sc1\n\ts_mov_b32 m0, %0"
                 : "=&s"(keep) : "v"(voff), "s"(sbase), "s"((unsigned)(size_t)l) : "memory");
}
template <int N>
__device__ __forceinline__ void wait_flags(gu64* const (&w)[N], u64 tag) {
#pragma unroll 1
    for (unsigned spins = 0; spins < (1u << 20); ++spins) {
        bool ok = true;
#pragma unroll
        for (int i = 0; i < N; ++i) ok &= __hip_atomic_load(w[i], RLX_AGENT) == tag;
        if (__all(ok)) break;
        __builtin_amdgcn_s_sleep(2);
    }
    __builtin_amdgcn_fence(__ATOMIC_ACQUIRE, "wavefront");
}
__device__ __forceinline__ u32x4 swap16(u32x2 a, u32x2 b) {
    auto x = __builtin_amdgcn_permlane16_swap(a[0], b[0], false, false);
    auto y = __builtin_amdgcn_permlane16_swap(a[1], b[1], false, false);
    return u32x4{x[0], y[0], x[1], y[1]};
}

constexpr int K1_WP = 1024 + 32;
constexpr int K1_STAGE = 16384 + 32 * K1_WP;
#define SB() __builtin_amdgcn_sched_barrier(0)

template <bool VT>
__device__ __forceinline__ void k1_main(f32x4 (&acc)[4][4], lds_ptr lds, int xa, int wa, const char* tp, int tstep, unsigned& d0) {
#define TOUCH() asm volatile("global_load_dword %0, %1, off" : "+v"(d0) : "v"(tp) : "memory")
    bf16x8 xf[2][4], wf[2][4];
    auto rd = [&](int so, int kk) {
#pragma unroll
        for (int j = 0; j < 4; ++j) {
            lds_ptr p = lds + so + wa + kk * (16 * K1_WP) + j * 32;
            wf[kk][j] = cat8(lds_tr(p), lds_tr(p + 512));
        }
#pragma unroll
        for (int i = 0; i < 4; ++i) xf[kk][i] = lds_b128(lds + so + (xa ^ (kk * 64)) + i * 2048);
    };
    auto mm = [&](int kk) {
#pragma unroll
        for (int i = 0; i < 4; ++i)
#pragma unroll
            for (int j = 0; j < 4; ++j)
                acc[i][j] = VT ? MFMA16(xf[kk][i], wf[kk][j], acc[i][j]) : MFMA16(wf[kk][j], xf[kk][i], acc[i][j]);
    };
#define ILV() do { _Pragma("unroll") for (int q_ = 0; q_ < 12; ++q_) { __builtin_amdgcn_sched_group_barrier(0x100, 1, 0); __builtin_amdgcn_sched_group_barrier(0x8, 1, 0); } __builtin_amdgcn_sched_group_barrier(0x8, 4, 0); SB(); } while (0)
    if (K1_PF && tstep < 0) TOUCH();
    RAW_BAR();
    if (K1_PF && tstep == 0) TOUCH();
    rd(0, 0); SB();
    rd(0, 1);
    mm(0); ILV();
    int so = K1_STAGE;
#pragma unroll 1
    for (int t = 1; t < 10; ++t) {
        RAW_BAR();
        if (K1_PF && tstep == t) TOUCH();
        rd(so, 0);
        mm(1); ILV();
        rd(so, 1);
        mm(0); ILV();
        so = so == 2 * K1_STAGE ? 0 : so + K1_STAGE;
    }
    mm(1);
}

#define SMFMAC(a, b8, c) __builtin_amdgcn_smfmac_f32_32x32x32_bf16(a, __builtin_bit_cast(bf16x16, b8), c, (int)0xDDDDDDDD, 0, 0)
#define LO8(s) __builtin_shufflevector(s, s, 0, 1, 2, 3, 4, 5, 6, 7)
#define HI8(s) __builtin_shufflevector(s, s, 8, 9, 10, 11, 12, 13, 14, 15)
constexpr unsigned ONE2 = 0x3F803F80u;

__device__ __forceinline__ float xhalf_max(float m) {
    auto r = __builtin_amdgcn_permlane32_swap(__float_as_uint(m), __float_as_uint(m), false, false);
    return fmaxf(__uint_as_float(r[0]), __uint_as_float(r[1]));
}
__device__ __forceinline__ float max3f(float a, float b, float c) { return __builtin_fmaxf(__builtin_fmaxf(a, b), c); }
__device__ __forceinline__ float max16(const f32x16& s, float m) {
#pragma unroll
    for (int r = 0; r < 16; r += 2) m = max3f(m, s[r], s[r + 1]);
    return m;
}
__device__ __forceinline__ float row_sum(const f32x16& o) {
    auto r = __builtin_amdgcn_permlane32_swap(__float_as_uint(o[8]), __float_as_uint(o[8]), false, false);
    return __uint_as_float(r[1]);
}
__device__ __forceinline__ u32x4 with_ref(u32x2 q, float m, int hh) {
    const unsigned h1 = __float_as_uint(-m) & 0xffff0000u;
    const float r1 = -m - __uint_as_float(h1);
    const unsigned h2 = __float_as_uint(r1) & 0xffff0000u;
    const unsigned h3 = __float_as_uint(r1 - __uint_as_float(h2)) >> 16;
    return u32x4{q[0], q[1], hh ? 0u : (h1 | (h2 >> 16)), hh ? 0u : h3};
}
__device__ __forceinline__ void put_o(u32x4 v, u32x2 t, rsrc_t wrs, unsigned voff, int soff, int head, int hh) {
    const int c0 = (head >> 1) * 5;
    ST16(v, voff + hh * 256, soff + (c0 + (head & 1) * 2) * 256);
    if (!hh) ST8(t, voff, soff + (c0 + 4) * 256 + (head & 1) * 8);
}
__device__ __forceinline__ unsigned pack_o(float a, float b) { return min(pack2(a, b), 0xFFFFFFFEu); }
__device__ __forceinline__ void store_o(const f32x16& o, float f, rsrc_t wrs, unsigned voff, int soff, int head, int hh) {
    put_o(u32x4{pack_o(o[0] * f, o[1] * f), pack_o(o[2] * f, o[3] * f), pack_o(o[4] * f, o[5] * f), pack_o(o[6] * f, o[7] * f)},
          u32x2{pack_o(o[8] * f, o[9] * f), pack_o(o[10] * f, o[11] * f)}, wrs, voff, soff, head, hh);
}

#define EXP4(C, a) "v_exp_f32 v[" #C "+" #a "], v[" #C "+" #a "]\n\tv_exp_f32 v[" #C "+" #a "+1], v[" #C "+" #a "+1]\n\t"         \
                   "v_exp_f32 v[" #C "+" #a "+2], v[" #C "+" #a "+2]\n\tv_exp_f32 v[" #C "+" #a "+3], v[" #C "+" #a "+3]\n\t"
#define PV(O, V, P) "v_smfmac_f32_32x32x32_bf16 v[" #O ":" #O "+15], v[" #V ":" #V "+3], v[" #P ":" #P "+7], v40\n\t"
#define QK0(S, Q) "v_mfma_f32_32x32x16_bf16 v[" #S ":" #S "+15], v[24:27], v[" #Q ":" #Q "+3], 0\n\t"
#define QK1(S, Q) "v_mfma_f32_32x32x16_bf16 v[" #S ":" #S "+15], v[28:31], v[" #Q "+4:" #Q "+7], v[" #S ":" #S "+15]\n\t"
#define VLOAD(T) "s_mov_b64 exec, %[vm]\n\tds_read_b128 v[32:35], v43 offset:" #T "*5120+1280\n\t"                                \
                 "ds_read_b128 v[36:39], v43 offset:" #T "*5120+1920\n\ts_mov_b64 exec, -1\n\t"
#define PAIR(T, MID)                                                                               \
    PV(96, 36, 72) VLOAD(T) EXP4(48, 0)                                                            \
    QK0(64, 16) "ds_read_b128 v[24:27], v41 offset:(" #T "+1)*5120\n\t" EXP4(48, 4)                \
    QK1(64, 16) "ds_read_b64 v[28:29], v42 offset:(" #T "+1)*5120+1024\n\t" EXP4(48, 8)            \
    "s_waitcnt lgkmcnt(2)\n\t" PV(80, 32, 48) EXP4(48, 12) MID "s_nop 1\n\t"                       \
    PV(80, 36, 56) EXP4(64, 0)                                                                     \
    "s_waitcnt lgkmcnt(0)\n\t" QK0(48, 8) EXP4(64, 4)                                              \
    QK1(48, 8) EXP4(64, 8)                                                                         \
    PV(96, 32, 64) EXP4(64, 12) "s_nop 1\n\t"
#define PAIR_LAST(T)                                                                               \
    PV(96, 36, 72) VLOAD(T) EXP4(48, 0)                                                            \
    QK0(64, 16) EXP4(48, 4)                                                                        \
    QK1(64, 16) EXP4(48, 8)                                                                        \
    "s_waitcnt lgkmcnt(0)\n\t" PV(80, 32, 48) EXP4(48, 12) "s_nop 2\n\t"                           \
    PV(80, 36, 56) EXP4(64, 0) EXP4(64, 4) EXP4(64, 8)                                             \
    PV(96, 32, 64) EXP4(64, 12) "s_nop 1\n\t"                                                      \
    PV(96, 36, 72) "s_nop 15\n\t"
#define PRIO(n, L) "s_cmp_ge_u32 %[psw], 4\n\ts_cbranch_scc1 " #L "f\n\ts_setprio " #n "\n\t" #L ":\n\t"

__global__ __launch_bounds__(768, 3) void gated_mix_kernel(const u16* __restrict__ x, const u16* __restrict__ wA, const u16* __restrict__ wB,
                                                            const u16* __restrict__ woA, const u16* __restrict__ woB,
                                                            const u16* __restrict__ gate_w, char* __restrict__ ws, u16* __restrict__ out) {
    __shared__ __attribute__((aligned(16))) char smem[KV_HEAD];
    lds_ptr lds = (lds_ptr)smem;
    const int tid = threadIdx.x, lane = tid & 63, wid = __builtin_amdgcn_readfirstlane(tid >> 6);
    const int g = lane >> 4, r16 = lane & 15;
    const rsrc_t wrs = __builtin_amdgcn_make_buffer_rsrc(ws, 0, (int)WS_END, 0x00020000);
    STAMP_DECL STAMP(0, 0);
    u64 tag = 0;
    const int xcd = blockIdx.x & 7, idx = blockIdx.x >> 3;
    const int mt = (xcd & 1) * 8 + (idx & 7), nt = (xcd >> 1) * 4 + (idx >> 3), m0 = mt * 128;
    const bool late = nt < 15 && (wid & 2);
    u32x4 pv[4][2];
    unsigned poff[4];
    int pstride = 0;
    auto publish = [&]() {
        const int ps = __builtin_amdgcn_readfirstlane(pstride);
#pragma unroll
        for (int j = 0; j < 4; ++j) {
            if (!(poff[j] & 1)) {
#pragma unroll
                for (int i = 0; i < 2; ++i) ST16(pv[j][i], poff[j], i * ps);
            } else {
#pragma unroll
                for (int i = 0; i < 2; ++i) {
                    ST8((u32x2{pv[j][i][0], pv[j][i][1]}), poff[j] - 1, i * ps);
                    ST8((u32x2{pv[j][i][2], pv[j][i][3]}), poff[j] - 1, (i + 64) * ps);
                }
            }
        }
    };
    {
        if (nt == 15) {
            if (wid >= 8) tag = __hip_atomic_load((gu64*)(ws + WS_EPOCH), RLX_AGENT) + 1;
            const int task = mt * 10 + wid, ks = task >> 2, cg = task & 3;
            lds_ptr st = lds + wid * 10240;
            if (wid < 10) {
                const char* wsrc = (const char*)((ks >= 20 ? woB : woA) + cg * 160);
#pragma unroll
                for (int i = 0; i < 10; ++i) {
                    const int ci = i * 64 + lane, slot = ci / 20, cc = ci - slot * 20;
                    const int c = (ks >= 20 ? ks - 20 : ks) * 4 + (slot >> 3), e = slot & 7, p = c / 5, r = c - p * 5;
                    const int k = r < 4 ? (2 * p + (r >> 1)) * 20 + 4 * (r & 1) + (e & 3) + 8 * (e >> 2) : (2 * p + (e >> 2)) * 20 + 16 + (e & 3);
                    glds16(wsrc, k * (DM * 2) + cc * 16, st + i * 1024);
                }
            }
            u32x4 gv[2];
            gv[0] = *(const u32x4*)(gate_w + lane * 8);
            gv[1] = lane < 16 ? *(const u32x4*)(gate_w + 512 + lane * 8) : u32x4{0u, 0u, 0u, 0u};
            float s[11];
#pragma unroll
            for (int u = 0; u < 11; ++u) {
                const int row = wid + 12 * u < 128 ? wid + 12 * u : wid;
                const u16* xr = x + (size_t)(m0 + row) * DM;
                const u32x4 xv[2] = {*(const u32x4*)(xr + lane * 8), *(const u32x4*)(xr + 512 + r16 * 8)};
                float a = 0.f;
#pragma unroll
                for (int c = 0; c < 2; ++c)
#pragma unroll
                    for (int e = 0; e < 4; ++e) {
                        a = fmaf(__uint_as_float(xv[c][e] << 16), __uint_as_float(gv[c][e] << 16), a);
                        a = fmaf(__uint_as_float(xv[c][e] & 0xffff0000u), __uint_as_float(gv[c][e] & 0xffff0000u), a);
                    }
                s[u] = a;
            }
#pragma unroll
            for (int o = 32; o; o >>= 1)
#pragma unroll
                for (int u = 0; u < 11; ++u) s[u] += __shfl_xor(s[u], o);
            float z = s[0];
#pragma unroll
            for (int u = 1; u < 11; ++u) z = lane == u ? s[u] : z;
            if (lane < 11 && wid + 12 * lane < 128) ST4(__float_as_uint(1.f / (1.f + __expf(-z))), (wid + 12 * lane) * 4, (int)WS_G + m0 * 4);
            WAIT_V(0);
            if (wid < 10) {
                lds_ptr rp = st + (8 * g + (r16 >> 2)) * 320 + (lane & 3) * 8;
#pragma unroll
                for (int j = 0; j < 10; ++j)
                    ST16(__builtin_bit_cast(u32x4, cat8(lds_tr(rp + j * 32), lds_tr(rp + j * 32 + 4 * 320))), lane * 16, (int)WS_WO + (ks * 40 + cg * 10 + j) * 1024);
            }
            STAMP(0, 4);
        } else if (wid >= 8) {
            const int lw = wid - 8;
            const char* xg = (const char*)(x + (size_t)m0 * DM);
            const char* wl = (const char*)((lane & 16 ? wA : wB) + nt * 128 + r16 * 8) + (size_t)(lw + 4 * (lane >> 5)) * 3840;
            const unsigned xoff = (lw * 8 + (lane >> 3)) * (DM * 2) + (((lane & 7) ^ ((lane >> 3) & 7)) << 4);
            auto issue = [&](int t, int so) {
                lds_ptr st = lds + so;
#pragma unroll
                for (int i = 0; i < 4; ++i) glds16(xg + t * 128 + i * (32 * DM * 2), xoff, st + (lw + 4 * i) * 1024);
#pragma unroll
                for (int i = 0; i < 8; ++i) glds16v(wl + (size_t)(t * 64 + i * 8) * 3840, st + 16384 + (lw + 4 * i) * K1_WP);
            };
            int so = 0;
#pragma unroll 1
            for (int t = -2; t < 9; ++t) {
                if (t >= 0) {
                    WAIT_V(12);
                    if (t == 0) STAMP(0, 1);
                    RAW_BAR();
                }
                if (t < 8) issue(t + 2, so);
                so = so == 2 * K1_STAGE ? 0 : so + K1_STAGE;
            }
            WAIT_V(0);
            STAMP(0, 2);
            RAW_BAR();
            tag = __hip_atomic_load((gu64*)(ws + WS_EPOCH), RLX_AGENT) + 1;
        } else {
            const char* tp;
            if (lane < 16) {
                tp = (const char*)((lane & 2 ? wA : wB) + nt * 128 + (lane & 1) * 64) + (size_t)((wid + 2) * 64 + mt * 4 + (lane >> 2)) * 3840;
            } else {
                const int r = nt * 9 + (lane < 25 ? lane - 16 : 0);
                tp = (const char*)(x + (size_t)(m0 + (r < 127 ? r : 127)) * DM) + (wid + 2) * 128;
            }
            unsigned d0 = 0;
            const int wm = wid >> 2, wn = wid & 3;
            const int xa = (wm * 64 + r16) * 128 + ((g ^ (lane & 7)) << 4);
            const int wa = 16384 + (4 * g + (r16 >> 2)) * K1_WP + (wn * 64 + 4 * (lane & 3)) * 2;
            f32x4 acc[4][4];
#pragma unroll
            for (int i = 0; i < 4; ++i)
#pragma unroll
                for (int j = 0; j < 4; ++j) acc[i][j] = f32x4{0.f, 0.f, 0.f, 0.f};
            const int tile0 = mt * 4 + wm * 2, br = wn < 2;
            if (nt < 10) {
                k1_main<false>(acc, lds, xa, wa, tp, wid + 2 - K1_PF, d0);
                const int cw = nt * 128 + (wn & 1) * 64;
                const bool isq = cw < 640;
                pstride = isq ? (int)Q_TILE : (int)KV_TILE;
                const int sbase = (int)(isq ? WS_Q : WS_KV) + (br * NH * 64 + tile0) * pstride;
                if (isq) {
#pragma unroll
                    for (int i = 0; i < 4; ++i)
#pragma unroll
                        for (int j = 0; j < 4; ++j) acc[i][j] *= QSCALE;
                }
                const int tk = (g & 1) * 16 + r16;
#pragma unroll
                for (int j = 0; j < 4; ++j) {
                    const int cp = (cw - (isq ? 0 : 640)) / 8 + j * 2 + (g >> 1), p = cp / 5, r = cp - p * 5;
                    const unsigned hoff = sbase + 2 * p * 64 * pstride;
                    poff[j] = r == 2 ? hoff + 1024 + tk * 8 + 1 : hoff + (r > 2 ? 64 * pstride + (r - 3) * 512 : r * 512) + tk * 16;
                }
            } else {
                k1_main<true>(acc, lds, xa, wa, tp, wid + 2 - K1_PF, d0);
                pstride = (int)KV_TILE;
#pragma unroll
                for (int j = 0; j < 4; ++j) {
                    const int c = br * 640 + (nt - 10) * 128 + (wn & 1) * 64 + j * 16 + r16, head = c / 20, d = c - head * 20;
                    poff[j] = (int)WS_KV + tile0 * (int)KV_TILE + 1280 + head * (int)KV_HEAD + (g & 1) * 640 + (g >> 1) * 320 + d * 16;
                }
            }
            STAMP(0, 1);
            PF_DONE();
#pragma unroll
            for (int j = 0; j < 4; ++j)
#pragma unroll
                for (int i = 0; i < 2; ++i) {
                    const f32x4 a = acc[2 * i][j], b = acc[2 * i + 1][j];
                    pv[j][i] = swap16(u32x2{pack2(a[0], a[1]), pack2(a[2], a[3])}, u32x2{pack2(b[0], b[1]), pack2(b[2], b[3])});
                }
            if (!late) publish();
            STAMP(0, 2);
        }
        asm volatile("s_waitcnt vmcnt(0)" : "+v"(tag) :: "memory");
        STAMP(0, 3);
        RAW_BAR();
        if (wid == 8 && lane == 0) __hip_atomic_store((gu64*)(ws + WS_F1) + nt * 16 + mt, tag, RLX_AGENT);
        FLUSH(0);
    }
    const int head = (blockIdx.x & 7) * 4 + ((blockIdx.x >> 3) & 3), qb = blockIdx.x >> 5;
    const int omt = head, ont = qb;
    const int r32 = lane & 31, hh = lane >> 5;
    const u32x4 vfill = r32 == 20 ? u32x4{ONE2, ONE2, ONE2, ONE2} : u32x4{0u, 0u, 0u, 0u};
    const int voff = hh * 320 + (r32 < 20 ? r32 : 0) * 16;
    const unsigned olane = (r32 >> 4) * (int)O_BLK + (r32 & 15) * 16;
    const int wimg = (int)WS_WO + ont * 5120;
    gu64* const f1 = (gu64*)(ws + WS_F1);
    constexpr int P_OFF = 102400;
    const int pslot = g * 256;
    STAMP(1, 0);
    if (wid >= 8) {
        const int hw = wid - 8;
        __builtin_amdgcn_s_setprio(3);
        if (hw == 0) {
            const int c = head * 20 + (g & 1) * 19;
            gu64* const w[2] = {f1 + (((g < 2 ? 640 : 1280) + c) >> 7) * 16 + r16, f1 + (c >> 7) * 16 + qb * 2 + (lane & 1)};
            wait_flags(w, tag);
        }
        RAW_BAR();
        STAMP(1, 1);
        const char* src = ws + WS_KV + (size_t)(NH + head) * KV_HEAD + hw * 1024;
#define LOADS(a, b) _Pragma("unroll 1") for (int i = a; i < b; ++i) glds16_dev(src + i * 4096, lane * 16, lds + hw * 1024 + i * 4096)
        LOADS(0, 12);
        WAIT_V(5); RAW_BAR();
        if (hw == 0 && lane == 0) __hip_atomic_store(f1 + 256 + nt * 16 + mt, tag, RLX_AGENT);
        LOADS(12, 40);
        f32x16 c0, c1;
        {
            const unsigned long long msk = ~(0x15554ull << r32) >> (4 * hh);
            const unsigned mlo = (unsigned)msk, mhi = (unsigned)(msk >> 32);
#pragma unroll
            for (int r = 0; r < 16; ++r) {
                const int u = (r & 3) + 8 * (r >> 2);
                c0[r] = r < 8 ? 0.f : __uint_as_float(__builtin_amdgcn_sbfe(mlo, (u - 16) & 31, 1) & 0xff800000u);
                c1[r] = __uint_as_float(__builtin_amdgcn_sbfe(u < 16 ? mlo : mhi, (u + 16) & 31, 1) & 0xff800000u);
            }
        }
        WAIT_V(28); RAW_BAR();
        WAIT_V(0); RAW_BAR();
        {
            gu64* const w[4] = {f1 + 256 + lane, f1 + 320 + lane, f1 + 384 + lane, f1 + 448 + lane};
            wait_flags(w, tag);
            if (blockIdx.x == 0 && hw == 0 && lane == 0) __hip_atomic_store((gu64*)(ws + WS_EPOCH), tag, RLX_AGENT);
        }
        STAMP(1, 2);
#pragma unroll 1
        for (int it = 0; it < (ABL & 1 ? 0 : 2); ++it) {
            const int qt = qb * 8 + hw * 2 + 1 - it;
            const int qA = (int)WS_Q + (head * 64 + qt) * (int)Q_TILE;
            const int k1 = (int)WS_KV + (head * 64 + qt) * (int)KV_TILE, k0 = qt ? k1 - (int)KV_TILE : k1;
            const u32x4 aqa = LD16(lane * 16, qA);
            u32x2 aqb = {0u, 0u};
            if (!hh) aqb = LD8(r32 * 8, qA + 1024);
            const u32x4 ka0 = LD16(lane * 16, k0), ka1 = LD16(lane * 16, k1);
            const u32x2 kb0 = LD8(r32 * 8, k0 + 1024), kb1 = LD8(r32 * 8, k1 + 1024);
            u32x4 v0h = vfill, v1l = vfill, v1h = vfill;
            if (r32 < 20) { v0h = LD16(voff, k0 + 1920); v1l = LD16(voff, k1 + 1280); v1h = LD16(voff, k1 + 1920); }
            const float gv = __uint_as_float(LD4(r32 * 4, (int)WS_G + qt * 128));
            if (qt == 0) {
#pragma unroll
                for (int r = 8; r < 16; ++r) c0[r] = -INFINITY;
            }
            const bf16x8 fqa = BF8(aqa), fqb = BF8((u32x4{aqb[0], aqb[1], 0u, 0u}));
            f32x16 s0 = MFMA32(BF8(ka0), fqa, c0);
            f32x16 s1 = MFMA32(BF8(ka1), fqa, c1);
            s0 = MFMA32(BF8((u32x4{kb0[0], kb0[1], 0u, 0u})), fqb, s0);
            s1 = MFMA32(BF8((u32x4{kb1[0], kb1[1], 0u, 0u})), fqb, s1);
            float m = max3f(s0[8], s0[9], s0[10]);
            m = max3f(m, s0[11], s0[12]); m = max3f(m, s0[13], s0[14]);
            m = xhalf_max(max16(s1, fmaxf(m, s0[15])));
#pragma unroll
            for (int r = 8; r < 16; ++r) s0[r] = __builtin_amdgcn_exp2f(s0[r] - m);
#pragma unroll
            for (int r = 0; r < 16; ++r) s1[r] = __builtin_amdgcn_exp2f(s1[r] - m);
            f32x16 o = SMFMAC(BF8(v0h), HI8(s0), f32x16{});
            o = SMFMAC(BF8(v1l), LO8(s1), o);
            o = SMFMAC(BF8(v1h), HI8(s1), o);
            store_o(o, gv * __builtin_amdgcn_rcpf(row_sum(o)), wrs, olane, (int)WS_O + qt * 2 * (int)O_BLK, head, hh);
            put_o(u32x4{~0u, ~0u, ~0u, ~0u}, u32x2{~0u, ~0u}, wrs, olane, (int)WS_O + (128 + qt * 2) * (int)O_BLK, head, hh);
        }
        WAIT_V(0);
        STAMP(1, 3);
        RAW_BAR();
        if (hw == 0 && lane == 0) __hip_atomic_store((gu64*)(ws + WS_FA) + qb * 32 + head, tag, RLX_AGENT);
#define WLOAD(ks) _Pragma("unroll") for (int j = 0; j < 5; ++j) glds16_dev(ws + wimg + (20 + (ks)) * 40960 + j * 1024, lane * 16, lds + (ks) * 5120 + j * 1024)
#pragma unroll 1
        for (int i = 0; i < 4; ++i) WLOAD(hw * 4 + i);
        {
            gu64* const w[1] = {(gu64*)(ws + WS_FA) + (omt >> 2) * 32 + r32};
            wait_flags(w, tag);
        }
        f32x4 acc[2][5];
        bf16x8 of[2][2], wf[2][5];
        const int osrc = (int)WS_O + (omt * 4 + (hw & 1) * 2) * (int)O_BLK + (hw >> 1) * 10240, wsrc = wimg + (hw >> 1) * 409600;
        auto ld = [&](int b, int ks) {
#pragma unroll
            for (int i = 0; i < 2; ++i) of[b][i] = BF8(LD16(lane * 16, osrc + i * (int)O_BLK + ks * 1024));
#pragma unroll
            for (int j = 0; j < 5; ++j) wf[b][j] = BF8(LD16(lane * 16, wsrc + ks * 40960 + j * 1024));
        };
        auto mm = [&](int b) {
#pragma unroll
            for (int j = 0; j < 5; ++j) {
#pragma unroll
                for (int i = 0; i < 2; ++i) acc[i][j] = MFMA16(wf[b][j], of[b][i], acc[i][j]);
                if (THIN) { SB(); __builtin_amdgcn_s_sleep(1); SB(); }
            }
        };
#pragma unroll
        for (int i = 0; i < 2; ++i)
#pragma unroll
            for (int j = 0; j < 5; ++j) acc[i][j] = f32x4{0.f, 0.f, 0.f, 0.f};
        ld(0, 0);
#pragma unroll 1
        for (int ks = 0; ks < (ABL & 2 ? 0 : 10); ks += 2) {
            ld(1, ks + 1);
            mm(0);
            ld(0, ks < 8 ? ks + 2 : 0);
            mm(1);
        }
        STAMP(1, 4);
        RAW_BAR();
        WLOAD(16 + hw);
        WAIT_V(0);
        STAMP(1, 5);
        auto put = [&]() {
#pragma unroll
            for (int i = 0; i < 2; ++i)
#pragma unroll
                for (int j = 0; j < 5; ++j)
                    *(LDS_AS f32x4*)(lds + P_OFF + (1 + (hw >> 1)) * 20480 + (((hw & 1) * 2 + i) * 5 + j) * 1024 + pslot + ((r16 + 2 * j + (g >> 1)) & 15) * 16) = acc[i][j];
        };
        RAW_BAR();
        if (hw >= 2) put();
        RAW_BAR();
        if (hw < 2) put();
    } else {
        const int qt = qb * 8 + wid;
        if (late) publish();
        RAW_BAR();
        u32x4 qa[2];
        u32x2 qb2[2] = {{0u, 0u}, {0u, 0u}};
#pragma unroll
        for (int i = 0; i < 2; ++i) {
            const int qB = (int)WS_Q + ((NH + head) * 64 + (qt ^ (4 * i))) * (int)Q_TILE;
            qa[i] = LD16(lane * 16, qB);
            if (!hh) qb2[i] = LD8(r32 * 8, qB + 1024);
        }
        WAIT_V(0);
        RAW_BAR();
        STAMP(1, 1);
        u32x4 qbs[2];
        {
            const u32x2 kb = *(const LDS_AS u32x2*)(lds + 1024 + r32 * 8);
            const bf16x8 ka = lds_b128(lds + lane * 16);
#pragma unroll
            for (int i = 0; i < 2; ++i) {
                f32x16 s = MFMA32(ka, BF8(qa[i]), f32x16{});
                s = MFMA32(BF8((u32x4{kb[0], kb[1], 0u, 0u})), BF8((u32x4{qb2[i][0], qb2[i][1], 0u, 0u})), s);
                qbs[i] = with_ref(qb2[i], xhalf_max(max16(s, -INFINITY)), hh);
            }
        }
        const unsigned long long vmask = 0x000fffff000fffffull;
        f32x16 o = {}, o2 = {}, s1 = {};
        float gate;
        u32x4 vlo = vfill, vhi = vfill;
        const unsigned lbase = (unsigned)(size_t)lds + (wid >> 2) * (int)KV_TILE;
        unsigned a_ka = lbase + lane * 16, a_kb = lbase + r32 * 8, a_v = lbase + voff;
        int nbar = 0x4B, it = 7;
        const int psw = PRIO_SW ? wid : 4;
        __builtin_amdgcn_s_setprio(1);
        asm volatile(
            "v_mov_b32 v30, %[one]\n\tv_mov_b32 v31, %[one]\n\tv_mov_b32 v40, %[sidx]\n\t"
            "ds_read_b128 v[24:27], v41\n\t"
            "ds_read_b64 v[28:29], v42 offset:1024\n\t"
            "s_waitcnt lgkmcnt(0)\n\t"
            QK0(48, 8) QK1(48, 8)
            "s_nop 7\n\t"
            "1:\n\t"
            PRIO(2, 3) PAIR(0, "") PAIR(1, "") PAIR(2, PRIO(0, 4)) PAIR(3, "")
            "v_add_u32 v41, 20480, v41\n\tv_add_u32 v42, 20480, v42\n\tv_add_u32 v43, 20480, v43\n\t"
            "s_bitcmp0_b32 %[nbar], 0\n\t"
            "s_cbranch_scc1 2f\n\t"
            "s_barrier\n\t"
            "2:\n\t"
            "s_lshr_b32 %[nbar], %[nbar], 1\n\t"
            "s_sub_i32 %[it], %[it], 1\n\t"
            "s_cmp_lg_u32 %[it], 0\n\t"
            "s_cbranch_scc1 1b\n\t"
            "global_load_dword v44, %[goff], %[gb] sc1\n\t"
            PRIO(2, 3) PAIR(0, "") PAIR(1, "") PAIR(2, PRIO(0, 4)) PAIR_LAST(3)
            "s_waitcnt vmcnt(0)"
            : "={v44}"(gate), "+{v[80:95]}"(o), "+{v[96:111]}"(o2), "+{v[64:79]}"(s1), "+{v[32:35]}"(vlo), "+{v[36:39]}"(vhi),
              "+{v41}"(a_ka), "+{v42}"(a_kb), "+{v43}"(a_v), [nbar] "+s"(nbar), [it] "+s"(it)
            : "{v[8:11]}"(qa[0]), "{v[12:15]}"(qbs[0]), "{v[16:19]}"(qa[1]), "{v[20:23]}"(qbs[1]), [vm] "s"(vmask), [one] "s"(ONE2),
              [sidx] "s"(0xDDDDDDDDu), [psw] "s"(psw), [goff] "v"(r32 * 4), [gb] "s"(ws + WS_G + qt * 128)
            : "v24", "v25", "v26", "v27", "v28", "v29", "v30", "v31", "v40", "v48", "v49", "v50", "v51", "v52", "v53", "v54", "v55",
              "v56", "v57", "v58", "v59", "v60", "v61", "v62", "v63", "scc", "memory");
        STAMP(1, 2);
        __builtin_amdgcn_s_setprio(3);
#pragma unroll
        for (int i = 0; i < 3; ++i)
            *(LDS_AS f32x4*)(lds + P_OFF + (wid ^ 4) * 3072 + i * 1024 + lane * 16) = f32x4{o2[4 * i], o2[4 * i + 1], o2[4 * i + 2], o2[4 * i + 3]};
        RAW_BAR();
#pragma unroll
        for (int i = 0; i < 3; ++i) {
            const f32x4 t = *(const LDS_AS f32x4*)(lds + P_OFF + wid * 3072 + i * 1024 + lane * 16);
#pragma unroll
            for (int e = 0; e < 4; ++e) o[4 * i + e] += t[e];
        }
        const float gB = 1.f - gate;
        float l = row_sum(o);
        float amax = hh ? 0.f : fmaxf(fmaxf(fabsf(o[8]), fabsf(o[9])), fmaxf(fabsf(o[10]), fabsf(o[11])));
#pragma unroll
        for (int i = 0; i < 8; ++i) amax = fmaxf(amax, fabsf(o[i]));
        if (EV == 0) if (FORCE_FALLBACK || !__all(l >= 0x1p-64f && l <= 0x1p64f && amax <= 0x1p126f)) {
            const int kv = (int)WS_KV + (NH + head) * (int)KV_HEAD;
            u32x4 qr = {qb2[0][0], qb2[0][1], 0u, 0u};
            float mt2 = -INFINITY;
            o = f32x16{};
#pragma unroll 1
            for (int t = 0; t < 128; ++t) {
                const int kt = kv + (t & 63) * (int)KV_TILE;
                const u32x2 kb = LD8(r32 * 8, kt + 1024);
                f32x16 sc = MFMA32(BF8(LD16(lane * 16, kt)), BF8(qa[0]), f32x16{});
                sc = MFMA32(BF8((u32x4{kb[0], kb[1], ONE2, ONE2})), BF8(qr), sc);
                if (t < 64) {
                    mt2 = max16(sc, mt2);
                    if (t == 63) qr = with_ref(qb2[0], xhalf_max(mt2), hh);
                } else {
                    u32x4 v0 = vfill, v1 = vfill;
                    if (r32 < 20) { v0 = LD16(voff, kt + 1280); v1 = LD16(voff, kt + 1920); }
#pragma unroll
                    for (int r = 0; r < 16; ++r) sc[r] = __builtin_amdgcn_exp2f(sc[r]);
                    o = SMFMAC(BF8(v0), LO8(sc), o);
                    o = SMFMAC(BF8(v1), HI8(sc), o);
                }
            }
            l = row_sum(o);
        }
        store_o(o, gB * __builtin_amdgcn_rcpf(l), wrs, olane, (int)WS_O + (128 + qt * 2) * (int)O_BLK, head, hh);
        STAMP(1, 3);
        RAW_BAR();
        const int mb = wid & 3, kh = wid >> 2;
        const int osrc = (int)WS_O + (128 + omt * 4 + mb) * (int)O_BLK + kh * 10240;
        f32x4 acc[5];
        lds_ptr pp = lds + P_OFF + kh * 40960 + mb * 5120 + pslot;
#pragma unroll
        for (int j = 0; j < 5; ++j) acc[j] = f32x4{0.f, 0.f, 0.f, 0.f};
        if (kh) {
#pragma unroll
            for (int j = 0; j < 5; ++j) acc[j] = *(const LDS_AS f32x4*)(pp + j * 1024 + ((r16 + 2 * j + (g >> 1)) & 15) * 16);
        }
        lds_ptr wp = lds + kh * 51200 + lane * 16;
        u32x4 of[10];
        unsigned have = 0;
        bool first = true;
        bf16x8 wf[20];
#pragma unroll
        for (int j = 0; j < 20; ++j) wf[j] = lds_b128(wp + j * 1024);
#pragma unroll 1
        for (unsigned spins = 0; spins < (1u << 20) && have != 0x3ffu; ++spins) {
            unsigned want = ~have & 0x3ffu;
            if (PROBE && __builtin_popcount(want) > 3) {
                const u32x4 a = LD16((lane < 40 ? lane : 0) * 256, osrc);
                u64 bad = __ballot(max(max(a[0], a[1]), max(a[2], a[3])) == ~0u);
                bad |= bad >> 1;
                bad |= bad >> 2;
#pragma unroll
                for (int s = 0; s < 10; ++s) want &= ~((unsigned)((bad >> (4 * s)) & 1) << s);
            }
#pragma unroll
            for (int s = 0; s < 10; ++s)
                if (want >> s & 1) of[s] = LD16(lane * 16, osrc + s * 1024);
#pragma unroll
            for (int s = 0; s < 10; ++s)
                if ((want >> s & 1) && !__any(max(max(of[s][0], of[s][1]), max(of[s][2], of[s][3])) == ~0u)) have |= 1u << s;
            if (first && (have & 0xfu) == 0xfu) {
#pragma unroll
                for (int j = 0; j < 20; ++j) {
                    acc[j % 5] = MFMA16(wf[j], BF8(of[j / 5]), acc[j % 5]);
                    wf[j] = lds_b128(wp + (20 + j) * 1024);
                }
                first = false;
            }
            if (spins == 0) STAMP(1, 4);
            asm volatile("" ::: "memory");
        }
        STAMP(1, 6);
#pragma unroll
        for (int j = 0; j < 30; ++j) {
            acc[j % 5] = MFMA16(wf[j % 20], BF8(of[4 + j / 5]), acc[j % 5]);
            if (j < 10) wf[j] = lds_b128(wp + (40 + j) * 1024);
        }
        STAMP(1, 5);
#pragma unroll
        for (int j = 0; j < 5; ++j) *(LDS_AS f32x4*)(pp + j * 1024 + ((r16 + 2 * j + (g >> 1)) & 15) * 16) = acc[j];
    }
    RAW_BAR();
    if (tid < 640) {
        const int row = tid / 10, c = tid - row * 10;
        lds_ptr q = lds + P_OFF + ((row >> 4) * 5 + (c >> 1)) * 1024 + (c & 1) * 512 + (((row & 15) + c) & 15) * 16;
        f32x4 a = {0.f, 0.f, 0.f, 0.f}, b = a;
#pragma unroll
        for (int p = 0; p < 3; ++p) {
            a += *(const LDS_AS f32x4*)(q + p * 20480);
            b += *(const LDS_AS f32x4*)(q + p * 20480 + 256);
        }
        u32x4 v = {pack2(a[0], a[1]), pack2(a[2], a[3]), pack2(b[0], b[1]), pack2(b[2], b[3])};
        *(u32x4*)(out + (size_t)(omt * 64 + row) * DM + ont * 80 + c * 8) = v;
    }
    DIAG_WAIT();
    STAMP(1, 7); FLUSH(1);
}

extern "C" void kernel_launch(void* const* d_in, const int* in_sizes, int n_in, void* d_out, int out_size, void* d_ws, size_t ws_size,
                              hipStream_t stream) {
    gated_mix_kernel<<<256, 768, 0, stream>>>((const u16*)d_in[0], (const u16*)d_in[1], (const u16*)d_in[4], (const u16*)d_in[2],
                                              (const u16*)d_in[5], (const u16*)d_in[6], (char*)d_ws, (u16*)d_out);
}
```

```cpp
#include <hip/hip_runtime.h>
#include <hip/hip_bf16.h>

typedef __attribute__((ext_vector_type(8))) __bf16 bf16x8;
typedef __attribute__((ext_vector_type(16))) __bf16 bf16x16;
typedef __attribute__((ext_vector_type(2))) __bf16 bf16x2;
typedef __attribute__((ext_vector_type(4))) short s16x4;
typedef __attribute__((ext_vector_type(8))) short s16x8;
typedef __attribute__((ext_vector_type(2))) float f32x2;
typedef __attribute__((ext_vector_type(4))) float f32x4;
typedef __attribute__((ext_vector_type(8))) float f32x8;
typedef __attribute__((ext_vector_type(16))) float f32x16;
typedef __attribute__((ext_vector_type(2))) unsigned u32x2;
typedef __attribute__((ext_vector_type(4))) unsigned u32x4;
typedef unsigned short u16;
#define LDS_AS __attribute__((address_space(3)))
typedef LDS_AS char* lds_ptr;

constexpr int SEQ = 2048, DM = 640, NH = 32, DH = 20;
constexpr size_t KV_TILE = 2560, KV_HEAD = 64 * KV_TILE;
constexpr size_t Q_TILE = 1280, Q_HEAD = 64 * Q_TILE;
constexpr size_t WS_KV = 0;
constexpr size_t WS_Q = WS_KV + 2 * NH * KV_HEAD;
constexpr size_t O_BLK = 80 * 256;
constexpr size_t WS_O = WS_Q + 2 * NH * Q_HEAD;
constexpr size_t WS_WO = WS_O + 2 * 128 * O_BLK;
constexpr size_t WS_G = WS_WO + 40 * 40 * 1024;
constexpr size_t WS_F1 = WS_G + SEQ * 4;
constexpr int FS = 16;
constexpr size_t WS_FA = WS_F1 + 512 * 128;
constexpr size_t WS_EPOCH = WS_FA + 256 * 128;
constexpr size_t WS_END = WS_EPOCH + 128;
#define SPAN_BEGIN()
#define SPAN_END()
#define STAMP_DECL
#define STAMP(k, i)
#define FLUSH(k)
#define DIAG_WAIT()
constexpr float QSCALE = 0.22360679774997896f * 1.4426950408889634f;

#define K1_PF 3
#define PF_DONE() asm volatile("s_waitcnt vmcnt(0)" : "+v"(d0) :: "memory")
#define PRIO_SW 5
#define ABL 0
#define B_DELAY 8
#define G6_AT 4
#define POLL1 2
#define THIN 1
#define FORCE_FALLBACK 0
#define WAIT_V(n) asm volatile("s_waitcnt vmcnt(" #n ")" ::: "memory")
#define RAW_BAR() asm volatile("s_waitcnt lgkmcnt(0)\n\ts_barrier" ::: "memory")
#define MFMA16(a, b, c) __builtin_amdgcn_mfma_f32_16x16x32_bf16(a, b, c, 0, 0, 0)
#define MFMA32(a, b, c) __builtin_amdgcn_mfma_f32_32x32x16_bf16(a, b, c, 0, 0, 0)
#define MFMA32H(a, b, c) __builtin_amdgcn_mfma_f32_32x32x8bf16_1k(__builtin_bit_cast(s16x4, a), __builtin_bit_cast(s16x4, b), c, 0, 0, 0)

__device__ __forceinline__ unsigned pack2(float lo, float hi) {
    f32x2 v = {lo, hi};
    return __builtin_bit_cast(unsigned, __builtin_convertvector(v, bf16x2));
}
__device__ __forceinline__ void glds16(const char* sbase, unsigned voff, lds_ptr l) {
    unsigned keep;
    asm volatile("s_mov_b32 %0, m0\n\ts_mov_b32 m0, %3\n\ts_nop 0\n\tglobal_load_lds_dwordx4 %1, %2\n\ts_mov_b32 m0, %0"
                 : "=&s"(keep) : "v"(voff), "s"(sbase), "s"((unsigned)(size_t)l) : "memory");
}
__device__ __forceinline__ void glds16x3(const char* s0, unsigned v0, unsigned l0, const char* s1, const char* s2, unsigned v1, unsigned l1, unsigned l2) {
    unsigned keep;
    asm volatile("s_mov_b32 %0, m0\n\ts_mov_b32 m0, %3\n\ts_nop 0\n\tglobal_load_lds_dwordx4 %1, %2\n\t"
                 "s_mov_b32 m0, %7\n\ts_nop 0\n\tglobal_load_lds_dwordx4 %4, %5\n\t"
                 "s_mov_b32 m0, %8\n\ts_nop 0\n\tglobal_load_lds_dwordx4 %4, %6\n\ts_mov_b32 m0, %0"
                 : "=&s"(keep) : "v"(v0), "s"(s0), "s"(l0), "v"(v1), "s"(s1), "s"(s2), "s"(l1), "s"(l2) : "memory");
}
__device__ __forceinline__ bf16x8 lds_b128(lds_ptr p) { return *(const LDS_AS bf16x8*)p; }
__device__ __forceinline__ s16x4 lds_tr(lds_ptr p) { return __builtin_amdgcn_ds_read_tr16_b64_v4i16((LDS_AS s16x4*)p); }
__device__ __forceinline__ bf16x8 cat8(s16x4 a, s16x4 b) {
    return __builtin_bit_cast(bf16x8, __builtin_shufflevector(a, b, 0, 1, 2, 3, 4, 5, 6, 7));
}
typedef __amdgpu_buffer_rsrc_t rsrc_t;
typedef unsigned long long u64;
typedef __attribute__((address_space(1))) u64 gu64;
#define RLX_AGENT __ATOMIC_RELAXED, __HIP_MEMORY_SCOPE_AGENT
constexpr int SC1 = 16;
#define LD16(voff, soff) __builtin_amdgcn_raw_buffer_load_b128(wrs, voff, soff, SC1)
#define LD8(voff, soff) __builtin_amdgcn_raw_buffer_load_b64(wrs, voff, soff, SC1)
#define LD4(voff, soff) __builtin_amdgcn_raw_buffer_load_b32(wrs, voff, soff, SC1)
#define ST16(v, voff, soff) __builtin_amdgcn_raw_buffer_store_b128(v, wrs, (voff) + (soff), 0, SC1)
#define ST8(v, voff, soff) __builtin_amdgcn_raw_buffer_store_b64(v, wrs, voff, soff, SC1)
#define ST4(v, voff, soff) __builtin_amdgcn_raw_buffer_store_b32(v, wrs, voff, soff, SC1)
#define BF8(x) __builtin_bit_cast(bf16x8, x)
__device__ __forceinline__ void glds16_dev(const char* sbase, unsigned voff, lds_ptr l) {
    unsigned keep;
    asm volatile("s_mov_b32 %0, m0\n\ts_mov_b32 m0, %3\n\ts_nop 0\n\tglobal_load_lds_dwordx4 %1, %2 sc1\n\ts_mov_b32 m0, %0"
                 : "=&s"(keep) : "v"(voff), "s"(sbase), "s"((unsigned)(size_t)l) : "memory");
}
template <int SLEEP, int N>
__device__ __forceinline__ void wait_flags(gu64* const (&w)[N], u64 tag) {
#pragma unroll 1
    for (unsigned spins = 0; spins < (1u << 20); ++spins) {
        bool ok = true;
#pragma unroll
        for (int i = 0; i < N; ++i) ok &= __hip_atomic_load(w[i], RLX_AGENT) == tag;
        if (__all(ok)) break;
        __builtin_amdgcn_s_sleep(SLEEP);
    }
    __builtin_amdgcn_fence(__ATOMIC_ACQUIRE, "wavefront");
}
__device__ __forceinline__ u32x4 swap16(u32x2 a, u32x2 b) {
    auto x = __builtin_amdgcn_permlane16_swap(a[0], b[0], false, false);
    auto y = __builtin_amdgcn_permlane16_swap(a[1], b[1], false, false);
    return u32x4{x[0], y[0], x[1], y[1]};
}

constexpr int K1_WP = 1024 + 32;
constexpr int K1_STAGE = 16384 + 32 * K1_WP;
constexpr int L_Q = 112 * 1024, L_STASH = 122 * 1024, L_CNT = 163824;
#define SB() __builtin_amdgcn_sched_barrier(0)
__device__ __forceinline__ void put_qkv(const u32x4 (&pv)[2], unsigned off, int ps, rsrc_t wrs) {
    if (!(off & 1)) {
#pragma unroll
        for (int i = 0; i < 2; ++i) ST16(pv[i], off, i * ps);
    } else {
#pragma unroll
        for (int i = 0; i < 2; ++i) {
            ST8((u32x2{pv[i][0], pv[i][1]}), off - 1, i * ps);
            ST8((u32x2{pv[i][2], pv[i][3]}), off - 1, (i + 64) * ps);
        }
    }
}

template <bool VT>
__device__ __forceinline__ void k1_main(f32x4 (&acc)[4][4], lds_ptr lds, int xa, int wa, const char* tp, int tstep, unsigned& d0) {
#define TOUCH() asm volatile("global_load_dword %0, %1, off" : "+v"(d0) : "v"(tp) : "memory")
    bf16x8 xf[2][4], wf[2][4];
    auto rd = [&](int so, int kk) {
#pragma unroll
        for (int j = 0; j < 4; ++j) {
            lds_ptr p = lds + so + wa + kk * 512 + j * 32;
            wf[kk][j] = cat8(lds_tr(p), lds_tr(p + 256));
        }
#pragma unroll
        for (int i = 0; i < 4; ++i) xf[kk][i] = lds_b128(lds + so + (xa ^ (kk * 64)) + i * 2048);
    };
    auto mm = [&](int kk) {
#pragma unroll
        for (int i = 0; i < 4; ++i)
#pragma unroll
            for (int j = 0; j < 4; ++j)
                acc[i][j] = VT ? MFMA16(xf[kk][i], wf[kk][j], acc[i][j]) : MFMA16(wf[kk][j], xf[kk][i], acc[i][j]);
    };
#define ILV() do { _Pragma("unroll") for (int q_ = 0; q_ < 12; ++q_) { __builtin_amdgcn_sched_group_barrier(0x100, 1, 0); __builtin_amdgcn_sched_group_barrier(0x8, 1, 0); } __builtin_amdgcn_sched_group_barrier(0x8, 4, 0); SB(); } while (0)
    if (K1_PF && tstep < 0) TOUCH();
    RAW_BAR();
    if (K1_PF && tstep == 0) TOUCH();
    rd(0, 0); SB();
    rd(0, 1);
    mm(0); ILV();
    int so = K1_STAGE;
#pragma unroll 1
    for (int t = 1; t < 10; ++t) {
        RAW_BAR();
        if (K1_PF && tstep == t) TOUCH();
        rd(so, 0);
        mm(1); ILV();
        rd(so, 1);
        mm(0); ILV();
        so = so == 2 * K1_STAGE ? 0 : so + K1_STAGE;
    }
    mm(1);
}

#define SMFMAC(a, b8, c) __builtin_amdgcn_smfmac_f32_32x32x32_bf16(a, __builtin_bit_cast(bf16x16, b8), c, (int)0xDDDDDDDD, 0, 0)
#define LO8(s) __builtin_shufflevector(s, s, 0, 1, 2, 3, 4, 5, 6, 7)
#define HI8(s) __builtin_shufflevector(s, s, 8, 9, 10, 11, 12, 13, 14, 15)
constexpr unsigned ONE2 = 0x3F803F80u;

__device__ __forceinline__ float xhalf_max(float m) {
    auto r = __builtin_amdgcn_permlane32_swap(__float_as_uint(m), __float_as_uint(m), false, false);
    return fmaxf(__uint_as_float(r[0]), __uint_as_float(r[1]));
}
__device__ __forceinline__ float max3f(float a, float b, float c) { return __builtin_fmaxf(__builtin_fmaxf(a, b), c); }
__device__ __forceinline__ float max16(const f32x16& s, float m) {
#pragma unroll
    for (int r = 0; r < 16; r += 2) m = max3f(m, s[r], s[r + 1]);
    return m;
}
__device__ __forceinline__ float row_sum(const f32x16& o) {
    auto r = __builtin_amdgcn_permlane32_swap(__float_as_uint(o[8]), __float_as_uint(o[8]), false, false);
    return __uint_as_float(r[1]);
}
__device__ __forceinline__ u32x2 with_ref(u32x2 q, float m, int hh) {
    const unsigned h1 = __float_as_uint(-m) & 0xffff0000u;
    const float r1 = -m - __uint_as_float(h1);
    const unsigned h2 = __float_as_uint(r1) & 0xffff0000u;
    const unsigned h3 = __float_as_uint(r1 - __uint_as_float(h2)) >> 16;
    return hh ? u32x2{h1 | (h2 >> 16), h3} : q;
}
__device__ __forceinline__ void put_o(u32x4 v, u32x2 t, rsrc_t wrs, unsigned voff, int soff, int head, int hh) {
    const int c0 = (head >> 1) * 5;
    ST16(v, voff + hh * 256, soff + (c0 + (head & 1) * 2) * 256);
    if (!hh) ST8(t, voff, soff + (c0 + 4) * 256 + (head & 1) * 8);
}
__device__ __forceinline__ unsigned pack_o(float a, float b) { return min(pack2(a, b), 0xFFFFFFFEu); }
__device__ __forceinline__ void store_o(const f32x16& o, float f, rsrc_t wrs, unsigned voff, int soff, int head, int hh) {
    put_o(u32x4{pack_o(o[0] * f, o[1] * f), pack_o(o[2] * f, o[3] * f), pack_o(o[4] * f, o[5] * f), pack_o(o[6] * f, o[7] * f)},
          u32x2{pack_o(o[8] * f, o[9] * f), pack_o(o[10] * f, o[11] * f)}, wrs, voff, soff, head, hh);
}

#define EXP4(C, a) "v_exp_f32 v[" #C "+" #a "], v[" #C "+" #a "]\n\tv_exp_f32 v[" #C "+" #a "+1], v[" #C "+" #a "+1]\n\t"         \
                   "v_exp_f32 v[" #C "+" #a "+2], v[" #C "+" #a "+2]\n\tv_exp_f32 v[" #C "+" #a "+3], v[" #C "+" #a "+3]\n\t"
#define PV(O, V, P) "v_smfmac_f32_32x32x32_bf16 v[" #O ":" #O "+15], v[" #V ":" #V "+3], v[" #P ":" #P "+7], v40\n\t"
#define QK0(S, Q) "v_mfma_f32_32x32x16_bf16 v[" #S ":" #S "+15], v[24:27], v[" #Q ":" #Q "+3], 0\n\t"
#define QK1(S, Q, K) "v_mfma_f32_32x32x8bf16_1k v[" #S ":" #S "+15], v[" #K ":" #K "+1], v[" #Q "+4:" #Q "+5], v[" #S ":" #S "+15]\n\t"
#define VLOAD(T) "s_mov_b64 exec, %[vm]\n\tds_read_b128 v[32:35], v43 offset:" #T "*5120+1280\n\t"                                \
                 "ds_read_b128 v[36:39], v43 offset:" #T "*5120+1920\n\t"
#define KBLOAD(T, K) "s_mov_b64 exec, %[km]\n\tds_read_b64 v[" #K ":" #K "+1], v42 offset:(" #T ")*5120+1024\n\ts_mov_b64 exec, -1\n\t"
#define PAIR(T, KC, KN, MID)                                                                       \
    PV(96, 36, 72) VLOAD(T) KBLOAD(T + 1, KN) EXP4(48, 0)                                          \
    QK0(64, 16) "ds_read_b128 v[24:27], v41 offset:(" #T "+1)*5120\n\t" EXP4(48, 4)                \
    QK1(64, 16, KC) EXP4(48, 8)                                                                    \
    "s_waitcnt lgkmcnt(2)\n\t" PV(80, 32, 48) EXP4(48, 12) MID "s_nop 1\n\t"                       \
    PV(80, 36, 56) EXP4(64, 0)                                                                     \
    "s_waitcnt lgkmcnt(0)\n\t" QK0(48, 8) EXP4(64, 4)                                              \
    QK1(48, 8, KN) EXP4(64, 8)                                                                     \
    PV(96, 32, 64) EXP4(64, 12) "s_nop 1\n\t"
#define PAIR_LAST(T, KC)                                                                           \
    PV(96, 36, 72) VLOAD(T) "s_mov_b64 exec, -1\n\t" EXP4(48, 0)                                   \
    QK0(64, 16) EXP4(48, 4)                                                                        \
    QK1(64, 16, KC) EXP4(48, 8)                                                                    \
    "s_waitcnt lgkmcnt(0)\n\t" PV(80, 32, 48) EXP4(48, 12) "s_nop 1\n\t"                           \
    PV(80, 36, 56) EXP4(64, 0) EXP4(64, 4) EXP4(64, 8)                                             \
    PV(96, 32, 64) EXP4(64, 12) "s_nop 1\n\t"                                                      \
    PV(96, 36, 72) "s_nop 15\n\t"
#define PRIO(n, L) "s_cmp_ge_u32 %[psw], 4\n\ts_cbranch_scc1 " #L "f\n\ts_setprio " #n "\n\t" #L ":\n\t"

__global__ __launch_bounds__(768, 3) void gated_mix_kernel(const u16* __restrict__ x, const u16* __restrict__ wA, const u16* __restrict__ wB,
                                                            const u16* __restrict__ woA, const u16* __restrict__ woB,
                                                            const u16* __restrict__ gate_w, char* __restrict__ ws, u16* __restrict__ out) {
    __shared__ __attribute__((aligned(16))) char smem[KV_HEAD];
    lds_ptr lds = (lds_ptr)smem;
    const int tid = threadIdx.x, lane = tid & 63, wid = __builtin_amdgcn_readfirstlane(tid >> 6);
    const int g = lane >> 4, r16 = lane & 15;
    const rsrc_t wrs = __builtin_amdgcn_make_buffer_rsrc(ws, 0, (int)WS_END, 0x00020000);
    SPAN_BEGIN();
    STAMP_DECL STAMP(0, 0);
    const int xcd = blockIdx.x & 7, idx = blockIdx.x >> 3;
    const int mt = (xcd & 1) * 8 + (idx & 7), nt = (xcd >> 1) * 4 + (idx >> 3), m0 = mt * 128;
    if (wid == 8) *(LDS_AS u32x2*)(lds + L_CNT) = u32x2{0u, 0u};
    if (wid >= 8 && nt < 15) {
        const int lw = wid - 8;
        const unsigned xoff = (lw * 8 + (lane >> 3)) * (DM * 2) + (((lane & 7) ^ ((lane >> 3) & 7)) << 4);
        const unsigned woff = (32 * (g >> 1) + 4 * (g & 1) + lw) * 3840 + r16 * 16;
        const char* xs = (const char*)(x + (size_t)m0 * DM);
        const char* bs = (const char*)(wB + nt * 128);
        const char* as = (const char*)(wA + nt * 128);
        unsigned so = (unsigned)(size_t)lds;
#pragma unroll 1
        for (int t = -2; t < 9; ++t) {
            if (t >= 0) {
                WAIT_V(12);
                if (t == 0) STAMP(0, 1);
                RAW_BAR();
            }
            if (t < 8) {
#pragma unroll 1
                for (int i = 0; i < 4; ++i) {
                    const unsigned lx = so + (lw + 4 * i) * 1024, lb = so + 16384 + (lw + 4 * i) * K1_WP;
                    glds16x3(xs + i * (32 * DM * 2), xoff, lx, bs + i * (8 * 3840), as + i * (8 * 3840), woff, lb, lb + 16 * K1_WP);
                }
                xs += 128; bs += 64 * 3840; as += 64 * 3840;
            }
            so = so == (unsigned)(size_t)lds + 2 * K1_STAGE ? (unsigned)(size_t)lds : so + K1_STAGE;
        }
        WAIT_V(0);
        STAMP(0, 2);
        RAW_BAR();
    }
    const int head = (blockIdx.x & 7) * 4 + ((blockIdx.x >> 3) & 3), qb = blockIdx.x >> 5;
    const int omt = head, ont = qb;
    const int r32 = lane & 31, hh = lane >> 5;
    const u32x4 vfill = r32 == 20 ? u32x4{ONE2, ONE2, ONE2, ONE2} : u32x4{0u, 0u, 0u, 0u};
    const int voff = hh * 320 + (r32 < 20 ? r32 : 0) * 16;
    const unsigned olane = (r32 >> 4) * (int)O_BLK + (r32 & 15) * 16;
    const int ps = nt < 5 ? (int)Q_TILE : (int)KV_TILE;
    const int wimg = (int)WS_WO + ont * 5120;
    gu64* const f1 = (gu64*)(ws + WS_F1);
    constexpr int P_OFF = 102400;
    const int pslot = g * 256;
    if (nt == 15) {
        const int task = mt * 10 + wid, ks = task >> 2, cg = task & 3;
        lds_ptr st = lds + wid * 10240;
        if (wid < 10) {
            const char* wsrc = (const char*)((ks >= 20 ? woB : woA) + cg * 160);
#pragma unroll
            for (int i = 0; i < 10; ++i) {
                const int ci = i * 64 + lane, slot = ci / 20, cc = ci - slot * 20;
                const int c = (ks >= 20 ? ks - 20 : ks) * 4 + (slot >> 3), e = slot & 7, p = c / 5, r = c - p * 5;
                const int k = r < 4 ? (2 * p + (r >> 1)) * 20 + 4 * (r & 1) + (e & 3) + 8 * (e >> 2) : (2 * p + (e >> 2)) * 20 + 16 + (e & 3);
                glds16(wsrc, k * (DM * 2) + cc * 16, st + i * 1024);
            }
        }
        u32x4 gv[2];
        gv[0] = *(const u32x4*)(gate_w + lane * 8);
        gv[1] = lane < 16 ? *(const u32x4*)(gate_w + 512 + lane * 8) : u32x4{0u, 0u, 0u, 0u};
        float s[11];
#pragma unroll
        for (int u = 0; u < 11; ++u) {
            const int row = wid + 12 * u < 128 ? wid + 12 * u : wid;
            const u16* xr = x + (size_t)(m0 + row) * DM;
            const u32x4 xv[2] = {*(const u32x4*)(xr + lane * 8), *(const u32x4*)(xr + 512 + r16 * 8)};
            float a = 0.f;
#pragma unroll
            for (int c = 0; c < 2; ++c)
#pragma unroll
                for (int e = 0; e < 4; ++e) {
                    a = fmaf(__uint_as_float(xv[c][e] << 16), __uint_as_float(gv[c][e] << 16), a);
                    a = fmaf(__uint_as_float(xv[c][e] & 0xffff0000u), __uint_as_float(gv[c][e] & 0xffff0000u), a);
                }
            s[u] = a;
        }
#pragma unroll
        for (int o = 32; o; o >>= 1)
#pragma unroll
            for (int u = 0; u < 11; ++u) s[u] += __shfl_xor(s[u], o);
        float z = s[0];
#pragma unroll
        for (int u = 1; u < 11; ++u) z = lane == u ? s[u] : z;
        if (lane < 11 && wid + 12 * lane < 128) ST4(__float_as_uint(1.f / (1.f + __expf(-z))), (wid + 12 * lane) * 4, (int)WS_G + m0 * 4);
        WAIT_V(0);
        if (wid < 10) {
            lds_ptr rp = st + (8 * g + (r16 >> 2)) * 320 + (lane & 3) * 8;
#pragma unroll
            for (int j = 0; j < 10; ++j)
                ST16(__builtin_bit_cast(u32x4, cat8(lds_tr(rp + j * 32), lds_tr(rp + j * 32 + 4 * 320))), lane * 16, (int)WS_WO + (ks * 40 + cg * 10 + j) * 1024);
        }
        STAMP(0, 4);
    }
    if (wid >= 8) {
        u64 tag = __hip_atomic_load((gu64*)(ws + WS_EPOCH), RLX_AGENT) + 1;
        f32x16 c0, c1;
        {
            const unsigned long long msk = ~(0x15554ull << r32) >> (4 * hh);
            const unsigned mlo = (unsigned)msk, mhi = (unsigned)(msk >> 32);
#pragma unroll
            for (int r = 0; r < 16; ++r) {
                const int u = (r & 3) + 8 * (r >> 2);
                c0[r] = r < 8 ? 0.f : __uint_as_float(__builtin_amdgcn_sbfe(mlo, (u - 16) & 31, 1) & 0xff800000u);
                c1[r] = __uint_as_float(__builtin_amdgcn_sbfe(u < 16 ? mlo : mhi, (u + 16) & 31, 1) & 0xff800000u);
            }
        }
        asm volatile("" : "+v"(c0), "+v"(c1));
        const int hw = wid - 8;
        __builtin_amdgcn_s_setprio(3);
        STAMP(0, 3);
        RAW_BAR();
        STAMP(1, 0);
        const int fc = head * 20 + (g & 1) * 19;
        gu64* const fkv = f1 + (((g < 2 ? 640 : 1280) + fc) >> 7) * 16 * FS;
        auto go = [&](int n, gu64* w0) {
            volatile LDS_AS int* gw = (volatile LDS_AS int*)(lds + L_CNT + 4);
            if (hw == 3) {
                gu64* const w[1] = {w0};
                wait_flags<POLL1>(w, tag);
                *gw = n;
            } else {
#pragma unroll 1
                while (__builtin_amdgcn_readfirstlane(*gw) < n) __builtin_amdgcn_s_sleep(1);
                asm volatile("" ::: "memory");
            }
        };
        go(1, r16 < 3 ? fkv + r16 * FS : f1 + ((fc >> 7) * 16 + qb * 2 + (lane & 1)) * FS);
        STAMP(1, 1);
        const char* src = ws + WS_KV + (size_t)(NH + head) * KV_HEAD + hw * 1024;
#define LOADS(a, b) _Pragma("unroll 1") for (int i = a; i < b; ++i) glds16_dev(src + i * 4096, lane * 16, lds + hw * 1024 + i * 4096)
#pragma unroll 1
        for (int p = hw; p < 10; p += 4) glds16_dev(ws + WS_Q + (size_t)((NH + head) * 64 + qb * 8) * Q_TILE + p * 1024, lane * 16, lds + L_Q + p * 1024);
        LOADS(0, 7);
        WAIT_V(0); RAW_BAR();
        go(2, fkv + r16 * FS);
        LOADS(7, 28);
        WAIT_V(16); RAW_BAR();
        if (nt < 15) {
            lds_ptr sp = lds + L_STASH + hw * 9216 + lane * 16;
            const u32x4 po = *(const LDS_AS u32x4*)(sp + 8192);
#pragma unroll
            for (int j = 0; j < 4; ++j) {
                const u32x4 pv[2] = {*(const LDS_AS u32x4*)(sp + j * 2048), *(const LDS_AS u32x4*)(sp + j * 2048 + 1024)};
                put_qkv(pv, po[j], ps, wrs);
            }
        }
        WAIT_V(0);
        STAMP(1, 6);
        RAW_BAR();
        if (hw == 0 && lane == 0) __hip_atomic_store(f1 + (256 + nt * 16 + mt) * FS, tag, RLX_AGENT);
        LOADS(28, 40);
#pragma unroll
        for (int i = 0; i < 2; ++i)
            put_o(u32x4{~0u, ~0u, ~0u, ~0u}, u32x2{~0u, ~0u}, wrs, olane, (int)WS_O + (128 + (qb * 8 + hw * 2 + i) * 2) * (int)O_BLK, head, hh);
        {
            const int sec = r16 >> 2, fm = max(qb * 2 - 1 + (r16 & 3), 0);
            gu64* const w[1] = {f1 + (256 + (g < 2 && sec < 3 && (r16 & 3) < 3 ? ((sec * 640 + fc) >> 7) * 16 + fm : 240 + r16)) * FS};
            wait_flags<8>(w, tag);
        }
        STAMP(1, 2);
        if (!(ABL & 1)) {
            const int q0 = qb * 8 + hw * 2;
            const int qA = (int)WS_Q + (head * 64 + q0) * (int)Q_TILE, k1 = (int)WS_KV + (head * 64 + q0) * (int)KV_TILE;
            u32x4 aqa[2], ka[3], vf[5];
            u32x2 aqb[2] = {{0u, 0u}, {0u, 0u}}, kb[3];
            float gv[2];
#pragma unroll
            for (int i = 0; i < 3; ++i) {
                const int kt = k1 + (i == 0 ? (q0 ? -(int)KV_TILE : 0) : (i - 1) * (int)KV_TILE);
                ka[i] = LD16(lane * 16, kt);
                kb[i] = LD8(r32 * 8, kt + 1024);
            }
#pragma unroll
            for (int i = 0; i < 2; ++i) aqa[i] = LD16(lane * 16, qA + i * (int)Q_TILE);
            if (!hh) {
#pragma unroll
                for (int i = 0; i < 2; ++i) aqb[i] = LD8(r32 * 8, qA + i * (int)Q_TILE + 1024);
            }
#pragma unroll
            for (int i = 0; i < 5; ++i) vf[i] = vfill;
            if (r32 < 20) {
                vf[0] = LD16(voff, k1 + (q0 ? -(int)KV_TILE : 0) + 1920);
#pragma unroll
                for (int i = 1; i < 5; ++i) vf[i] = LD16(voff, k1 + ((i - 1) >> 1) * (int)KV_TILE + 1280 + ((i - 1) & 1) * 640);
            }
#pragma unroll
            for (int i = 0; i < 2; ++i) gv[i] = __uint_as_float(LD4(r32 * 4, (int)WS_G + (q0 + i) * 128));
#pragma unroll
            for (int i = 1; i >= 0; --i) {
                if (i == 0 && q0 == 0) {
#pragma unroll
                    for (int r = 8; r < 16; ++r) c0[r] = -INFINITY;
                }
                f32x16 s0 = MFMA32(BF8(ka[i]), BF8(aqa[i]), c0);
                f32x16 s1 = MFMA32(BF8(ka[i + 1]), BF8(aqa[i]), c1);
                s0 = MFMA32H(kb[i], aqb[i], s0);
                s1 = MFMA32H(kb[i + 1], aqb[i], s1);
                float m = max3f(s0[8], s0[9], s0[10]);
                m = max3f(m, s0[11], s0[12]); m = max3f(m, s0[13], s0[14]);
                m = xhalf_max(max16(s1, fmaxf(m, s0[15])));
                const u32x2 ref = with_ref(u32x2{0u, 0u}, m, hh);
                s0 = MFMA32H((u32x2{ONE2, ONE2}), ref, s0);
                s1 = MFMA32H((u32x2{ONE2, ONE2}), ref, s1);
#pragma unroll
                for (int r = 8; r < 16; ++r) s0[r] = __builtin_amdgcn_exp2f(s0[r]);
#pragma unroll
                for (int r = 0; r < 16; ++r) s1[r] = __builtin_amdgcn_exp2f(s1[r]);
                f32x16 o = SMFMAC(BF8(vf[2 * i]), HI8(s0), f32x16{});
                o = SMFMAC(BF8(vf[2 * i + 1]), LO8(s1), o);
                o = SMFMAC(BF8(vf[2 * i + 2]), HI8(s1), o);
                const float f = gv[i] * __builtin_amdgcn_rcpf(row_sum(o));
                put_o(u32x4{pack2(o[0] * f, o[1] * f), pack2(o[2] * f, o[3] * f), pack2(o[4] * f, o[5] * f), pack2(o[6] * f, o[7] * f)},
                      u32x2{pack2(o[8] * f, o[9] * f), pack2(o[10] * f, o[11] * f)}, wrs, olane, (int)WS_O + (q0 + i) * 2 * (int)O_BLK, head, hh);
            }
        }
        WAIT_V(0);
        STAMP(1, 3);
        RAW_BAR();
        if (hw == 0 && lane == 0) __hip_atomic_store((gu64*)(ws + WS_FA) + (qb * 32 + head) * FS, tag, RLX_AGENT);
#define WLOAD(ks) _Pragma("unroll") for (int j = 0; j < 5; ++j) glds16_dev(ws + wimg + (20 + (ks)) * 40960 + j * 1024, lane * 16, lds + (ks) * 5120 + j * 1024)
#pragma unroll 1
        for (int i = 0; i < 5; ++i) WLOAD(hw * 5 + i);
        {
            gu64* const w[1] = {(gu64*)(ws + WS_FA) + ((omt >> 2) * 32 + r32) * FS};
            wait_flags<8>(w, tag);
        }
        if (blockIdx.x == 0 && hw == 0) {
            gu64* const w[4] = {f1 + (256 + lane) * FS, f1 + (320 + lane) * FS, f1 + (384 + lane) * FS, f1 + (448 + lane) * FS};
            wait_flags<8>(w, tag);
            if (lane == 0) __hip_atomic_store((gu64*)(ws + WS_EPOCH), tag, RLX_AGENT);
        }
        f32x4 acc[2][5];
        bf16x8 of[2][2], wf[2][5];
        const int osrc = (int)WS_O + (omt * 4 + (hw & 1) * 2) * (int)O_BLK + (hw >> 1) * 10240, wsrc = wimg + (hw >> 1) * 409600;
        auto ld = [&](int b, int ks) {
#pragma unroll
            for (int i = 0; i < 2; ++i) of[b][i] = BF8(LD16(lane * 16, osrc + i * (int)O_BLK + ks * 1024));
#pragma unroll
            for (int j = 0; j < 5; ++j) wf[b][j] = BF8(LD16(lane * 16, wsrc + ks * 40960 + j * 1024));
        };
        auto mm = [&](int b) {
#pragma unroll
            for (int j = 0; j < 5; ++j) {
#pragma unroll
                for (int i = 0; i < 2; ++i) acc[i][j] = MFMA16(wf[b][j], of[b][i], acc[i][j]);
                if (THIN) { SB(); __builtin_amdgcn_s_sleep(1); SB(); }
            }
        };
#pragma unroll
        for (int i = 0; i < 2; ++i)
#pragma unroll
            for (int j = 0; j < 5; ++j) acc[i][j] = f32x4{0.f, 0.f, 0.f, 0.f};
        ld(0, 0);
#pragma unroll 1
        for (int ks = 0; ks < 10; ks += 2) {
            ld(1, ks + 1);
            mm(0);
            ld(0, ks < 8 ? ks + 2 : 0);
            mm(1);
            if (ks == G6_AT) {
                STAMP(1, 4);
                RAW_BAR();
            }
        }
        WAIT_V(0);
        STAMP(1, 5);
        auto put = [&]() {
#pragma unroll
            for (int i = 0; i < 2; ++i)
#pragma unroll
                for (int j = 0; j < 5; ++j)
                    *(LDS_AS f32x4*)(lds + P_OFF + (1 + (hw >> 1)) * 20480 + (((hw & 1) * 2 + i) * 5 + j) * 1024 + pslot + ((r16 + 2 * j + (g >> 1)) & 15) * 16) = acc[i][j];
        };
        RAW_BAR();
        if (hw >= 2) put();
        RAW_BAR();
        if (hw < 2) put();
    } else {
        if (nt < 15) {
            const u64 tv = __hip_atomic_load((gu64*)(ws + WS_EPOCH), RLX_AGENT) + 1;
            const char* tp;
            if (lane < 16) {
                tp = (const char*)((lane & 2 ? wA : wB) + nt * 128 + (lane & 1) * 64) + (size_t)((wid + 2) * 64 + mt * 4 + (lane >> 2)) * 3840;
            } else {
                const int r = nt * 9 + (lane < 25 ? lane - 16 : 0);
                tp = (const char*)(x + (size_t)(m0 + (r < 127 ? r : 127)) * DM) + (wid + 2) * 128;
            }
            unsigned d0 = 0;
            const int br = wid < 4, wm = (wid >> 1) & 1, wn = wid & 1;
            const int xa = (wm * 64 + r16) * 128 + ((g ^ (lane & 7)) << 4);
            const int wa = 16384 + ((wid >> 2) * 16 + 4 * g + (r16 >> 2)) * K1_WP + (wn * 64 + 4 * (lane & 3)) * 2;
            const int tile0 = mt * 4 + wm * 2, cw = nt * 128 + wn * 64;
            unsigned poff[4];
#pragma unroll
            for (int j = 0; j < 4; ++j) {
                if (nt < 10) {
                    const int cp = (cw - (nt < 5 ? 0 : 640)) / 8 + j * 2 + (g >> 1), p = cp / 5, r = cp - p * 5, tk = (g & 1) * 16 + r16;
                    const unsigned hoff = (int)(nt < 5 ? WS_Q : WS_KV) + ((br * NH + 2 * p) * 64 + tile0) * ps;
                    poff[j] = r == 2 ? hoff + 1024 + tk * 8 + 1 : hoff + (r > 2 ? 64 * ps + (r - 3) * 512 : r * 512) + tk * 16;
                } else {
                    const int c = cw - 1280 + j * 16 + r16, h = c / 20, d = c - h * 20;
                    poff[j] = (int)WS_KV + ((br * NH + h) * 64 + tile0) * (int)KV_TILE + 1280 + (g & 1) * 640 + (g >> 1) * 320 + d * 16;
                }
            }
            asm volatile("" : "+v"(poff[0]), "+v"(poff[1]), "+v"(poff[2]), "+v"(poff[3]));
            const u64 tag = (u64)__builtin_amdgcn_readfirstlane((unsigned)(tv >> 32)) << 32 | (unsigned)__builtin_amdgcn_readfirstlane((unsigned)tv);
            f32x4 acc[4][4];
#pragma unroll
            for (int i = 0; i < 4; ++i)
#pragma unroll
                for (int j = 0; j < 4; ++j) acc[i][j] = f32x4{0.f, 0.f, 0.f, 0.f};
            if (br) __builtin_amdgcn_s_setprio(1);
            if (nt < 10) k1_main<false>(acc, lds, xa, wa, tp, wid + 2 - K1_PF, d0); else k1_main<true>(acc, lds, xa, wa, tp, wid + 2 - K1_PF, d0);
            STAMP(0, 1);
            PF_DONE();
            const bool urgent = br && (nt < 5 || mt < 3);
            if (!urgent) RAW_BAR();
            if (br && !urgent) __builtin_amdgcn_s_sleep(B_DELAY);
            if (nt < 5) {
#pragma unroll
                for (int i = 0; i < 4; ++i)
#pragma unroll
                    for (int j = 0; j < 4; ++j) acc[i][j] *= QSCALE;
            }
#pragma unroll
            for (int j = 0; j < 4; ++j) {
                u32x4 pv[2];
#pragma unroll
                for (int i = 0; i < 2; ++i) {
                    const f32x4 a = acc[2 * i][j], b = acc[2 * i + 1][j];
                    pv[i] = swap16(u32x2{pack2(a[0], a[1]), pack2(a[2], a[3])}, u32x2{pack2(b[0], b[1]), pack2(b[2], b[3])});
                }
                if (br) {
                    put_qkv(pv, poff[j], ps, wrs);
                } else {
#pragma unroll
                    for (int i = 0; i < 2; ++i) *(LDS_AS u32x4*)(lds + L_STASH + (wid - 4) * 9216 + j * 2048 + i * 1024 + lane * 16) = pv[i];
                }
            }
            STAMP(0, 2);
            if (urgent) RAW_BAR();
            if (br) {
                WAIT_V(0);
                if (lane == 0 && __hip_atomic_fetch_add((LDS_AS int*)(lds + L_CNT), 1, __ATOMIC_RELAXED, __HIP_MEMORY_SCOPE_WORKGROUP) == 3)
                    __hip_atomic_store(f1 + (nt * 16 + mt) * FS, tag, RLX_AGENT);
            } else {
                *(LDS_AS u32x4*)(lds + L_STASH + (wid - 4) * 9216 + 8192 + lane * 16) = u32x4{poff[0], poff[1], poff[2], poff[3]};
            }
            STAMP(0, 3);
        } else {
            RAW_BAR();
            WAIT_V(0);
        }
        const int qt = qb * 8 + wid;
        RAW_BAR();
        u32x4 qa[2];
        u32x2 qb2[2] = {{0u, 0u}, {0u, 0u}};
#pragma unroll
        for (int i = 0; i < 2; ++i) {
            lds_ptr qp = lds + L_Q + (wid ^ (4 * i)) * (int)Q_TILE;
            qa[i] = *(const LDS_AS u32x4*)(qp + lane * 16);
            if (!hh) qb2[i] = *(const LDS_AS u32x2*)(qp + 1024 + r32 * 8);
        }
        STAMP(1, 1);
        u32x2 qbs[2];
        {
            const u32x2 kb = *(const LDS_AS u32x2*)(lds + 1024 + r32 * 8);
            const bf16x8 ka = lds_b128(lds + lane * 16);
#pragma unroll
            for (int i = 0; i < 2; ++i) {
                f32x16 s = MFMA32(ka, BF8(qa[i]), f32x16{});
                s = MFMA32H(kb, qb2[i], s);
                qbs[i] = with_ref(qb2[i], xhalf_max(max16(s, -INFINITY)), hh);
            }
        }
        const unsigned long long vmask = 0x000fffff000fffffull;
        f32x16 o = {}, o2 = {}, s1 = {};
        float gate;
        u32x4 vlo = vfill, vhi = vfill;
        const unsigned lbase = (unsigned)(size_t)lds + (wid >> 2) * (int)KV_TILE;
        unsigned a_ka = lbase + lane * 16, a_kb = lbase + r32 * 8, a_v = lbase + voff;
        int nbar = 0x53, it = 7;
        const int psw = PRIO_SW ? wid : 4;
        __builtin_amdgcn_s_setprio(1);
        asm volatile(
            "v_mov_b32 v28, %[one]\n\tv_mov_b32 v29, %[one]\n\tv_mov_b32 v30, %[one]\n\tv_mov_b32 v31, %[one]\n\tv_mov_b32 v40, %[sidx]\n\t"
            "ds_read_b128 v[24:27], v41\n\t"
            KBLOAD(0, 28)
            "s_waitcnt lgkmcnt(0)\n\t"
            QK0(48, 8) QK1(48, 8, 28)
            "s_nop 7\n\t"
            "1:\n\t"
            PRIO(2, 3) PAIR(0, 28, 30, "") PAIR(1, 30, 28, "") PAIR(2, 28, 30, PRIO(0, 4)) PAIR(3, 30, 28, "")
            "v_add_u32 v41, 20480, v41\n\tv_add_u32 v42, 20480, v42\n\tv_add_u32 v43, 20480, v43\n\t"
            "s_bitcmp0_b32 %[nbar], 0\n\t"
            "s_cbranch_scc1 2f\n\t"
            "s_barrier\n\t"
            "2:\n\t"
            "s_lshr_b32 %[nbar], %[nbar], 1\n\t"
            "s_sub_i32 %[it], %[it], 1\n\t"
            "s_cmp_lg_u32 %[it], 0\n\t"
            "s_cbranch_scc1 1b\n\t"
            "global_load_dword v44, %[goff], %[gb] sc1\n\t"
            PRIO(2, 3) PAIR(0, 28, 30, "") PAIR(1, 30, 28, "") PAIR(2, 28, 30, PRIO(0, 4)) PAIR_LAST(3, 30)
            "s_waitcnt vmcnt(0)"
            : "={v44}"(gate), "+{v[80:95]}"(o), "+{v[96:111]}"(o2), "+{v[64:79]}"(s1), "+{v[32:35]}"(vlo), "+{v[36:39]}"(vhi),
              "+{v41}"(a_ka), "+{v42}"(a_kb), "+{v43}"(a_v), [nbar] "+s"(nbar), [it] "+s"(it)
            : "{v[8:11]}"(qa[0]), "{v[12:13]}"(qbs[0]), "{v[16:19]}"(qa[1]), "{v[20:21]}"(qbs[1]), [vm] "s"(vmask), [km] "s"(0xffffffffull), [one] "s"(ONE2),
              [sidx] "s"(0xDDDDDDDDu), [psw] "s"(psw), [goff] "v"(r32 * 4), [gb] "s"(ws + WS_G + qt * 128)
            : "v24", "v25", "v26", "v27", "v28", "v29", "v30", "v31", "v40", "v48", "v49", "v50", "v51", "v52", "v53", "v54", "v55",
              "v56", "v57", "v58", "v59", "v60", "v61", "v62", "v63", "scc", "memory");
        STAMP(1, 2);
        __builtin_amdgcn_s_setprio(3);
#pragma unroll
        for (int i = 0; i < 3; ++i)
            *(LDS_AS f32x4*)(lds + P_OFF + (wid ^ 4) * 3072 + i * 1024 + lane * 16) = f32x4{o2[4 * i], o2[4 * i + 1], o2[4 * i + 2], o2[4 * i + 3]};
        RAW_BAR();
#pragma unroll
        for (int i = 0; i < 3; ++i) {
            const f32x4 t = *(const LDS_AS f32x4*)(lds + P_OFF + wid * 3072 + i * 1024 + lane * 16);
#pragma unroll
            for (int e = 0; e < 4; ++e) o[4 * i + e] += t[e];
        }
        const float gB = 1.f - gate;
        float l = row_sum(o);
        float amax = hh ? 0.f : fmaxf(fmaxf(fabsf(o[8]), fabsf(o[9])), fmaxf(fabsf(o[10]), fabsf(o[11])));
#pragma unroll
        for (int i = 0; i < 8; ++i) amax = fmaxf(amax, fabsf(o[i]));
        if (FORCE_FALLBACK || !__all(l >= 0x1p-64f && l <= 0x1p64f && amax <= 0x1p126f)) {
            const int kv = (int)WS_KV + (NH + head) * (int)KV_HEAD;
            u32x2 qr = qb2[0];
            float mt2 = -INFINITY;
            o = f32x16{};
#pragma unroll 1
            for (int t = 0; t < 128; ++t) {
                const int kt = kv + (t & 63) * (int)KV_TILE;
                u32x2 kb = {ONE2, ONE2};
                if (!hh) kb = LD8(r32 * 8, kt + 1024);
                f32x16 sc = MFMA32(BF8(LD16(lane * 16, kt)), BF8(qa[0]), f32x16{});
                sc = MFMA32H(kb, qr, sc);
                if (t < 64) {
                    mt2 = max16(sc, mt2);
                    if (t == 63) qr = with_ref(qb2[0], xhalf_max(mt2), hh);
                } else {
                    u32x4 v0 = vfill, v1 = vfill;
                    if (r32 < 20) { v0 = LD16(voff, kt + 1280); v1 = LD16(voff, kt + 1920); }
#pragma unroll
                    for (int r = 0; r < 16; ++r) sc[r] = __builtin_amdgcn_exp2f(sc[r]);
                    o = SMFMAC(BF8(v0), LO8(sc), o);
                    o = SMFMAC(BF8(v1), HI8(sc), o);
                }
            }
            l = row_sum(o);
        }
        store_o(o, gB * __builtin_amdgcn_rcpf(l), wrs, olane, (int)WS_O + (128 + qt * 2) * (int)O_BLK, head, hh);
        STAMP(1, 3);
        RAW_BAR();
        const int mb = wid & 3, kh = wid >> 2;
        const int osrc = (int)WS_O + (128 + omt * 4 + mb) * (int)O_BLK + kh * 10240;
        f32x4 acc[5];
        lds_ptr pp = lds + P_OFF + kh * 40960 + mb * 5120 + pslot;
#pragma unroll
        for (int j = 0; j < 5; ++j) acc[j] = f32x4{0.f, 0.f, 0.f, 0.f};
        if (kh) {
#pragma unroll
            for (int j = 0; j < 5; ++j) acc[j] = *(const LDS_AS f32x4*)(pp + j * 1024 + ((r16 + 2 * j + (g >> 1)) & 15) * 16);
        }
        lds_ptr wp = lds + kh * 51200 + lane * 16;
        u32x4 of[10];
        unsigned have = 0;
        bool first = true;
        bf16x8 wf[20];
#pragma unroll
        for (int j = 0; j < 20; ++j) wf[j] = lds_b128(wp + j * 1024);
#pragma unroll 1
        for (unsigned spins = 0; spins < (1u << 20) && have != 0x3ffu; ++spins) {
            const unsigned want = ~have & 0x3ffu;
#pragma unroll
            for (int s = 0; s < 10; ++s)
                if (want >> s & 1) of[s] = LD16(lane * 16, osrc + s * 1024);
#pragma unroll
            for (int s = 0; s < 10; ++s)
                if ((want >> s & 1) && !__any(max(max(of[s][0], of[s][1]), max(of[s][2], of[s][3])) == ~0u)) have |= 1u << s;
            if (first && (have & 0xfu) == 0xfu) {
#pragma unroll
                for (int j = 0; j < 20; ++j) {
                    acc[j % 5] = MFMA16(wf[j], BF8(of[j / 5]), acc[j % 5]);
                    wf[j] = lds_b128(wp + (20 + j) * 1024);
                }
                first = false;
            }
            if (spins == 0) STAMP(1, 4);
            asm volatile("" ::: "memory");
        }
        STAMP(1, 6);
#pragma unroll
        for (int j = 0; j < 30; ++j) {
            acc[j % 5] = MFMA16(wf[j % 20], BF8(of[4 + j / 5]), acc[j % 5]);
            if (j < 10) wf[j] = lds_b128(wp + (40 + j) * 1024);
        }
        STAMP(1, 5);
#pragma unroll
        for (int j = 0; j < 5; ++j) *(LDS_AS f32x4*)(pp + j * 1024 + ((r16 + 2 * j + (g >> 1)) & 15) * 16) = acc[j];
    }
    RAW_BAR();
    if (tid < 640) {
        const int row = tid / 10, c = tid - row * 10;
        lds_ptr q = lds + P_OFF + ((row >> 4) * 5 + (c >> 1)) * 1024 + (c & 1) * 512 + (((row & 15) + c) & 15) * 16;
        f32x4 a = {0.f, 0.f, 0.f, 0.f}, b = a;
#pragma unroll
        for (int p = 0; p < 3; ++p) {
            a += *(const LDS_AS f32x4*)(q + p * 20480);
            b += *(const LDS_AS f32x4*)(q + p * 20480 + 256);
        }
        u32x4 v = {pack2(a[0], a[1]), pack2(a[2], a[3]), pack2(b[0], b[1]), pack2(b[2], b[3])};
        *(u32x4*)(out + (size_t)(omt * 64 + row) * DM + ont * 80 + c * 8) = v;
    }
    SPAN_END();
    DIAG_WAIT();
    STAMP(1, 7); FLUSH(1);
}

extern "C" void kernel_launch(void* const* d_in, const int* in_sizes, int n_in, void* d_out, int out_size, void* d_ws, size_t ws_size,
                              hipStream_t stream) {
    gated_mix_kernel<<<256, 768, 0, stream>>>((const u16*)d_in[0], (const u16*)d_in[1], (const u16*)d_in[4], (const u16*)d_in[2],
                                              (const u16*)d_in[5], (const u16*)d_in[6], (char*)d_ws, (u16*)d_out);
}
```

```cpp
#include <hip/hip_runtime.h>
#include <hip/hip_bf16.h>

typedef __attribute__((ext_vector_type(8))) __bf16 bf16x8;
typedef __attribute__((ext_vector_type(16))) __bf16 bf16x16;
typedef __attribute__((ext_vector_type(2))) __bf16 bf16x2;
typedef __attribute__((ext_vector_type(4))) short s16x4;
typedef __attribute__((ext_vector_type(8))) short s16x8;
typedef __attribute__((ext_vector_type(2))) float f32x2;
typedef __attribute__((ext_vector_type(4))) float f32x4;
typedef __attribute__((ext_vector_type(8))) float f32x8;
typedef __attribute__((ext_vector_type(16))) float f32x16;
typedef __attribute__((ext_vector_type(2))) unsigned u32x2;
typedef __attribute__((ext_vector_type(4))) unsigned u32x4;
typedef unsigned short u16;
#define LDS_AS __attribute__((address_space(3)))
typedef LDS_AS char* lds_ptr;

constexpr int SEQ = 2048, DM = 640, NH = 32, DH = 20;
constexpr size_t KV_TILE = 2560, KV_HEAD = 64 * KV_TILE;
constexpr size_t Q_TILE = 1280, Q_HEAD = 64 * Q_TILE;
constexpr size_t WS_KV = 0;
constexpr size_t WS_Q = WS_KV + 2 * NH * KV_HEAD;
constexpr size_t O_BLK = 80 * 256;
constexpr size_t WS_O = WS_Q + 2 * NH * Q_HEAD;
constexpr size_t WS_WO = WS_O + 2 * 128 * O_BLK;
constexpr size_t WS_G = WS_WO + 40 * 40 * 1024;
constexpr size_t WS_F1 = WS_G + SEQ * 4;
constexpr int FS = 16;
constexpr size_t WS_FA = WS_F1 + 512 * 128;
constexpr size_t WS_EPOCH = WS_FA + 256 * 128;
constexpr size_t WS_END = WS_EPOCH + 128;
#define SPAN_BEGIN()
#define SPAN_END()
#define STAMP_DECL
#define STAMP(k, i)
#define FLUSH(k)
#define DIAG_WAIT()
constexpr float QSCALE = 0.22360679774997896f * 1.4426950408889634f;

#define K1_PF 3
#define PF_DONE() asm volatile("s_waitcnt vmcnt(0)" : "+v"(d0) :: "memory")
#define PRIO_SW 5
#define ABL 0
#define B_DELAY 8
#define G6_AT 9
#define POLL1 2
#define W_AT 9
#define THIN 1
#define FORCE_FALLBACK 0
#define WAIT_V(n) asm volatile("s_waitcnt vmcnt(" #n ")" ::: "memory")
#define RAW_BAR() asm volatile("s_waitcnt lgkmcnt(0)\n\ts_barrier" ::: "memory")
#define MFMA16(a, b, c) __builtin_amdgcn_mfma_f32_16x16x32_bf16(a, b, c, 0, 0, 0)
#define MFMA32(a, b, c) __builtin_amdgcn_mfma_f32_32x32x16_bf16(a, b, c, 0, 0, 0)
#define MFMA32H(a, b, c) __builtin_amdgcn_mfma_f32_32x32x8bf16_1k(__builtin_bit_cast(s16x4, a), __builtin_bit_cast(s16x4, b), c, 0, 0, 0)

__device__ __forceinline__ unsigned pack2(float lo, float hi) {
    f32x2 v = {lo, hi};
    return __builtin_bit_cast(unsigned, __builtin_convertvector(v, bf16x2));
}
__device__ __forceinline__ void glds16(const char* sbase, unsigned voff, lds_ptr l) {
    unsigned keep;
    asm volatile("s_mov_b32 %0, m0\n\ts_mov_b32 m0, %3\n\ts_nop 0\n\tglobal_load_lds_dwordx4 %1, %2\n\ts_mov_b32 m0, %0"
                 : "=&s"(keep) : "v"(voff), "s"(sbase), "s"((unsigned)(size_t)l) : "memory");
}
__device__ __forceinline__ void glds16x3(const char* s0, unsigned v0, unsigned l0, const char* s1, const char* s2, unsigned v1, unsigned l1, unsigned l2) {
    unsigned keep;
    asm volatile("s_mov_b32 %0, m0\n\ts_mov_b32 m0, %3\n\ts_nop 0\n\tglobal_load_lds_dwordx4 %1, %2\n\t"
                 "s_mov_b32 m0, %7\n\ts_nop 0\n\tglobal_load_lds_dwordx4 %4, %5\n\t"
                 "s_mov_b32 m0, %8\n\ts_nop 0\n\tglobal_load_lds_dwordx4 %4, %6\n\ts_mov_b32 m0, %0"
                 : "=&s"(keep) : "v"(v0), "s"(s0), "s"(l0), "v"(v1), "s"(s1), "s"(s2), "s"(l1), "s"(l2) : "memory");
}
__device__ __forceinline__ bf16x8 lds_b128(lds_ptr p) { return *(const LDS_AS bf16x8*)p; }
__device__ __forceinline__ s16x4 lds_tr(lds_ptr p) { return __builtin_amdgcn_ds_read_tr16_b64_v4i16((LDS_AS s16x4*)p); }
__device__ __forceinline__ bf16x8 cat8(s16x4 a, s16x4 b) {
    return __builtin_bit_cast(bf16x8, __builtin_shufflevector(a, b, 0, 1, 2, 3, 4, 5, 6, 7));
}
typedef __amdgpu_buffer_rsrc_t rsrc_t;
typedef unsigned long long u64;
typedef __attribute__((address_space(1))) u64 gu64;
#define RLX_AGENT __ATOMIC_RELAXED, __HIP_MEMORY_SCOPE_AGENT
constexpr int SC1 = 16;
#define LD16(voff, soff) __builtin_amdgcn_raw_buffer_load_b128(wrs, voff, soff, SC1)
#define LD8(voff, soff) __builtin_amdgcn_raw_buffer_load_b64(wrs, voff, soff, SC1)
#define LD4(voff, soff) __builtin_amdgcn_raw_buffer_load_b32(wrs, voff, soff, SC1)
#define ST16(v, voff, soff) __builtin_amdgcn_raw_buffer_store_b128(v, wrs, (voff) + (soff), 0, SC1)
#define ST8(v, voff, soff) __builtin_amdgcn_raw_buffer_store_b64(v, wrs, voff, soff, SC1)
#define ST4(v, voff, soff) __builtin_amdgcn_raw_buffer_store_b32(v, wrs, voff, soff, SC1)
#define BF8(x) __builtin_bit_cast(bf16x8, x)
__device__ __forceinline__ void glds16_dev(const char* sbase, unsigned voff, lds_ptr l) {
    unsigned keep;
    asm volatile("s_mov_b32 %0, m0\n\ts_mov_b32 m0, %3\n\ts_nop 0\n\tglobal_load_lds_dwordx4 %1, %2 sc1\n\ts_mov_b32 m0, %0"
                 : "=&s"(keep) : "v"(voff), "s"(sbase), "s"((unsigned)(size_t)l) : "memory");
}
template <int SLEEP, int N>
__device__ __forceinline__ void wait_flags(gu64* const (&w)[N], u64 tag) {
#pragma unroll 1
    for (unsigned spins = 0; spins < (1u << 20); ++spins) {
        bool ok = true;
#pragma unroll
        for (int i = 0; i < N; ++i) ok &= __hip_atomic_load(w[i], RLX_AGENT) == tag;
        if (__all(ok)) break;
        __builtin_amdgcn_s_sleep(SLEEP);
    }
    __builtin_amdgcn_fence(__ATOMIC_ACQUIRE, "wavefront");
}
constexpr int K1_WP = 1024 + 32;
constexpr int K1_STAGE = 16384 + 32 * K1_WP;
constexpr int L_Q = 112 * 1024, L_STASH = 122 * 1024, L_CNT = 163824;
#define SB() __builtin_amdgcn_sched_barrier(0)
__device__ __forceinline__ void put_qkv(const u32x4 (&pv)[2], unsigned off0, unsigned off1, int ps, rsrc_t wrs) {
#pragma unroll
    for (int i = 0; i < 2; ++i) {
        ST8((u32x2{pv[i][0], pv[i][1]}), off0, i * ps);
        ST8((u32x2{pv[i][2], pv[i][3]}), off1, i * ps);
    }
}

template <bool VT>
__device__ __forceinline__ void k1_main(f32x4 (&acc)[4][4], lds_ptr lds, int xa, int wa, const char* tp, int tstep, unsigned& d0) {
#define TOUCH() asm volatile("global_load_dword %0, %1, off" : "+v"(d0) : "v"(tp) : "memory")
    bf16x8 xf[2][4], wf[2][4];
    auto rd = [&](int so, int kk) {
#pragma unroll
        for (int j = 0; j < 4; ++j) {
            lds_ptr p = lds + so + wa + kk * 512 + j * 32;
            wf[kk][j] = cat8(lds_tr(p), lds_tr(p + 256));
        }
#pragma unroll
        for (int i = 0; i < 4; ++i) xf[kk][i] = lds_b128(lds + so + (xa ^ (kk * 64)) + i * 2048);
    };
    auto mm = [&](int kk) {
#pragma unroll
        for (int i = 0; i < 4; ++i)
#pragma unroll
            for (int j = 0; j < 4; ++j)
                acc[i][j] = VT ? MFMA16(xf[kk][i], wf[kk][j], acc[i][j]) : MFMA16(wf[kk][j], xf[kk][i], acc[i][j]);
    };
#define ILV() do { _Pragma("unroll") for (int q_ = 0; q_ < 12; ++q_) { __builtin_amdgcn_sched_group_barrier(0x100, 1, 0); __builtin_amdgcn_sched_group_barrier(0x8, 1, 0); } __builtin_amdgcn_sched_group_barrier(0x8, 4, 0); SB(); } while (0)
    if (K1_PF && tstep < 0) TOUCH();
    RAW_BAR();
    if (K1_PF && tstep == 0) TOUCH();
    rd(0, 0); SB();
    rd(0, 1);
    mm(0); ILV();
    int so = K1_STAGE;
#pragma unroll 1
    for (int t = 1; t < 10; ++t) {
        RAW_BAR();
        if (K1_PF && tstep == t) TOUCH();
        rd(so, 0);
        mm(1); ILV();
        rd(so, 1);
        mm(0); ILV();
        so = so == 2 * K1_STAGE ? 0 : so + K1_STAGE;
    }
    mm(1);
}

#define SMFMAC(a, b8, c) __builtin_amdgcn_smfmac_f32_32x32x32_bf16(a, __builtin_bit_cast(bf16x16, b8), c, (int)0xDDDDDDDD, 0, 0)
#define LO8(s) __builtin_shufflevector(s, s, 0, 1, 2, 3, 4, 5, 6, 7)
#define HI8(s) __builtin_shufflevector(s, s, 8, 9, 10, 11, 12, 13, 14, 15)
constexpr unsigned ONE2 = 0x3F803F80u;

__device__ __forceinline__ float xhalf_max(float m) {
    auto r = __builtin_amdgcn_permlane32_swap(__float_as_uint(m), __float_as_uint(m), false, false);
    return fmaxf(__uint_as_float(r[0]), __uint_as_float(r[1]));
}
__device__ __forceinline__ float max3f(float a, float b, float c) { return __builtin_fmaxf(__builtin_fmaxf(a, b), c); }
__device__ __forceinline__ float max16(const f32x16& s, float m) {
#pragma unroll
    for (int r = 0; r < 16; r += 2) m = max3f(m, s[r], s[r + 1]);
    return m;
}
__device__ __forceinline__ float row_sum(const f32x16& o) {
    auto r = __builtin_amdgcn_permlane32_swap(__float_as_uint(o[8]), __float_as_uint(o[8]), false, false);
    return __uint_as_float(r[1]);
}
__device__ __forceinline__ u32x2 with_ref(u32x2 q, float m, int hh) {
    const unsigned h1 = __float_as_uint(-m) & 0xffff0000u;
    const float r1 = -m - __uint_as_float(h1);
    const unsigned h2 = __float_as_uint(r1) & 0xffff0000u;
    const unsigned h3 = __float_as_uint(r1 - __uint_as_float(h2)) >> 16;
    return hh ? u32x2{h1 | (h2 >> 16), h3} : q;
}
__device__ __forceinline__ void put_o(u32x4 v, u32x2 t, rsrc_t wrs, unsigned voff, int soff, int head, int hh) {
    const int c0 = (head >> 1) * 5;
    ST16(v, voff + hh * 256, soff + (c0 + (head & 1) * 2) * 256);
    if (!hh) ST8(t, voff, soff + (c0 + 4) * 256 + (head & 1) * 8);
}
__device__ __forceinline__ unsigned pack_o(float a, float b) { return min(pack2(a, b), 0xFFFFFFFEu); }
__device__ __forceinline__ void store_o(const f32x16& o, float f, rsrc_t wrs, unsigned voff, int soff, int head, int hh) {
    put_o(u32x4{pack_o(o[0] * f, o[1] * f), pack_o(o[2] * f, o[3] * f), pack_o(o[4] * f, o[5] * f), pack_o(o[6] * f, o[7] * f)},
          u32x2{pack_o(o[8] * f, o[9] * f), pack_o(o[10] * f, o[11] * f)}, wrs, voff, soff, head, hh);
}

#define EXP4(C, a) "v_exp_f32 v[" #C "+" #a "], v[" #C "+" #a "]\n\tv_exp_f32 v[" #C "+" #a "+1], v[" #C "+" #a "+1]\n\t"         \
                   "v_exp_f32 v[" #C "+" #a "+2], v[" #C "+" #a "+2]\n\tv_exp_f32 v[" #C "+" #a "+3], v[" #C "+" #a "+3]\n\t"
#define PV(O, V, P) "v_smfmac_f32_32x32x32_bf16 v[" #O ":" #O "+15], v[" #V ":" #V "+3], v[" #P ":" #P "+7], v40\n\t"
#define QK0(S, Q) "v_mfma_f32_32x32x16_bf16 v[" #S ":" #S "+15], v[24:27], v[" #Q ":" #Q "+3], 0\n\t"
#define QK1(S, Q, K) "v_mfma_f32_32x32x8bf16_1k v[" #S ":" #S "+15], v[" #K ":" #K "+1], v[" #Q "+4:" #Q "+5], v[" #S ":" #S "+15]\n\t"
#define VLOAD(T) "s_mov_b64 exec, %[vm]\n\tds_read_b128 v[32:35], v43 offset:" #T "*5120+1280\n\t"                                \
                 "ds_read_b128 v[36:39], v43 offset:" #T "*5120+1920\n\t"
#define KBLOAD(T, K) "s_mov_b64 exec, %[km]\n\tds_read_b64 v[" #K ":" #K "+1], v42 offset:(" #T ")*5120+1024\n\ts_mov_b64 exec, -1\n\t"
#define PAIR(T, KC, KN, MID)                                                                       \
    PV(96, 36, 72) VLOAD(T) KBLOAD(T + 1, KN) EXP4(48, 0)                                          \
    QK0(64, 16) "ds_read_b128 v[24:27], v41 offset:(" #T "+1)*5120\n\t" EXP4(48, 4)                \
    QK1(64, 16, KC) EXP4(48, 8)                                                                    \
    "s_waitcnt lgkmcnt(2)\n\t" PV(80, 32, 48) EXP4(48, 12) MID "s_nop 1\n\t"                       \
    PV(80, 36, 56) EXP4(64, 0)                                                                     \
    "s_waitcnt lgkmcnt(0)\n\t" QK0(48, 8) EXP4(64, 4)                                              \
    QK1(48, 8, KN) EXP4(64, 8)                                                                     \
    PV(96, 32, 64) EXP4(64, 12) "s_nop 1\n\t"
#define PAIR_LAST(T, KC)                                                                           \
    PV(96, 36, 72) VLOAD(T) "s_mov_b64 exec, -1\n\t" EXP4(48, 0)                                   \
    QK0(64, 16) EXP4(48, 4)                                                                        \
    QK1(64, 16, KC) EXP4(48, 8)                                                                    \
    "s_waitcnt lgkmcnt(0)\n\t" PV(80, 32, 48) EXP4(48, 12) "s_nop 1\n\t"                           \
    PV(80, 36, 56) EXP4(64, 0) EXP4(64, 4) EXP4(64, 8)                                             \
    PV(96, 32, 64) EXP4(64, 12) "s_nop 1\n\t"                                                      \
    PV(96, 36, 72) "s_nop 15\n\t"
#define ZERO4(R) "v_mov_b64 v[" #R ":" #R "+1], 0\n\tv_mov_b64 v[" #R "+2:" #R "+3], 0\n\t"
#define MAX2(i) "v_max3_f32 v14, v14, v[116+" #i "], v[117+" #i "]\n\tv_max3_f32 v22, v22, v[132+" #i "], v[133+" #i "]\n\t"
#define PRIO(n, L) "s_cmp_ge_u32 %[psw], 4\n\ts_cbranch_scc1 " #L "f\n\ts_setprio " #n "\n\t" #L ":\n\t"

__global__ __launch_bounds__(768, 3) void gated_mix_kernel(const u16* __restrict__ x, const u16* __restrict__ wA, const u16* __restrict__ wB,
                                                            const u16* __restrict__ woA, const u16* __restrict__ woB,
                                                            const u16* __restrict__ gate_w, char* __restrict__ ws, u16* __restrict__ out) {
    __shared__ __attribute__((aligned(16))) char smem[KV_HEAD];
    lds_ptr lds = (lds_ptr)smem;
    const int tid = threadIdx.x, lane = tid & 63, wid = __builtin_amdgcn_readfirstlane(tid >> 6);
    const int g = lane >> 4, r16 = lane & 15;
    const rsrc_t wrs = __builtin_amdgcn_make_buffer_rsrc(ws, 0, (int)WS_END, 0x00020000);
    SPAN_BEGIN();
    STAMP_DECL STAMP(0, 0);
    const int xcd = blockIdx.x & 7, idx = blockIdx.x >> 3;
    const int mt = (idx & 7) * 2 + (xcd & 1), nt = (xcd >> 1) + (idx >> 3) * 4, m0 = mt * 128;
    if (wid == 8) *(LDS_AS u32x2*)(lds + L_CNT) = u32x2{0u, 0u};
    if (wid >= 8 && nt < 15) {
        const int lw = wid - 8;
        const unsigned xoff = (lw * 8 + (lane >> 3)) * (DM * 2) + (((lane & 7) ^ ((lane >> 3) & 7)) << 4);
        const unsigned woff = (32 * (g >> 1) + 4 * (g & 1) + lw) * 3840 + r16 * 16;
        const char* xs = (const char*)(x + (size_t)m0 * DM);
        const char* bs = (const char*)(wB + nt * 128);
        const char* as = (const char*)(wA + nt * 128);
        unsigned so = (unsigned)(size_t)lds;
#pragma unroll 1
        for (int t = -2; t < 9; ++t) {
            if (t >= 0) {
                WAIT_V(12);
                if (t == 0) STAMP(0, 1);
                RAW_BAR();
            }
            if (t < 8) {
#pragma unroll 1
                for (int i = 0; i < 4; ++i) {
                    const unsigned lx = so + (lw + 4 * i) * 1024, lb = so + 16384 + (lw + 4 * i) * K1_WP;
                    glds16x3(xs + i * (32 * DM * 2), xoff, lx, bs + i * (8 * 3840), as + i * (8 * 3840), woff, lb, lb + 16 * K1_WP);
                }
                xs += 128; bs += 64 * 3840; as += 64 * 3840;
            }
            so = so == (unsigned)(size_t)lds + 2 * K1_STAGE ? (unsigned)(size_t)lds : so + K1_STAGE;
        }
        WAIT_V(0);
        STAMP(0, 2);
        RAW_BAR();
    }
    const int head = (blockIdx.x & 7) * 4 + ((blockIdx.x >> 3) & 3), qb = blockIdx.x >> 5;
    const int omt = head, ont = qb;
    const int r32 = lane & 31, hh = lane >> 5;
    const u32x4 vfill = r32 == 20 ? u32x4{ONE2, ONE2, ONE2, ONE2} : u32x4{0u, 0u, 0u, 0u};
    const int voff = hh * 320 + (r32 < 20 ? r32 : 0) * 16;
    const unsigned olane = (r32 >> 4) * (int)O_BLK + (r32 & 15) * 16;
    const int ps = nt < 5 ? (int)Q_TILE : (int)KV_TILE;
    const int wimg = (int)WS_WO + ont * 5120;
    gu64* const f1 = (gu64*)(ws + WS_F1);
    constexpr int P_OFF = 102400;
    const int pslot = g * 256;
    if (nt == 15) {
        const int task = mt * 10 + wid, ks = task >> 2, cg = task & 3;
        lds_ptr st = lds + wid * 10240;
        if (wid < 10) {
            const char* wsrc = (const char*)((ks >= 20 ? woB : woA) + cg * 160);
#pragma unroll
            for (int i = 0; i < 10; ++i) {
                const int ci = i * 64 + lane, slot = ci / 20, cc = ci - slot * 20;
                const int c = (ks >= 20 ? ks - 20 : ks) * 4 + (slot >> 3), e = slot & 7, p = c / 5, r = c - p * 5;
                const int k = r < 4 ? (2 * p + (r >> 1)) * 20 + 4 * (r & 1) + (e & 3) + 8 * (e >> 2) : (2 * p + (e >> 2)) * 20 + 16 + (e & 3);
                glds16(wsrc, k * (DM * 2) + cc * 16, st + i * 1024);
            }
        }
        u32x4 gv[2];
        gv[0] = *(const u32x4*)(gate_w + lane * 8);
        gv[1] = lane < 16 ? *(const u32x4*)(gate_w + 512 + lane * 8) : u32x4{0u, 0u, 0u, 0u};
        float s[11];
#pragma unroll
        for (int u = 0; u < 11; ++u) {
            const int row = wid + 12 * u < 128 ? wid + 12 * u : wid;
            const u16* xr = x + (size_t)(m0 + row) * DM;
            const u32x4 xv[2] = {*(const u32x4*)(xr + lane * 8), *(const u32x4*)(xr + 512 + r16 * 8)};
            float a = 0.f;
#pragma unroll
            for (int c = 0; c < 2; ++c)
#pragma unroll
                for (int e = 0; e < 4; ++e) {
                    a = fmaf(__uint_as_float(xv[c][e] << 16), __uint_as_float(gv[c][e] << 16), a);
                    a = fmaf(__uint_as_float(xv[c][e] & 0xffff0000u), __uint_as_float(gv[c][e] & 0xffff0000u), a);
                }
            s[u] = a;
        }
#pragma unroll
        for (int o = 32; o; o >>= 1)
#pragma unroll
            for (int u = 0; u < 11; ++u) s[u] += __shfl_xor(s[u], o);
        float z = s[0];
#pragma unroll
        for (int u = 1; u < 11; ++u) z = lane == u ? s[u] : z;
        if (lane < 11 && wid + 12 * lane < 128) ST4(__float_as_uint(1.f / (1.f + __expf(-z))), (wid + 12 * lane) * 4, (int)WS_G + m0 * 4);
        WAIT_V(0);
        if (wid < 10) {
            lds_ptr rp = st + (8 * g + (r16 >> 2)) * 320 + (lane & 3) * 8;
#pragma unroll
            for (int j = 0; j < 10; ++j)
                ST16(__builtin_bit_cast(u32x4, cat8(lds_tr(rp + j * 32), lds_tr(rp + j * 32 + 4 * 320))), lane * 16, (int)WS_WO + (ks * 40 + cg * 10 + j) * 1024);
        }
        STAMP(0, 4);
    }
    if (wid >= 8) {
        u64 tag = __hip_atomic_load((gu64*)(ws + WS_EPOCH), RLX_AGENT) + 1;
        f32x16 c0, c1;
        {
            const unsigned long long msk = ~(0x15554ull << r32) >> (4 * hh);
            const unsigned mlo = (unsigned)msk, mhi = (unsigned)(msk >> 32);
#pragma unroll
            for (int r = 0; r < 16; ++r) {
                const int u = (r & 3) + 8 * (r >> 2);
                c0[r] = r < 8 ? 0.f : __uint_as_float(__builtin_amdgcn_sbfe(mlo, (u - 16) & 31, 1) & 0xff800000u);
                c1[r] = __uint_as_float(__builtin_amdgcn_sbfe(u < 16 ? mlo : mhi, (u + 16) & 31, 1) & 0xff800000u);
            }
        }
        asm volatile("" : "+v"(c0), "+v"(c1));
        const int hw = wid - 8;
        __builtin_amdgcn_s_setprio(3);
        STAMP(0, 3);
        RAW_BAR();
        STAMP(1, 0);
        const int fc = head * 20 + (g & 1) * 19;
        gu64* const fkv = f1 + (((g < 2 ? 640 : 1280) + fc) >> 7) * 16 * FS;
        auto go = [&](int n, gu64* w0) {
            volatile LDS_AS int* gw = (volatile LDS_AS int*)(lds + L_CNT + 4);
            if (hw == 3) {
                gu64* const w[1] = {w0};
                wait_flags<POLL1>(w, tag);
                *gw = n;
            } else {
#pragma unroll 1
                while (__builtin_amdgcn_readfirstlane(*gw) < n) __builtin_amdgcn_s_sleep(1);
                asm volatile("" ::: "memory");
            }
        };
        go(1, r16 < 3 ? fkv + r16 * FS : f1 + ((fc >> 7) * 16 + qb * 2 + (lane & 1)) * FS);
        STAMP(1, 1);
        const char* src = ws + WS_KV + (size_t)(NH + head) * KV_HEAD + hw * 1024;
#define LOADS(a, b) _Pragma("unroll 1") for (int i = a; i < b; ++i) glds16_dev(src + i * 4096, lane * 16, lds + hw * 1024 + i * 4096)
#pragma unroll 1
        for (int p = hw; p < 10; p += 4) glds16_dev(ws + WS_Q + (size_t)((NH + head) * 64 + qb * 8) * Q_TILE + p * 1024, lane * 16, lds + L_Q + p * 1024);
        LOADS(0, 7);
        WAIT_V(0); RAW_BAR();
        go(2, fkv + r16 * FS);
        LOADS(7, 28);
        WAIT_V(16); RAW_BAR();
        *(LDS_AS int*)lds = 0;
        if (nt < 15) {
            lds_ptr sp = lds + L_STASH + hw * 9216 + lane * 16;
            const u32x4 po = *(const LDS_AS u32x4*)(sp + 8192);
#pragma unroll
            for (int j = 0; j < 4; ++j) {
                const u32x4 pv[2] = {*(const LDS_AS u32x4*)(sp + j * 2048), *(const LDS_AS u32x4*)(sp + j * 2048 + 1024)};
                put_qkv(pv, po[j] & ~7u, (po[j] & ~7u) + (po[j] & 7u) * 128, ps, wrs);
            }
        }
        WAIT_V(0);
        STAMP(1, 6);
        RAW_BAR();
        if (hw == 0 && lane == 0) __hip_atomic_store(f1 + (256 + nt * 16 + mt) * FS, tag, RLX_AGENT);
        LOADS(28, 40);
#pragma unroll
        for (int i = 0; i < 2; ++i)
            put_o(u32x4{~0u, ~0u, ~0u, ~0u}, u32x2{~0u, ~0u}, wrs, olane, (int)WS_O + (128 + (qb * 8 + hw * 2 + i) * 2) * (int)O_BLK, head, hh);
        {
            const int sec = r16 >> 2, fm = max(qb * 2 - 1 + (r16 & 3), 0);
            gu64* const w[1] = {f1 + (256 + (g < 2 && sec < 3 && (r16 & 3) < 3 ? ((sec * 640 + fc) >> 7) * 16 + fm : 240 + r16)) * FS};
            wait_flags<8>(w, tag);
        }
        STAMP(1, 2);
        if (!(ABL & 1)) {
            const int q0 = qb * 8 + hw * 2;
            const int qA = (int)WS_Q + (head * 64 + q0) * (int)Q_TILE, k1 = (int)WS_KV + (head * 64 + q0) * (int)KV_TILE;
            u32x4 aqa[2], ka[3], vf[5];
            u32x2 aqb[2] = {{0u, 0u}, {0u, 0u}}, kb[3];
            float gv[2];
#pragma unroll
            for (int i = 0; i < 3; ++i) {
                const int kt = k1 + (i == 0 ? (q0 ? -(int)KV_TILE : 0) : (i - 1) * (int)KV_TILE);
                ka[i] = LD16(lane * 16, kt);
                kb[i] = LD8(r32 * 8, kt + 1024);
            }
#pragma unroll
            for (int i = 0; i < 2; ++i) aqa[i] = LD16(lane * 16, qA + i * (int)Q_TILE);
            if (!hh) {
#pragma unroll
                for (int i = 0; i < 2; ++i) aqb[i] = LD8(r32 * 8, qA + i * (int)Q_TILE + 1024);
            }
#pragma unroll
            for (int i = 0; i < 5; ++i) vf[i] = vfill;
            if (r32 < 20) {
                vf[0] = LD16(voff, k1 + (q0 ? -(int)KV_TILE : 0) + 1920);
#pragma unroll
                for (int i = 1; i < 5; ++i) vf[i] = LD16(voff, k1 + ((i - 1) >> 1) * (int)KV_TILE + 1280 + ((i - 1) & 1) * 640);
            }
#pragma unroll
            for (int i = 0; i < 2; ++i) gv[i] = __uint_as_float(LD4(r32 * 4, (int)WS_G + (q0 + i) * 128));
#pragma unroll
            for (int i = 1; i >= 0; --i) {
                if (i == 0 && q0 == 0) {
#pragma unroll
                    for (int r = 8; r < 16; ++r) c0[r] = -INFINITY;
                }
                f32x16 s0 = MFMA32(BF8(ka[i]), BF8(aqa[i]), c0);
                f32x16 s1 = MFMA32(BF8(ka[i + 1]), BF8(aqa[i]), c1);
                s0 = MFMA32H(kb[i], aqb[i], s0);
                s1 = MFMA32H(kb[i + 1], aqb[i], s1);
                float m = max3f(s0[8], s0[9], s0[10]);
                m = max3f(m, s0[11], s0[12]); m = max3f(m, s0[13], s0[14]);
                m = xhalf_max(max16(s1, fmaxf(m, s0[15])));
                const u32x2 ref = with_ref(u32x2{0u, 0u}, m, hh);
                s0 = MFMA32H((u32x2{ONE2, ONE2}), ref, s0);
                s1 = MFMA32H((u32x2{ONE2, ONE2}), ref, s1);
#pragma unroll
                for (int r = 8; r < 16; ++r) s0[r] = __builtin_amdgcn_exp2f(s0[r]);
#pragma unroll
                for (int r = 0; r < 16; ++r) s1[r] = __builtin_amdgcn_exp2f(s1[r]);
                f32x16 o = SMFMAC(BF8(vf[2 * i]), HI8(s0), f32x16{});
                o = SMFMAC(BF8(vf[2 * i + 1]), LO8(s1), o);
                o = SMFMAC(BF8(vf[2 * i + 2]), HI8(s1), o);
                const float f = gv[i] * __builtin_amdgcn_rcpf(row_sum(o));
                put_o(u32x4{pack2(o[0] * f, o[1] * f), pack2(o[2] * f, o[3] * f), pack2(o[4] * f, o[5] * f), pack2(o[6] * f, o[7] * f)},
                      u32x2{pack2(o[8] * f, o[9] * f), pack2(o[10] * f, o[11] * f)}, wrs, olane, (int)WS_O + (q0 + i) * 2 * (int)O_BLK, head, hh);
            }
        }
        WAIT_V(0);
        if (lane == 0 && __hip_atomic_fetch_add((LDS_AS int*)lds, 1, __ATOMIC_RELAXED, __HIP_MEMORY_SCOPE_WORKGROUP) == 3)
            __hip_atomic_store((gu64*)(ws + WS_FA) + (qb * 32 + head) * FS, tag, RLX_AGENT);
        STAMP(1, 3);
        f32x4 acc[2][5];
        bf16x8 of[3][2], wf[3][5];
        const int osrc = (int)WS_O + (omt * 4 + (hw & 1) * 2) * (int)O_BLK + (hw >> 1) * 10240, wsrc = wimg + (hw >> 1) * 409600;
        auto ldo = [&](int ks) {
#pragma unroll
            for (int i = 0; i < 2; ++i) of[ks % 3][i] = BF8(LD16(lane * 16, osrc + i * (int)O_BLK + ks * 1024));
        };
        auto ldw = [&](int ks) {
#pragma unroll
            for (int j = 0; j < 5; ++j) wf[ks % 3][j] = BF8(LD16(lane * 16, wsrc + ks * 40960 + j * 1024));
        };
        ldw(0); ldw(1); ldw(2);
        SB();
        RAW_BAR();
        {
            gu64* const w[1] = {(gu64*)(ws + WS_FA) + ((omt >> 2) * 32 + r32) * FS};
            wait_flags<8>(w, tag);
        }
        ldo(0); ldo(1); ldo(2);
        SB();
#pragma unroll
        for (int ks = 0; ks < 10; ++ks) {
#pragma unroll
            for (int j = 0; j < 5; ++j) {
#pragma unroll
                for (int i = 0; i < 2; ++i) acc[i][j] = MFMA16(wf[ks % 3][j], of[ks % 3][i], ks ? acc[i][j] : (f32x4{0.f, 0.f, 0.f, 0.f}));
                SB();
                if (THIN) __builtin_amdgcn_s_sleep(THIN);
                SB();
            }
            if (ks < 7) { ldo(ks + 3); ldw(ks + 3); SB(); }
            if (ks == W_AT) {
#define WLOAD(ks) _Pragma("unroll") for (int j = 0; j < 5; ++j) glds16_dev(ws + wimg + (20 + (ks)) * 40960 + j * 1024, lane * 16, lds + (ks) * 5120 + j * 1024)
#pragma unroll 1
                for (int i = 0; i < 5; ++i) WLOAD(hw * 5 + i);
            }
            if (ks == G6_AT) {
                STAMP(1, 4);
                RAW_BAR();
            }
        }
        if (blockIdx.x == 0 && hw == 0) {
            gu64* const w[4] = {f1 + (256 + lane) * FS, f1 + (320 + lane) * FS, f1 + (384 + lane) * FS, f1 + (448 + lane) * FS};
            wait_flags<8>(w, tag);
            if (lane == 0) __hip_atomic_store((gu64*)(ws + WS_EPOCH), tag, RLX_AGENT);
        }
        WAIT_V(0);
        STAMP(1, 5);
        auto put = [&]() {
#pragma unroll
            for (int i = 0; i < 2; ++i)
#pragma unroll
                for (int j = 0; j < 5; ++j)
                    *(LDS_AS f32x4*)(lds + P_OFF + (1 + (hw >> 1)) * 20480 + (((hw & 1) * 2 + i) * 5 + j) * 1024 + pslot + ((r16 + 2 * j + (g >> 1)) & 15) * 16) = acc[i][j];
        };
        RAW_BAR();
        if (hw >= 2) put();
        RAW_BAR();
        if (hw < 2) put();
    } else {
        if (nt < 15) {
            const u64 tv = __hip_atomic_load((gu64*)(ws + WS_EPOCH), RLX_AGENT) + 1;
            const char* tp;
            if (lane < 16) {
                tp = (const char*)((lane & 2 ? wA : wB) + nt * 128 + (lane & 1) * 64) + (size_t)((wid + 2) * 64 + mt * 4 + (lane >> 2)) * 3840;
            } else {
                const int r = nt * 9 + (lane < 25 ? lane - 16 : 0);
                tp = (const char*)(x + (size_t)(m0 + (r < 127 ? r : 127)) * DM) + (wid + 2) * 128;
            }
            unsigned d0 = 0;
            const int br = wid < 4, wm = (wid >> 1) & 1, wn = wid & 1;
            const int xa = (wm * 64 + r16) * 128 + ((g ^ (lane & 7)) << 4);
            const int wa = 16384 + ((wid >> 2) * 16 + 4 * g + (r16 >> 2)) * K1_WP + (wn * 64 + 4 * (lane & 3)) * 2;
            const int tile0 = mt * 4 + wm * 2, cw = nt * 128 + wn * 64;
            unsigned poff[4], poff1[4];
#pragma unroll
            for (int j = 0; j < 4; ++j) {
                if (nt < 10) {
                    const int cp = (cw - (nt < 5 ? 0 : 640)) / 8 + j * 2 + (g >> 1), p = cp / 5, r = cp - p * 5;
                    const unsigned hoff = (int)(nt < 5 ? WS_Q : WS_KV) + ((br * NH + 2 * p) * 64 + tile0) * ps;
                    poff[j] = r == 2 ? hoff + (g & 1) * 64 * ps + 1024 + r16 * 8 : hoff + (r > 2 ? 64 * ps + (r - 3) * 512 : r * 512) + r16 * 16 + (g & 1) * 8;
                    poff1[j] = poff[j] + (r == 2 ? 128 : 256);
                } else {
                    const int c = cw - 1280 + j * 16 + r16, h = c / 20, d = c - h * 20;
                    poff[j] = (int)WS_KV + ((br * NH + h) * 64 + tile0) * (int)KV_TILE + 1280 + (g >> 1) * 320 + d * 16 + (g & 1) * 8;
                    poff1[j] = poff[j] + 640;
                }
            }
            asm volatile("" : "+v"(poff[0]), "+v"(poff[1]), "+v"(poff[2]), "+v"(poff[3]), "+v"(poff1[0]), "+v"(poff1[1]), "+v"(poff1[2]), "+v"(poff1[3]));
            const u64 tag = (u64)__builtin_amdgcn_readfirstlane((unsigned)(tv >> 32)) << 32 | (unsigned)__builtin_amdgcn_readfirstlane((unsigned)tv);
            f32x4 acc[4][4];
#pragma unroll
            for (int i = 0; i < 4; ++i)
#pragma unroll
                for (int j = 0; j < 4; ++j) acc[i][j] = f32x4{0.f, 0.f, 0.f, 0.f};
            if (br) __builtin_amdgcn_s_setprio(1);
            if (nt < 10) k1_main<false>(acc, lds, xa, wa, tp, wid + 2 - K1_PF, d0); else k1_main<true>(acc, lds, xa, wa, tp, wid + 2 - K1_PF, d0);
            STAMP(0, 1);
            PF_DONE();
            const bool urgent = br && (nt < 5 || mt < 3);
            if (!urgent) RAW_BAR();
            if (br && !urgent) __builtin_amdgcn_s_sleep(B_DELAY);
            if (nt < 5) {
#pragma unroll
                for (int i = 0; i < 4; ++i)
#pragma unroll
                    for (int j = 0; j < 4; ++j) acc[i][j] *= QSCALE;
            }
            auto packed = [&](int i, int j) {
                const f32x4 a = acc[2 * i][j], b = acc[2 * i + 1][j];
                return u32x4{pack2(a[0], a[1]), pack2(a[2], a[3]), pack2(b[0], b[1]), pack2(b[2], b[3])};
            };
            if (br) {
#pragma unroll
                for (int j = 0; j < 4; ++j) {
                    const u32x4 pv[2] = {packed(0, j), packed(1, j)};
                    put_qkv(pv, poff[j], poff1[j], ps, wrs);
                }
            } else {
#pragma unroll
                for (int j = 0; j < 4; ++j)
#pragma unroll
                    for (int i = 0; i < 2; ++i) *(LDS_AS u32x4*)(lds + L_STASH + (wid - 4) * 9216 + j * 2048 + i * 1024 + lane * 16) = packed(i, j);
            }
            STAMP(0, 2);
            if (urgent) RAW_BAR();
            if (br) {
                WAIT_V(0);
                if (lane == 0 && __hip_atomic_fetch_add((LDS_AS int*)(lds + L_CNT), 1, __ATOMIC_RELAXED, __HIP_MEMORY_SCOPE_WORKGROUP) == 3)
                    __hip_atomic_store(f1 + (nt * 16 + mt) * FS, tag, RLX_AGENT);
            } else {
                *(LDS_AS u32x4*)(lds + L_STASH + (wid - 4) * 9216 + 8192 + lane * 16) =
                    u32x4{poff[0] + ((poff1[0] - poff[0]) >> 7), poff[1] + ((poff1[1] - poff[1]) >> 7), poff[2] + ((poff1[2] - poff[2]) >> 7), poff[3] + ((poff1[3] - poff[3]) >> 7)};
            }
            STAMP(0, 3);
        } else {
            RAW_BAR();
            WAIT_V(0);
        }
        const int qt = qb * 8 + wid;
        const unsigned long long vmask = 0x000fffff000fffffull;
        f32x16 o, o2;
        float gate;
        u32x4 qa0, vlo = vfill, vhi = vfill;
        u32x2 qb0;
        const unsigned l0 = (unsigned)(size_t)lds, kap = (wid >> 2) * (int)KV_TILE;
        unsigned a_ka = l0 + kap + lane * 16, a_kb = l0 + kap + r32 * 8, a_v = l0 + kap + voff;
        const unsigned qs0 = L_Q + wid * (int)Q_TILE - kap, qs1 = L_Q + (wid ^ 4) * (int)Q_TILE - kap;
        int nbar = 0x53, it = 7;
        const int psw = PRIO_SW ? wid : 4;
        __builtin_amdgcn_s_setprio(1);
        asm volatile(
            "v_mov_b32 v28, %[one]\n\tv_mov_b32 v29, %[one]\n\tv_mov_b32 v30, %[one]\n\tv_mov_b32 v31, %[one]\n\tv_mov_b32 v40, %[sidx]\n\t"
            ZERO4(72) ZERO4(76) ZERO4(80) ZERO4(84) ZERO4(88) ZERO4(92) ZERO4(96) ZERO4(100) ZERO4(104) ZERO4(108)
            "v_mov_b64 v[12:13], 0\n\tv_mov_b64 v[20:21], 0\n\t"
            "v_add_u32 v48, %[qs0], v41\n\tv_add_u32 v49, %[qs1], v41\n\tv_subrev_u32 v50, %[kap], v41\n\t"
            "v_add_u32 v51, %[qs0], v42\n\tv_add_u32 v52, %[qs1], v42\n\tv_subrev_u32 v53, %[kap], v42\n\t"
            "s_waitcnt lgkmcnt(0)\n\ts_barrier\n\t"
            "ds_read_b128 v[112:115], v50\n\tds_read_b128 v[8:11], v48\n\tds_read_b128 v[16:19], v49\n\t"
            "s_mov_b64 exec, %[km]\n\t"
            "ds_read_b64 v[30:31], v53 offset:1024\n\tds_read_b64 v[12:13], v51 offset:1024\n\tds_read_b64 v[20:21], v52 offset:1024\n\t"
            "ds_read_b64 v[28:29], v42 offset:1024\n\t"
            "s_mov_b64 exec, -1\n\t"
            "ds_read_b128 v[24:27], v41\n\t"
            "s_waitcnt lgkmcnt(5)\n\t"
            "v_mfma_f32_32x32x16_bf16 v[116:131], v[112:115], v[8:11], 0\n\t"
            "v_mfma_f32_32x32x16_bf16 v[132:147], v[112:115], v[16:19], 0\n\t"
            "s_waitcnt lgkmcnt(0)\n\t"
            QK1(116, 8, 30) QK1(132, 16, 30) QK0(48, 8)
            "s_nop 10\n\t"
            "v_max_f32 v14, v116, v117\n\tv_max_f32 v22, v132, v133\n\t"
            MAX2(2) MAX2(4) MAX2(6) MAX2(8) MAX2(10) MAX2(12) MAX2(14)
            "v_mov_b32 v15, v14\n\tv_mov_b32 v23, v22\n\ts_nop 1\n\t"
            "v_permlane32_swap_b32 v14, v15\n\tv_permlane32_swap_b32 v22, v23\n\t"
            "s_not_b64 exec, %[km]\n\t"
            "v_min_f32 v14, -v14, -v15\n\tv_min_f32 v22, -v22, -v23\n\t"
            "v_and_b32 v12, 0xffff0000, v14\n\tv_and_b32 v20, 0xffff0000, v22\n\t"
            "s_mov_b64 exec, -1\n\ts_nop 1\n\t"
            QK1(48, 8, 28)
            "s_nop 5\n\t"
            "1:\n\t"
            PRIO(2, 3) PAIR(0, 28, 30, "") PAIR(1, 30, 28, "") PAIR(2, 28, 30, PRIO(0, 4)) PAIR(3, 30, 28, "")
            "v_add_u32 v41, 20480, v41\n\tv_add_u32 v42, 20480, v42\n\tv_add_u32 v43, 20480, v43\n\t"
            "s_bitcmp0_b32 %[nbar], 0\n\t"
            "s_cbranch_scc1 2f\n\t"
            "s_barrier\n\t"
            "2:\n\t"
            "s_lshr_b32 %[nbar], %[nbar], 1\n\t"
            "s_sub_i32 %[it], %[it], 1\n\t"
            "s_cmp_lg_u32 %[it], 0\n\t"
            "s_cbranch_scc1 1b\n\t"
            "global_load_dword v44, %[goff], %[gb] sc1\n\t"
            PRIO(2, 3) PAIR(0, 28, 30, "") PAIR(1, 30, 28, "") PAIR(2, 28, 30, PRIO(0, 4)) PAIR_LAST(3, 30)
            "s_waitcnt vmcnt(0)"
            : "=&{v44}"(gate), "=&{v[80:95]}"(o), "=&{v[96:111]}"(o2), "=&{v[8:11]}"(qa0), "=&{v[12:13]}"(qb0), "+{v[32:35]}"(vlo),
              "+{v[36:39]}"(vhi), "+{v41}"(a_ka), "+{v42}"(a_kb), "+{v43}"(a_v), [nbar] "+s"(nbar), [it] "+s"(it)
            : [vm] "s"(vmask), [km] "s"(0xffffffffull), [one] "s"(ONE2), [sidx] "s"(0xDDDDDDDDu), [psw] "s"(psw), [goff] "v"(r32 * 4),
              [gb] "s"(ws + WS_G + qt * 128), [qs0] "s"(qs0), [qs1] "s"(qs1), [kap] "s"(kap)
            : "v14", "v15", "v16", "v17", "v18", "v19", "v20", "v21", "v22", "v23", "v24", "v25", "v26", "v27", "v28", "v29", "v30",
              "v31", "v40", "v48", "v49", "v50", "v51", "v52", "v53", "v54", "v55", "v56", "v57", "v58", "v59", "v60", "v61", "v62",
              "v63", "v64", "v65", "v66", "v67", "v68", "v69", "v70", "v71", "v72", "v73", "v74", "v75", "v76", "v77", "v78", "v79",
              "v112", "v113", "v114", "v115", "v116", "v117", "v118", "v119", "v120", "v121", "v122", "v123", "v124", "v125", "v126",
              "v127", "v128", "v129", "v130", "v131", "v132", "v133", "v134", "v135", "v136", "v137", "v138", "v139", "v140", "v141",
              "v142", "v143", "v144", "v145", "v146", "v147", "scc", "memory");
        STAMP(1, 2);
        __builtin_amdgcn_s_setprio(3);
#pragma unroll
        for (int i = 0; i < 3; ++i)
            *(LDS_AS f32x4*)(lds + P_OFF + (wid ^ 4) * 3072 + i * 1024 + lane * 16) = f32x4{o2[4 * i], o2[4 * i + 1], o2[4 * i + 2], o2[4 * i + 3]};
        RAW_BAR();
#pragma unroll
        for (int i = 0; i < 3; ++i) {
            const f32x4 t = *(const LDS_AS f32x4*)(lds + P_OFF + wid * 3072 + i * 1024 + lane * 16);
#pragma unroll
            for (int e = 0; e < 4; ++e) o[4 * i + e] += t[e];
        }
        const float gB = 1.f - gate;
        float l = row_sum(o);
        float amax = hh ? 0.f : fmaxf(fmaxf(fabsf(o[8]), fabsf(o[9])), fmaxf(fabsf(o[10]), fabsf(o[11])));
#pragma unroll
        for (int i = 0; i < 8; ++i) amax = fmaxf(amax, fabsf(o[i]));
        if (FORCE_FALLBACK || !__all(l >= 0x1p-64f && l <= 0x1p64f && amax <= 0x1p126f)) {
            const int kv = (int)WS_KV + (NH + head) * (int)KV_HEAD;
            u32x2 qr = hh ? u32x2{0u, 0u} : qb0;
            float mt2 = -INFINITY;
            o = f32x16{};
#pragma unroll 1
            for (int t = 0; t < 128; ++t) {
                const int kt = kv + (t & 63) * (int)KV_TILE;
                u32x2 kb = {ONE2, ONE2};
                if (!hh) kb = LD8(r32 * 8, kt + 1024);
                f32x16 sc = MFMA32(BF8(LD16(lane * 16, kt)), BF8(qa0), f32x16{});
                sc = MFMA32H(kb, qr, sc);
                if (t < 64) {
                    mt2 = max16(sc, mt2);
                    if (t == 63) qr = with_ref(qb0, xhalf_max(mt2), hh);
                } else {
                    u32x4 v0 = vfill, v1 = vfill;
                    if (r32 < 20) { v0 = LD16(voff, kt + 1280); v1 = LD16(voff, kt + 1920); }
#pragma unroll
                    for (int r = 0; r < 16; ++r) sc[r] = __builtin_amdgcn_exp2f(sc[r]);
                    o = SMFMAC(BF8(v0), LO8(sc), o);
                    o = SMFMAC(BF8(v1), HI8(sc), o);
                }
            }
            l = row_sum(o);
        }
        store_o(o, gB * __builtin_amdgcn_rcpf(l), wrs, olane, (int)WS_O + (128 + qt * 2) * (int)O_BLK, head, hh);
        STAMP(1, 3);
        RAW_BAR();
        const int mb = wid & 3, kh = wid >> 2;
        const int osrc = (int)WS_O + (128 + omt * 4 + mb) * (int)O_BLK + kh * 10240;
        u32x4 of[10];
#pragma unroll
        for (int s = 0; s < 10; ++s) of[s] = LD16(lane * 16, osrc + s * 1024);
        SB();
        f32x4 acc[5];
        lds_ptr pp = lds + P_OFF + kh * 40960 + mb * 5120 + pslot;
#pragma unroll
        for (int j = 0; j < 5; ++j) acc[j] = f32x4{0.f, 0.f, 0.f, 0.f};
        if (kh) {
#pragma unroll
            for (int j = 0; j < 5; ++j) acc[j] = *(const LDS_AS f32x4*)(pp + j * 1024 + ((r16 + 2 * j + (g >> 1)) & 15) * 16);
        }
        lds_ptr wp = lds + kh * 51200 + lane * 16;
        bf16x8 wf[2][10];
#pragma unroll
        for (int j = 0; j < 10; ++j) wf[0][j] = lds_b128(wp + j * 1024);
        auto bad = [&](int s) {
            const u32x4 a = __builtin_elementwise_max(of[s], of[s + 1]);
            return __any(max(max(a[0], a[1]), max(a[2], a[3])) == ~0u);
        };
#pragma unroll
        for (int s = 0; s < 10; s += 2) {
            if (s < 8) {
#pragma unroll
                for (int j = 0; j < 10; ++j) wf[(s / 2 + 1) & 1][j] = lds_b128(wp + (s + 2) * 5120 + j * 1024);
            }
            if (bad(s)) {
#pragma unroll 1
                for (unsigned spins = 0; spins < (1u << 20); ++spins) {
                    asm volatile("" ::: "memory");
#pragma unroll
                    for (int t = 8; t >= s; t -= 2)
                        if (t == s || bad(t)) { of[t] = LD16(lane * 16, osrc + t * 1024); of[t + 1] = LD16(lane * 16, osrc + (t + 1) * 1024); }
                    if (!bad(s)) break;
                }
            }
            if (s == 0) STAMP(1, 4);
            if (s == 8) STAMP(1, 6);
#pragma unroll
            for (int j = 0; j < 10; ++j) acc[j % 5] = MFMA16(wf[(s / 2) & 1][j], BF8(of[s + j / 5]), acc[j % 5]);
        }
        STAMP(1, 5);
#pragma unroll
        for (int j = 0; j < 5; ++j) *(LDS_AS f32x4*)(pp + j * 1024 + ((r16 + 2 * j + (g >> 1)) & 15) * 16) = acc[j];
    }
    RAW_BAR();
    if (tid < 640) {
        const int row = tid / 10, c = tid - row * 10;
        lds_ptr q = lds + P_OFF + ((row >> 4) * 5 + (c >> 1)) * 1024 + (c & 1) * 512 + (((row & 15) + c) & 15) * 16;
        f32x4 a = {0.f, 0.f, 0.f, 0.f}, b = a;
#pragma unroll
        for (int p = 0; p < 3; ++p) {
            a += *(const LDS_AS f32x4*)(q + p * 20480);
            b += *(const LDS_AS f32x4*)(q + p * 20480 + 256);
        }
        u32x4 v = {pack2(a[0], a[1]), pack2(a[2], a[3]), pack2(b[0], b[1]), pack2(b[2], b[3])};
        *(u32x4*)(out + (size_t)(omt * 64 + row) * DM + ont * 80 + c * 8) = v;
    }
    SPAN_END();
    DIAG_WAIT();
    STAMP(1, 7); FLUSH(1);
}

extern "C" void kernel_launch(void* const* d_in, const int* in_sizes, int n_in, void* d_out, int out_size, void* d_ws, size_t ws_size,
                              hipStream_t stream) {
    gated_mix_kernel<<<256, 768, 0, stream>>>((const u16*)d_in[0], (const u16*)d_in[1], (const u16*)d_in[4], (const u16*)d_in[2],
                                              (const u16*)d_in[5], (const u16*)d_in[6], (char*)d_ws, (u16*)d_out);
}
```

```cpp
#include <hip/hip_runtime.h>
#include <hip/hip_bf16.h>

typedef __attribute__((ext_vector_type(8))) __bf16 bf16x8;
typedef __attribute__((ext_vector_type(16))) __bf16 bf16x16;
typedef __attribute__((ext_vector_type(2))) __bf16 bf16x2;
typedef __attribute__((ext_vector_type(4))) short s16x4;
typedef __attribute__((ext_vector_type(8))) short s16x8;
typedef __attribute__((ext_vector_type(2))) float f32x2;
typedef __attribute__((ext_vector_type(4))) float f32x4;
typedef __attribute__((ext_vector_type(8))) float f32x8;
typedef __attribute__((ext_vector_type(16))) float f32x16;
typedef __attribute__((ext_vector_type(2))) unsigned u32x2;
typedef __attribute__((ext_vector_type(4))) unsigned u32x4;
typedef unsigned short u16;
#define LDS_AS __attribute__((address_space(3)))
typedef LDS_AS char* lds_ptr;

constexpr int SEQ = 2048, DM = 640, NH = 32, DH = 20;
constexpr size_t KV_TILE = 2560, KV_HEAD = 64 * KV_TILE;
constexpr size_t Q_TILE = 1280, Q_HEAD = 64 * Q_TILE;
constexpr size_t WS_KV = 0;
constexpr size_t WS_Q = WS_KV + 2 * NH * KV_HEAD;
constexpr size_t O_BLK = 80 * 256;
constexpr size_t WS_O = WS_Q + 2 * NH * Q_HEAD;
constexpr size_t WS_WO = WS_O + 2 * 128 * O_BLK;
constexpr size_t WS_G = WS_WO + 40 * 40 * 1024;
constexpr size_t WS_F1 = WS_G + SEQ * 4;
constexpr int FS = 16;
constexpr size_t WS_FA = WS_F1 + 512 * 128;
constexpr size_t WS_EPOCH = WS_FA + 256 * 128;
constexpr size_t WS_END = WS_EPOCH + 128;
#define SPAN_BEGIN()
#define SPAN_END()
#define STAMP_DECL
#define STAMP(k, i)
#define FLUSH(k)
#define DIAG_WAIT()
constexpr float QSCALE = 0.22360679774997896f * 1.4426950408889634f;

#define K1_PF 3
#define PF_DONE() asm volatile("s_waitcnt vmcnt(0)" : "+v"(d0) :: "memory")
#define PRIO_SW 5
#define ABL 0
#define B_DELAY 8
#define G6_AT 9
#define POLL1 2
#define W_AT 9
#define PGS 65536
#define TP_STEP 1
#define THIN 1
#define FORCE_FALLBACK 0
#define WAIT_V(n) asm volatile("s_waitcnt vmcnt(" #n ")" ::: "memory")
#define RAW_BAR() asm volatile("s_waitcnt lgkmcnt(0)\n\ts_barrier" ::: "memory")
#define MFMA16(a, b, c) __builtin_amdgcn_mfma_f32_16x16x32_bf16(a, b, c, 0, 0, 0)
#define MFMA32(a, b, c) __builtin_amdgcn_mfma_f32_32x32x16_bf16(a, b, c, 0, 0, 0)
#define MFMA32H(a, b, c) __builtin_amdgcn_mfma_f32_32x32x8bf16_1k(__builtin_bit_cast(s16x4, a), __builtin_bit_cast(s16x4, b), c, 0, 0, 0)

__device__ __forceinline__ unsigned pack2(float lo, float hi) {
    f32x2 v = {lo, hi};
    return __builtin_bit_cast(unsigned, __builtin_convertvector(v, bf16x2));
}
__device__ __forceinline__ void glds16(const char* sbase, unsigned voff, lds_ptr l) {
    unsigned keep;
    asm volatile("s_mov_b32 %0, m0\n\ts_mov_b32 m0, %3\n\ts_nop 0\n\tglobal_load_lds_dwordx4 %1, %2\n\ts_mov_b32 m0, %0"
                 : "=&s"(keep) : "v"(voff), "s"(sbase), "s"((unsigned)(size_t)l) : "memory");
}
__device__ __forceinline__ void glds16x3(const char* s0, unsigned v0, unsigned l0, const char* s1, const char* s2, unsigned v1, unsigned l1, unsigned l2) {
    unsigned keep;
    asm volatile("s_mov_b32 %0, m0\n\ts_mov_b32 m0, %3\n\ts_nop 0\n\tglobal_load_lds_dwordx4 %1, %2\n\t"
                 "s_mov_b32 m0, %7\n\ts_nop 0\n\tglobal_load_lds_dwordx4 %4, %5\n\t"
                 "s_mov_b32 m0, %8\n\ts_nop 0\n\tglobal_load_lds_dwordx4 %4, %6\n\ts_mov_b32 m0, %0"
                 : "=&s"(keep) : "v"(v0), "s"(s0), "s"(l0), "v"(v1), "s"(s1), "s"(s2), "s"(l1), "s"(l2) : "memory");
}
__device__ __forceinline__ bf16x8 lds_b128(lds_ptr p) { return *(const LDS_AS bf16x8*)p; }
__device__ __forceinline__ s16x4 lds_tr(lds_ptr p) { return __builtin_amdgcn_ds_read_tr16_b64_v4i16((LDS_AS s16x4*)p); }
__device__ __forceinline__ bf16x8 cat8(s16x4 a, s16x4 b) {
    return __builtin_bit_cast(bf16x8, __builtin_shufflevector(a, b, 0, 1, 2, 3, 4, 5, 6, 7));
}
typedef __amdgpu_buffer_rsrc_t rsrc_t;
typedef unsigned long long u64;
typedef __attribute__((address_space(1))) u64 gu64;
#define RLX_AGENT __ATOMIC_RELAXED, __HIP_MEMORY_SCOPE_AGENT
constexpr int SC1 = 16;
#define LD16(voff, soff) __builtin_amdgcn_raw_buffer_load_b128(wrs, voff, soff, SC1)
#define LD8(voff, soff) __builtin_amdgcn_raw_buffer_load_b64(wrs, voff, soff, SC1)
#define LD4(voff, soff) __builtin_amdgcn_raw_buffer_load_b32(wrs, voff, soff, SC1)
#define ST16(v, voff, soff) __builtin_amdgcn_raw_buffer_store_b128(v, wrs, (voff) + (soff), 0, SC1)
#define ST8(v, voff, soff) __builtin_amdgcn_raw_buffer_store_b64(v, wrs, voff, soff, SC1)
#define ST4(v, voff, soff) __builtin_amdgcn_raw_buffer_store_b32(v, wrs, voff, soff, SC1)
#define BF8(x) __builtin_bit_cast(bf16x8, x)
__device__ __forceinline__ void glds16_dev(const char* sbase, unsigned voff, lds_ptr l) {
    unsigned keep;
    asm volatile("s_mov_b32 %0, m0\n\ts_mov_b32 m0, %3\n\ts_nop 0\n\tglobal_load_lds_dwordx4 %1, %2 sc1\n\ts_mov_b32 m0, %0"
                 : "=&s"(keep) : "v"(voff), "s"(sbase), "s"((unsigned)(size_t)l) : "memory");
}
template <int SLEEP, int N>
__device__ __forceinline__ void wait_flags(gu64* const (&w)[N], u64 tag) {
#pragma unroll 1
    for (unsigned spins = 0; spins < (1u << 20); ++spins) {
        bool ok = true;
#pragma unroll
        for (int i = 0; i < N; ++i) ok &= __hip_atomic_load(w[i], RLX_AGENT) == tag;
        if (__all(ok)) break;
        __builtin_amdgcn_s_sleep(SLEEP);
    }
    __builtin_amdgcn_fence(__ATOMIC_ACQUIRE, "wavefront");
}
constexpr int K1_WP = 1024 + 32;
constexpr int K1_STAGE = 16384 + 32 * K1_WP;
constexpr int L_Q = 112 * 1024, L_STASH = 122 * 1024, L_CNT = 163824;
#define SB() __builtin_amdgcn_sched_barrier(0)
__device__ __forceinline__ void put_qkv(const u32x4 (&pv)[2], unsigned off0, unsigned off1, int ps, rsrc_t wrs) {
#pragma unroll
    for (int i = 0; i < 2; ++i) {
        ST8((u32x2{pv[i][0], pv[i][1]}), off0, i * ps);
        ST8((u32x2{pv[i][2], pv[i][3]}), off1, i * ps);
    }
}

template <bool VT>
__device__ __forceinline__ void k1_main(f32x4 (&acc)[4][4], lds_ptr lds, int xa, int wa, const char* tp, int tstep, unsigned& d0) {
#define TOUCH() asm volatile("global_load_dword %0, %1, off" : "+v"(d0) : "v"(tp) : "memory")
    bf16x8 xf[2][4], wf[2][4];
    auto rd = [&](int so, int kk) {
#pragma unroll
        for (int j = 0; j < 4; ++j) {
            lds_ptr p = lds + so + wa + kk * 512 + j * 32;
            wf[kk][j] = cat8(lds_tr(p), lds_tr(p + 256));
        }
#pragma unroll
        for (int i = 0; i < 4; ++i) xf[kk][i] = lds_b128(lds + so + (xa ^ (kk * 64)) + i * 2048);
    };
    auto mm = [&](int kk) {
#pragma unroll
        for (int i = 0; i < 4; ++i)
#pragma unroll
            for (int j = 0; j < 4; ++j)
                acc[i][j] = VT ? MFMA16(xf[kk][i], wf[kk][j], acc[i][j]) : MFMA16(wf[kk][j], xf[kk][i], acc[i][j]);
    };
#define ILV() do { _Pragma("unroll") for (int q_ = 0; q_ < 12; ++q_) { __builtin_amdgcn_sched_group_barrier(0x100, 1, 0); __builtin_amdgcn_sched_group_barrier(0x8, 1, 0); } __builtin_amdgcn_sched_group_barrier(0x8, 4, 0); SB(); } while (0)
    if (K1_PF && tstep < 0) TOUCH();
    RAW_BAR();
    if (K1_PF && tstep == 0) TOUCH();
    rd(0, 0); SB();
    rd(0, 1);
    mm(0); ILV();
    int so = K1_STAGE;
#pragma unroll 1
    for (int t = 1; t < 10; ++t) {
        RAW_BAR();
        if (K1_PF && tstep == t) TOUCH();
        rd(so, 0);
        mm(1); ILV();
        rd(so, 1);
        mm(0); ILV();
        so = so == 2 * K1_STAGE ? 0 : so + K1_STAGE;
    }
    mm(1);
}

#define SMFMAC(a, b8, c) __builtin_amdgcn_smfmac_f32_32x32x32_bf16(a, __builtin_bit_cast(bf16x16, b8), c, (int)0xDDDDDDDD, 0, 0)
#define LO8(s) __builtin_shufflevector(s, s, 0, 1, 2, 3, 4, 5, 6, 7)
#define HI8(s) __builtin_shufflevector(s, s, 8, 9, 10, 11, 12, 13, 14, 15)
constexpr unsigned ONE2 = 0x3F803F80u;

__device__ __forceinline__ float xhalf_max(float m) {
    auto r = __builtin_amdgcn_permlane32_swap(__float_as_uint(m), __float_as_uint(m), false, false);
    return fmaxf(__uint_as_float(r[0]), __uint_as_float(r[1]));
}
__device__ __forceinline__ float max3f(float a, float b, float c) { return __builtin_fmaxf(__builtin_fmaxf(a, b), c); }
__device__ __forceinline__ float max16(const f32x16& s, float m) {
#pragma unroll
    for (int r = 0; r < 16; r += 2) m = max3f(m, s[r], s[r + 1]);
    return m;
}
__device__ __forceinline__ float row_sum(const f32x16& o) {
    auto r = __builtin_amdgcn_permlane32_swap(__float_as_uint(o[8]), __float_as_uint(o[8]), false, false);
    return __uint_as_float(r[1]);
}
__device__ __forceinline__ u32x2 with_ref(u32x2 q, float m, int hh) {
    const unsigned h1 = __float_as_uint(-m) & 0xffff0000u;
    const float r1 = -m - __uint_as_float(h1);
    const unsigned h2 = __float_as_uint(r1) & 0xffff0000u;
    const unsigned h3 = __float_as_uint(r1 - __uint_as_float(h2)) >> 16;
    return hh ? u32x2{h1 | (h2 >> 16), h3} : q;
}
__device__ __forceinline__ void put_o(u32x4 v, u32x2 t, rsrc_t wrs, unsigned voff, int soff, int head, int hh) {
    const int c0 = (head >> 1) * 5;
    ST16(v, voff + hh * 256, soff + (c0 + (head & 1) * 2) * 256);
    if (!hh) ST8(t, voff, soff + (c0 + 4) * 256 + (head & 1) * 8);
}
__device__ __forceinline__ unsigned pack_o(float a, float b) { return min(pack2(a, b), 0xFFFFFFFEu); }
__device__ __forceinline__ void store_o(const f32x16& o, float f, rsrc_t wrs, unsigned voff, int soff, int head, int hh) {
    put_o(u32x4{pack_o(o[0] * f, o[1] * f), pack_o(o[2] * f, o[3] * f), pack_o(o[4] * f, o[5] * f), pack_o(o[6] * f, o[7] * f)},
          u32x2{pack_o(o[8] * f, o[9] * f), pack_o(o[10] * f, o[11] * f)}, wrs, voff, soff, head, hh);
}

#define EXP4(C, a) "v_exp_f32 v[" #C "+" #a "], v[" #C "+" #a "]\n\tv_exp_f32 v[" #C "+" #a "+1], v[" #C "+" #a "+1]\n\t"         \
                   "v_exp_f32 v[" #C "+" #a "+2], v[" #C "+" #a "+2]\n\tv_exp_f32 v[" #C "+" #a "+3], v[" #C "+" #a "+3]\n\t"
#define PV(O, V, P) "v_smfmac_f32_32x32x32_bf16 v[" #O ":" #O "+15], v[" #V ":" #V "+3], v[" #P ":" #P "+7], v40\n\t"
#define QK0(S, Q) "v_mfma_f32_32x32x16_bf16 v[" #S ":" #S "+15], v[24:27], v[" #Q ":" #Q "+3], 0\n\t"
#define QK1(S, Q, K) "v_mfma_f32_32x32x8bf16_1k v[" #S ":" #S "+15], v[" #K ":" #K "+1], v[" #Q "+4:" #Q "+5], v[" #S ":" #S "+15]\n\t"
#define VLOAD(T) "s_mov_b64 exec, %[vm]\n\tds_read_b128 v[32:35], v43 offset:" #T "*5120+1280\n\t"                                \
                 "ds_read_b128 v[36:39], v43 offset:" #T "*5120+1920\n\t"
#define KBLOAD(T, K) "s_mov_b64 exec, %[km]\n\tds_read_b64 v[" #K ":" #K "+1], v42 offset:(" #T ")*5120+1024\n\ts_mov_b64 exec, -1\n\t"
#define PAIR(T, KC, KN, MID)                                                                       \
    PV(96, 36, 72) VLOAD(T) KBLOAD(T + 1, KN) EXP4(48, 0)                                          \
    QK0(64, 16) "ds_read_b128 v[24:27], v41 offset:(" #T "+1)*5120\n\t" EXP4(48, 4)                \
    QK1(64, 16, KC) EXP4(48, 8)                                                                    \
    "s_waitcnt lgkmcnt(2)\n\t" PV(80, 32, 48) EXP4(48, 12) MID "s_nop 1\n\t"                       \
    PV(80, 36, 56) EXP4(64, 0)                                                                     \
    "s_waitcnt lgkmcnt(0)\n\t" QK0(48, 8) EXP4(64, 4)                                              \
    QK1(48, 8, KN) EXP4(64, 8)                                                                     \
    PV(96, 32, 64) EXP4(64, 12) "s_nop 1\n\t"
#define PAIR_LAST(T, KC)                                                                           \
    PV(96, 36, 72) VLOAD(T) "s_mov_b64 exec, -1\n\t" EXP4(48, 0)                                   \
    QK0(64, 16) EXP4(48, 4)                                                                        \
    QK1(64, 16, KC) EXP4(48, 8)                                                                    \
    "s_waitcnt lgkmcnt(0)\n\t" PV(80, 32, 48) EXP4(48, 12) "s_nop 1\n\t"                           \
    PV(80, 36, 56) EXP4(64, 0) EXP4(64, 4) EXP4(64, 8)                                             \
    PV(96, 32, 64) EXP4(64, 12) "s_nop 1\n\t"                                                      \
    PV(96, 36, 72) "s_nop 15\n\t"
#define ZERO4(R) "v_mov_b64 v[" #R ":" #R "+1], 0\n\tv_mov_b64 v[" #R "+2:" #R "+3], 0\n\t"
#define MAX2(i) "v_max3_f32 v14, v14, v[116+" #i "], v[117+" #i "]\n\tv_max3_f32 v22, v22, v[132+" #i "], v[133+" #i "]\n\t"
#define PRIO(n, L) "s_cmp_ge_u32 %[psw], 4\n\ts_cbranch_scc1 " #L "f\n\ts_setprio " #n "\n\t" #L ":\n\t"

__global__ __launch_bounds__(768, 3) void gated_mix_kernel(const u16* __restrict__ x, const u16* __restrict__ wA, const u16* __restrict__ wB,
                                                            const u16* __restrict__ woA, const u16* __restrict__ woB,
                                                            const u16* __restrict__ gate_w, char* __restrict__ ws, u16* __restrict__ out) {
    __shared__ __attribute__((aligned(16))) char smem[KV_HEAD];
    lds_ptr lds = (lds_ptr)smem;
    const int tid = threadIdx.x, lane = tid & 63, wid = __builtin_amdgcn_readfirstlane(tid >> 6);
    const int g = lane >> 4, r16 = lane & 15;
    const rsrc_t wrs = __builtin_amdgcn_make_buffer_rsrc(ws, 0, (int)WS_END, 0x00020000);
    SPAN_BEGIN();
    STAMP_DECL STAMP(0, 0);
    const int xcd = blockIdx.x & 7, idx = blockIdx.x >> 3;
    const int mt = (idx & 7) * 2 + (xcd & 1), nt = (xcd >> 1) + (idx >> 3) * 4, m0 = mt * 128;
    if (wid == 8) *(LDS_AS u32x2*)(lds + L_CNT) = u32x2{0u, 0u};
    if (wid >= 8 && nt < 15) {
        const int lw = wid - 8;
        const unsigned xoff = (lw * 8 + (lane >> 3)) * (DM * 2) + (((lane & 7) ^ ((lane >> 3) & 7)) << 4);
        const unsigned woff = (32 * (g >> 1) + 4 * (g & 1) + lw) * 3840 + r16 * 16;
        const char* xs = (const char*)(x + (size_t)m0 * DM);
        const char* bs = (const char*)(wB + nt * 128);
        const char* as = (const char*)(wA + nt * 128);
        unsigned so = (unsigned)(size_t)lds;
#pragma unroll 1
        for (int t = -2; t < 9; ++t) {
            if (t >= 0) {
                WAIT_V(12);
                if (t == 0) STAMP(0, 1);
                RAW_BAR();
            }
            if (t < 8) {
#pragma unroll 1
                for (int i = 0; i < 4; ++i) {
                    const unsigned lx = so + (lw + 4 * i) * 1024, lb = so + 16384 + (lw + 4 * i) * K1_WP;
                    glds16x3(xs + i * (32 * DM * 2), xoff, lx, bs + i * (8 * 3840), as + i * (8 * 3840), woff, lb, lb + 16 * K1_WP);
                }
                xs += 128; bs += 64 * 3840; as += 64 * 3840;
            }
            so = so == (unsigned)(size_t)lds + 2 * K1_STAGE ? (unsigned)(size_t)lds : so + K1_STAGE;
        }
        WAIT_V(0);
        STAMP(0, 2);
        RAW_BAR();
    }
    const int head = (blockIdx.x & 7) * 4 + ((blockIdx.x >> 3) & 3), qb = blockIdx.x >> 5;
    const int omt = head, ont = qb;
    const int r32 = lane & 31, hh = lane >> 5;
    const u32x4 vfill = r32 == 20 ? u32x4{ONE2, ONE2, ONE2, ONE2} : u32x4{0u, 0u, 0u, 0u};
    const int voff = hh * 320 + (r32 < 20 ? r32 : 0) * 16;
    const unsigned olane = (r32 >> 4) * (int)O_BLK + (r32 & 15) * 16;
    const int ps = nt < 5 ? (int)Q_TILE : (int)KV_TILE;
    const int wimg = (int)WS_WO + ont * 5120;
    gu64* const f1 = (gu64*)(ws + WS_F1);
    constexpr int P_OFF = 102400;
    const int pslot = g * 256;
    if (nt == 15) {
        const int task = mt * 10 + wid, ks = task >> 2, cg = task & 3;
        lds_ptr st = lds + wid * 10240;
        if (wid < 10) {
            const char* wsrc = (const char*)((ks >= 20 ? woB : woA) + cg * 160);
#pragma unroll
            for (int i = 0; i < 10; ++i) {
                const int ci = i * 64 + lane, slot = ci / 20, cc = ci - slot * 20;
                const int c = (ks >= 20 ? ks - 20 : ks) * 4 + (slot >> 3), e = slot & 7, p = c / 5, r = c - p * 5;
                const int k = r < 4 ? (2 * p + (r >> 1)) * 20 + 4 * (r & 1) + (e & 3) + 8 * (e >> 2) : (2 * p + (e >> 2)) * 20 + 16 + (e & 3);
                glds16(wsrc, k * (DM * 2) + cc * 16, st + i * 1024);
            }
        }
        u32x4 gv[2];
        gv[0] = *(const u32x4*)(gate_w + lane * 8);
        gv[1] = lane < 16 ? *(const u32x4*)(gate_w + 512 + lane * 8) : u32x4{0u, 0u, 0u, 0u};
        float s[11];
#pragma unroll
        for (int u = 0; u < 11; ++u) {
            const int row = wid + 12 * u < 128 ? wid + 12 * u : wid;
            const u16* xr = x + (size_t)(m0 + row) * DM;
            const u32x4 xv[2] = {*(const u32x4*)(xr + lane * 8), *(const u32x4*)(xr + 512 + r16 * 8)};
            float a = 0.f;
#pragma unroll
            for (int c = 0; c < 2; ++c)
#pragma unroll
                for (int e = 0; e < 4; ++e) {
                    a = fmaf(__uint_as_float(xv[c][e] << 16), __uint_as_float(gv[c][e] << 16), a);
                    a = fmaf(__uint_as_float(xv[c][e] & 0xffff0000u), __uint_as_float(gv[c][e] & 0xffff0000u), a);
                }
            s[u] = a;
        }
#pragma unroll
        for (int o = 32; o; o >>= 1)
#pragma unroll
            for (int u = 0; u < 11; ++u) s[u] += __shfl_xor(s[u], o);
        float z = s[0];
#pragma unroll
        for (int u = 1; u < 11; ++u) z = lane == u ? s[u] : z;
        if (lane < 11 && wid + 12 * lane < 128) ST4(__float_as_uint(1.f / (1.f + __expf(-z))), (wid + 12 * lane) * 4, (int)WS_G + m0 * 4);
        WAIT_V(0);
        if (wid < 10) {
            lds_ptr rp = st + (8 * g + (r16 >> 2)) * 320 + (lane & 3) * 8;
#pragma unroll
            for (int j = 0; j < 10; ++j)
                ST16(__builtin_bit_cast(u32x4, cat8(lds_tr(rp + j * 32), lds_tr(rp + j * 32 + 4 * 320))), lane * 16, (int)WS_WO + (ks * 40 + cg * 10 + j) * 1024);
        }
        STAMP(0, 4);
    }
    if (wid >= 8) {
        u64 tag = __hip_atomic_load((gu64*)(ws + WS_EPOCH), RLX_AGENT) + 1;
        f32x16 c0, c1;
        {
            const unsigned long long msk = ~(0x15554ull << r32) >> (4 * hh);
            const unsigned mlo = (unsigned)msk, mhi = (unsigned)(msk >> 32);
#pragma unroll
            for (int r = 0; r < 16; ++r) {
                const int u = (r & 3) + 8 * (r >> 2);
                c0[r] = r < 8 ? 0.f : __uint_as_float(__builtin_amdgcn_sbfe(mlo, (u - 16) & 31, 1) & 0xff800000u);
                c1[r] = __uint_as_float(__builtin_amdgcn_sbfe(u < 16 ? mlo : mhi, (u + 16) & 31, 1) & 0xff800000u);
            }
        }
        asm volatile("" : "+v"(c0), "+v"(c1));
        const int hw = wid - 8;
        __builtin_amdgcn_s_setprio(3);
        STAMP(0, 3);
        RAW_BAR();
        STAMP(1, 0);
        const int fc = head * 20 + (g & 1) * 19;
        gu64* const fkv = f1 + (((g < 2 ? 640 : 1280) + fc) >> 7) * 16 * FS;
        auto go = [&](int n, gu64* w0) {
            volatile LDS_AS int* gw = (volatile LDS_AS int*)(lds + L_CNT + 4);
            if (hw == 3) {
                gu64* const w[1] = {w0};
                wait_flags<POLL1>(w, tag);
                *gw = n;
            } else {
#pragma unroll 1
                while (__builtin_amdgcn_readfirstlane(*gw) < n) __builtin_amdgcn_s_sleep(1);
                asm volatile("" ::: "memory");
            }
        };
        go(1, r16 < 3 ? fkv + r16 * FS : f1 + ((fc >> 7) * 16 + qb * 2 + (lane & 1)) * FS);
        STAMP(1, 1);
        const char* src = ws + WS_KV + (size_t)(NH + head) * KV_HEAD + hw * 1024;
#define LOADS(a, b) _Pragma("unroll 1") for (int i = a; i < b; ++i) glds16_dev(src + i * 4096, lane * 16, lds + hw * 1024 + i * 4096)
#pragma unroll 1
        for (int p = hw; p < 10; p += 4) glds16_dev(ws + WS_Q + (size_t)((NH + head) * 64 + qb * 8) * Q_TILE + p * 1024, lane * 16, lds + L_Q + p * 1024);
        LOADS(0, 7);
        WAIT_V(0); RAW_BAR();
        go(2, fkv + r16 * FS);
        LOADS(7, 28);
        WAIT_V(16); RAW_BAR();
        *(LDS_AS int*)lds = 0;
        if (nt < 15) {
            lds_ptr sp = lds + L_STASH + hw * 9216 + lane * 16;
            const u32x4 po = *(const LDS_AS u32x4*)(sp + 8192);
#pragma unroll
            for (int j = 0; j < 4; ++j) {
                const u32x4 pv[2] = {*(const LDS_AS u32x4*)(sp + j * 2048), *(const LDS_AS u32x4*)(sp + j * 2048 + 1024)};
                put_qkv(pv, po[j] & ~7u, (po[j] & ~7u) + (po[j] & 7u) * 128, ps, wrs);
            }
        }
        WAIT_V(0);
        STAMP(1, 6);
        RAW_BAR();
        if (hw == 0 && lane == 0) __hip_atomic_store(f1 + (256 + nt * 16 + mt) * FS, tag, RLX_AGENT);
        LOADS(28, 40);
#pragma unroll
        for (int i = 0; i < 2; ++i)
            put_o(u32x4{~0u, ~0u, ~0u, ~0u}, u32x2{~0u, ~0u}, wrs, olane, (int)WS_O + (128 + (qb * 8 + hw * 2 + i) * 2) * (int)O_BLK, head, hh);
        {
            const int sec = r16 >> 2, fm = max(qb * 2 - 1 + (r16 & 3), 0);
            gu64* const w[1] = {f1 + (256 + (g < 2 && sec < 3 && (r16 & 3) < 3 ? ((sec * 640 + fc) >> 7) * 16 + fm : 240 + r16)) * FS};
            wait_flags<8>(w, tag);
        }
        STAMP(1, 2);
        if (!(ABL & 1)) {
            const int q0 = qb * 8 + hw * 2;
            const int qA = (int)WS_Q + (head * 64 + q0) * (int)Q_TILE, k1 = (int)WS_KV + (head * 64 + q0) * (int)KV_TILE;
            u32x4 aqa[2], ka[3], vf[5];
            u32x2 aqb[2] = {{0u, 0u}, {0u, 0u}}, kb[3];
            float gv[2];
#pragma unroll
            for (int i = 0; i < 3; ++i) {
                const int kt = k1 + (i == 0 ? (q0 ? -(int)KV_TILE : 0) : (i - 1) * (int)KV_TILE);
                ka[i] = LD16(lane * 16, kt);
                kb[i] = LD8(r32 * 8, kt + 1024);
            }
#pragma unroll
            for (int i = 0; i < 2; ++i) aqa[i] = LD16(lane * 16, qA + i * (int)Q_TILE);
            if (!hh) {
#pragma unroll
                for (int i = 0; i < 2; ++i) aqb[i] = LD8(r32 * 8, qA + i * (int)Q_TILE + 1024);
            }
#pragma unroll
            for (int i = 0; i < 5; ++i) vf[i] = vfill;
            if (r32 < 20) {
                vf[0] = LD16(voff, k1 + (q0 ? -(int)KV_TILE : 0) + 1920);
#pragma unroll
                for (int i = 1; i < 5; ++i) vf[i] = LD16(voff, k1 + ((i - 1) >> 1) * (int)KV_TILE + 1280 + ((i - 1) & 1) * 640);
            }
#pragma unroll
            for (int i = 0; i < 2; ++i) gv[i] = __uint_as_float(LD4(r32 * 4, (int)WS_G + (q0 + i) * 128));
#pragma unroll
            for (int i = 1; i >= 0; --i) {
                if (i == 0 && q0 == 0) {
#pragma unroll
                    for (int r = 8; r < 16; ++r) c0[r] = -INFINITY;
                }
                f32x16 s0 = MFMA32(BF8(ka[i]), BF8(aqa[i]), c0);
                f32x16 s1 = MFMA32(BF8(ka[i + 1]), BF8(aqa[i]), c1);
                s0 = MFMA32H(kb[i], aqb[i], s0);
                s1 = MFMA32H(kb[i + 1], aqb[i], s1);
                float m = max3f(s0[8], s0[9], s0[10]);
                m = max3f(m, s0[11], s0[12]); m = max3f(m, s0[13], s0[14]);
                m = xhalf_max(max16(s1, fmaxf(m, s0[15])));
                const u32x2 ref = with_ref(u32x2{0u, 0u}, m, hh);
                s0 = MFMA32H((u32x2{ONE2, ONE2}), ref, s0);
                s1 = MFMA32H((u32x2{ONE2, ONE2}), ref, s1);
#pragma unroll
                for (int r = 8; r < 16; ++r) s0[r] = __builtin_amdgcn_exp2f(s0[r]);
#pragma unroll
                for (int r = 0; r < 16; ++r) s1[r] = __builtin_amdgcn_exp2f(s1[r]);
                f32x16 o = SMFMAC(BF8(vf[2 * i]), HI8(s0), f32x16{});
                o = SMFMAC(BF8(vf[2 * i + 1]), LO8(s1), o);
                o = SMFMAC(BF8(vf[2 * i + 2]), HI8(s1), o);
                const float f = gv[i] * __builtin_amdgcn_rcpf(row_sum(o));
                put_o(u32x4{pack2(o[0] * f, o[1] * f), pack2(o[2] * f, o[3] * f), pack2(o[4] * f, o[5] * f), pack2(o[6] * f, o[7] * f)},
                      u32x2{pack2(o[8] * f, o[9] * f), pack2(o[10] * f, o[11] * f)}, wrs, olane, (int)WS_O + (q0 + i) * 2 * (int)O_BLK, head, hh);
            }
        }
        WAIT_V(0);
        if (lane == 0 && __hip_atomic_fetch_add((LDS_AS int*)lds, 1, __ATOMIC_RELAXED, __HIP_MEMORY_SCOPE_WORKGROUP) == 3)
            __hip_atomic_store((gu64*)(ws + WS_FA) + (qb * 32 + head) * FS, tag, RLX_AGENT);
        STAMP(1, 3);
        f32x4 acc[2][5];
        bf16x8 of[3][2], wf[3][5];
        const int osrc = (int)WS_O + (omt * 4 + (hw & 1) * 2) * (int)O_BLK + (hw >> 1) * 10240, wsrc = wimg + (hw >> 1) * 409600;
        auto ldo = [&](int ks) {
#pragma unroll
            for (int i = 0; i < 2; ++i) of[ks % 3][i] = BF8(LD16(lane * 16, osrc + i * (int)O_BLK + ks * 1024));
        };
        auto ldw = [&](int ks) {
#pragma unroll
            for (int j = 0; j < 5; ++j) wf[ks % 3][j] = BF8(LD16(lane * 16, wsrc + ks * 40960 + j * 1024));
        };
        ldw(0); ldw(1); ldw(2);
        SB();
        RAW_BAR();
        {
            gu64* const w[1] = {(gu64*)(ws + WS_FA) + ((omt >> 2) * 32 + r32) * FS};
            wait_flags<8>(w, tag);
        }
        ldo(0); ldo(1); ldo(2);
        SB();
#pragma unroll
        for (int ks = 0; ks < 10; ++ks) {
#pragma unroll
            for (int j = 0; j < 5; ++j) {
#pragma unroll
                for (int i = 0; i < 2; ++i) acc[i][j] = MFMA16(wf[ks % 3][j], of[ks % 3][i], ks ? acc[i][j] : (f32x4{0.f, 0.f, 0.f, 0.f}));
                SB();
                if (THIN) __builtin_amdgcn_s_sleep(THIN);
                SB();
            }
            if (ks < 7) { ldo(ks + 3); ldw(ks + 3); SB(); }
            if (ks == W_AT) {
#define WLOAD(ks) _Pragma("unroll") for (int j = 0; j < 5; ++j) glds16_dev(ws + wimg + (20 + (ks)) * 40960 + j * 1024, lane * 16, lds + (ks) * 5120 + j * 1024)
#pragma unroll 1
                for (int i = 0; i < 5; ++i) WLOAD(hw * 5 + i);
            }
            if (ks == G6_AT) {
                STAMP(1, 4);
                RAW_BAR();
            }
        }
        if (blockIdx.x == 0 && hw == 0) {
            gu64* const w[4] = {f1 + (256 + lane) * FS, f1 + (320 + lane) * FS, f1 + (384 + lane) * FS, f1 + (448 + lane) * FS};
            wait_flags<8>(w, tag);
            if (lane == 0) __hip_atomic_store((gu64*)(ws + WS_EPOCH), tag, RLX_AGENT);
        }
        WAIT_V(0);
        STAMP(1, 5);
        auto put = [&]() {
#pragma unroll
            for (int i = 0; i < 2; ++i)
#pragma unroll
                for (int j = 0; j < 5; ++j)
                    *(LDS_AS f32x4*)(lds + P_OFF + (1 + (hw >> 1)) * 20480 + (((hw & 1) * 2 + i) * 5 + j) * 1024 + pslot + ((r16 + 2 * j + (g >> 1)) & 15) * 16) = acc[i][j];
        };
        RAW_BAR();
        if (hw >= 2) put();
        RAW_BAR();
        if (hw < 2) put();
    } else {
        if (nt < 15) {
            const u64 tv = __hip_atomic_load((gu64*)(ws + WS_EPOCH), RLX_AGENT) + 1;
            const char* tp;
            {
                const unsigned pg = ((idx < 24 ? idx : idx - 8) * 8 + wid) * 64 + lane;
                constexpr unsigned NW = (unsigned)((WS_END + PGS - 1) / PGS), NO = SEQ * DM * 2 / PGS;
                tp = pg < NW ? ws + (size_t)pg * PGS : pg < NW + NO ? (const char*)out + (size_t)(pg - NW) * PGS : ws;
            }
            unsigned d0 = 0;
            const int br = wid < 4, wm = (wid >> 1) & 1, wn = wid & 1;
            const int xa = (wm * 64 + r16) * 128 + ((g ^ (lane & 7)) << 4);
            const int wa = 16384 + ((wid >> 2) * 16 + 4 * g + (r16 >> 2)) * K1_WP + (wn * 64 + 4 * (lane & 3)) * 2;
            const int tile0 = mt * 4 + wm * 2, cw = nt * 128 + wn * 64;
            unsigned poff[4], poff1[4];
#pragma unroll
            for (int j = 0; j < 4; ++j) {
                if (nt < 10) {
                    const int cp = (cw - (nt < 5 ? 0 : 640)) / 8 + j * 2 + (g >> 1), p = cp / 5, r = cp - p * 5;
                    const unsigned hoff = (int)(nt < 5 ? WS_Q : WS_KV) + ((br * NH + 2 * p) * 64 + tile0) * ps;
                    poff[j] = r == 2 ? hoff + (g & 1) * 64 * ps + 1024 + r16 * 8 : hoff + (r > 2 ? 64 * ps + (r - 3) * 512 : r * 512) + r16 * 16 + (g & 1) * 8;
                    poff1[j] = poff[j] + (r == 2 ? 128 : 256);
                } else {
                    const int c = cw - 1280 + j * 16 + r16, h = c / 20, d = c - h * 20;
                    poff[j] = (int)WS_KV + ((br * NH + h) * 64 + tile0) * (int)KV_TILE + 1280 + (g >> 1) * 320 + d * 16 + (g & 1) * 8;
                    poff1[j] = poff[j] + 640;
                }
            }
            asm volatile("" : "+v"(poff[0]), "+v"(poff[1]), "+v"(poff[2]), "+v"(poff[3]), "+v"(poff1[0]), "+v"(poff1[1]), "+v"(poff1[2]), "+v"(poff1[3]));
            const u64 tag = (u64)__builtin_amdgcn_readfirstlane((unsigned)(tv >> 32)) << 32 | (unsigned)__builtin_amdgcn_readfirstlane((unsigned)tv);
            f32x4 acc[4][4];
#pragma unroll
            for (int i = 0; i < 4; ++i)
#pragma unroll
                for (int j = 0; j < 4; ++j) acc[i][j] = f32x4{0.f, 0.f, 0.f, 0.f};
            if (br) __builtin_amdgcn_s_setprio(1);
            if (nt < 10) k1_main<false>(acc, lds, xa, wa, tp, TP_STEP, d0); else k1_main<true>(acc, lds, xa, wa, tp, TP_STEP, d0);
            STAMP(0, 1);
            PF_DONE();
            const bool urgent = br && (nt < 5 || mt < 3);
            if (!urgent) RAW_BAR();
            if (br && !urgent) __builtin_amdgcn_s_sleep(B_DELAY);
            if (nt < 5) {
#pragma unroll
                for (int i = 0; i < 4; ++i)
#pragma unroll
                    for (int j = 0; j < 4; ++j) acc[i][j] *= QSCALE;
            }
            auto packed = [&](int i, int j) {
                const f32x4 a = acc[2 * i][j], b = acc[2 * i + 1][j];
                return u32x4{pack2(a[0], a[1]), pack2(a[2], a[3]), pack2(b[0], b[1]), pack2(b[2], b[3])};
            };
            if (br) {
#pragma unroll
                for (int j = 0; j < 4; ++j) {
                    const u32x4 pv[2] = {packed(0, j), packed(1, j)};
                    put_qkv(pv, poff[j], poff1[j], ps, wrs);
                }
            } else {
#pragma unroll
                for (int j = 0; j < 4; ++j)
#pragma unroll
                    for (int i = 0; i < 2; ++i) *(LDS_AS u32x4*)(lds + L_STASH + (wid - 4) * 9216 + j * 2048 + i * 1024 + lane * 16) = packed(i, j);
            }
            STAMP(0, 2);
            if (urgent) RAW_BAR();
            if (br) {
                WAIT_V(0);
                if (lane == 0 && __hip_atomic_fetch_add((LDS_AS int*)(lds + L_CNT), 1, __ATOMIC_RELAXED, __HIP_MEMORY_SCOPE_WORKGROUP) == 3)
                    __hip_atomic_store(f1 + (nt * 16 + mt) * FS, tag, RLX_AGENT);
            } else {
                *(LDS_AS u32x4*)(lds + L_STASH + (wid - 4) * 9216 + 8192 + lane * 16) =
                    u32x4{poff[0] + ((poff1[0] - poff[0]) >> 7), poff[1] + ((poff1[1] - poff[1]) >> 7), poff[2] + ((poff1[2] - poff[2]) >> 7), poff[3] + ((poff1[3] - poff[3]) >> 7)};
            }
            STAMP(0, 3);
        } else {
            RAW_BAR();
            WAIT_V(0);
        }
        const int qt = qb * 8 + wid;
        const unsigned long long vmask = 0x000fffff000fffffull;
        f32x16 o, o2;
        float gate;
        u32x4 qa0, vlo = vfill, vhi = vfill;
        u32x2 qb0;
        const unsigned l0 = (unsigned)(size_t)lds, kap = (wid >> 2) * (int)KV_TILE;
        unsigned a_ka = l0 + kap + lane * 16, a_kb = l0 + kap + r32 * 8, a_v = l0 + kap + voff;
        const unsigned qs0 = L_Q + wid * (int)Q_TILE - kap, qs1 = L_Q + (wid ^ 4) * (int)Q_TILE - kap;
        int nbar = 0x53, it = 7;
        const int psw = PRIO_SW ? wid : 4;
        __builtin_amdgcn_s_setprio(1);
        asm volatile(
            "v_mov_b32 v28, %[one]\n\tv_mov_b32 v29, %[one]\n\tv_mov_b32 v30, %[one]\n\tv_mov_b32 v31, %[one]\n\tv_mov_b32 v40, %[sidx]\n\t"
            ZERO4(72) ZERO4(76) ZERO4(80) ZERO4(84) ZERO4(88) ZERO4(92) ZERO4(96) ZERO4(100) ZERO4(104) ZERO4(108)
            "v_mov_b64 v[12:13], 0\n\tv_mov_b64 v[20:21], 0\n\t"
            "v_add_u32 v48, %[qs0], v41\n\tv_add_u32 v49, %[qs1], v41\n\tv_subrev_u32 v50, %[kap], v41\n\t"
            "v_add_u32 v51, %[qs0], v42\n\tv_add_u32 v52, %[qs1], v42\n\tv_subrev_u32 v53, %[kap], v42\n\t"
            "s_waitcnt lgkmcnt(0)\n\ts_barrier\n\t"
            "ds_read_b128 v[112:115], v50\n\tds_read_b128 v[8:11], v48\n\tds_read_b128 v[16:19], v49\n\t"
            "s_mov_b64 exec, %[km]\n\t"
            "ds_read_b64 v[30:31], v53 offset:1024\n\tds_read_b64 v[12:13], v51 offset:1024\n\tds_read_b64 v[20:21], v52 offset:1024\n\t"
            "ds_read_b64 v[28:29], v42 offset:1024\n\t"
            "s_mov_b64 exec, -1\n\t"
            "ds_read_b128 v[24:27], v41\n\t"
            "s_waitcnt lgkmcnt(5)\n\t"
            "v_mfma_f32_32x32x16_bf16 v[116:131], v[112:115], v[8:11], 0\n\t"
            "v_mfma_f32_32x32x16_bf16 v[132:147], v[112:115], v[16:19], 0\n\t"
            "s_waitcnt lgkmcnt(0)\n\t"
            QK1(116, 8, 30) QK1(132, 16, 30) QK0(48, 8)
            "s_nop 10\n\t"
            "v_max_f32 v14, v116, v117\n\tv_max_f32 v22, v132, v133\n\t"
            MAX2(2) MAX2(4) MAX2(6) MAX2(8) MAX2(10) MAX2(12) MAX2(14)
            "v_mov_b32 v15, v14\n\tv_mov_b32 v23, v22\n\ts_nop 1\n\t"
            "v_permlane32_swap_b32 v14, v15\n\tv_permlane32_swap_b32 v22, v23\n\t"
            "s_not_b64 exec, %[km]\n\t"
            "v_min_f32 v14, -v14, -v15\n\tv_min_f32 v22, -v22, -v23\n\t"
            "v_and_b32 v12, 0xffff0000, v14\n\tv_and_b32 v20, 0xffff0000, v22\n\t"
            "s_mov_b64 exec, -1\n\ts_nop 1\n\t"
            QK1(48, 8, 28)
            "s_nop 5\n\t"
            "1:\n\t"
            PRIO(2, 3) PAIR(0, 28, 30, "") PAIR(1, 30, 28, "") PAIR(2, 28, 30, PRIO(0, 4)) PAIR(3, 30, 28, "")
            "v_add_u32 v41, 20480, v41\n\tv_add_u32 v42, 20480, v42\n\tv_add_u32 v43, 20480, v43\n\t"
            "s_bitcmp0_b32 %[nbar], 0\n\t"
            "s_cbranch_scc1 2f\n\t"
            "s_barrier\n\t"
            "2:\n\t"
            "s_lshr_b32 %[nbar], %[nbar], 1\n\t"
            "s_sub_i32 %[it], %[it], 1\n\t"
            "s_cmp_lg_u32 %[it], 0\n\t"
            "s_cbranch_scc1 1b\n\t"
            "global_load_dword v44, %[goff], %[gb] sc1\n\t"
            PRIO(2, 3) PAIR(0, 28, 30, "") PAIR(1, 30, 28, "") PAIR(2, 28, 30, PRIO(0, 4)) PAIR_LAST(3, 30)
            "s_waitcnt vmcnt(0)"
            : "=&{v44}"(gate), "=&{v[80:95]}"(o), "=&{v[96:111]}"(o2), "=&{v[8:11]}"(qa0), "=&{v[12:13]}"(qb0), "+{v[32:35]}"(vlo),
              "+{v[36:39]}"(vhi), "+{v41}"(a_ka), "+{v42}"(a_kb), "+{v43}"(a_v), [nbar] "+s"(nbar), [it] "+s"(it)
            : [vm] "s"(vmask), [km] "s"(0xffffffffull), [one] "s"(ONE2), [sidx] "s"(0xDDDDDDDDu), [psw] "s"(psw), [goff] "v"(r32 * 4),
              [gb] "s"(ws + WS_G + qt * 128), [qs0] "s"(qs0), [qs1] "s"(qs1), [kap] "s"(kap)
            : "v14", "v15", "v16", "v17", "v18", "v19", "v20", "v21", "v22", "v23", "v24", "v25", "v26", "v27", "v28", "v29", "v30",
              "v31", "v40", "v48", "v49", "v50", "v51", "v52", "v53", "v54", "v55", "v56", "v57", "v58", "v59", "v60", "v61", "v62",
              "v63", "v64", "v65", "v66", "v67", "v68", "v69", "v70", "v71", "v72", "v73", "v74", "v75", "v76", "v77", "v78", "v79",
              "v112", "v113", "v114", "v115", "v116", "v117", "v118", "v119", "v120", "v121", "v122", "v123", "v124", "v125", "v126",
              "v127", "v128", "v129", "v130", "v131", "v132", "v133", "v134", "v135", "v136", "v137", "v138", "v139", "v140", "v141",
              "v142", "v143", "v144", "v145", "v146", "v147", "scc", "memory");
        STAMP(1, 2);
        __builtin_amdgcn_s_setprio(3);
#pragma unroll
        for (int i = 0; i < 3; ++i)
            *(LDS_AS f32x4*)(lds + P_OFF + (wid ^ 4) * 3072 + i * 1024 + lane * 16) = f32x4{o2[4 * i], o2[4 * i + 1], o2[4 * i + 2], o2[4 * i + 3]};
        RAW_BAR();
#pragma unroll
        for (int i = 0; i < 3; ++i) {
            const f32x4 t = *(const LDS_AS f32x4*)(lds + P_OFF + wid * 3072 + i * 1024 + lane * 16);
#pragma unroll
            for (int e = 0; e < 4; ++e) o[4 * i + e] += t[e];
        }
        const float gB = 1.f - gate;
        float l = row_sum(o);
        float amax = hh ? 0.f : fmaxf(fmaxf(fabsf(o[8]), fabsf(o[9])), fmaxf(fabsf(o[10]), fabsf(o[11])));
#pragma unroll
        for (int i = 0; i < 8; ++i) amax = fmaxf(amax, fabsf(o[i]));
        if (FORCE_FALLBACK || !__all(l >= 0x1p-64f && l <= 0x1p64f && amax <= 0x1p126f)) {
            const int kv = (int)WS_KV + (NH + head) * (int)KV_HEAD;
            u32x2 qr = hh ? u32x2{0u, 0u} : qb0;
            float mt2 = -INFINITY;
            o = f32x16{};
#pragma unroll 1
            for (int t = 0; t < 128; ++t) {
                const int kt = kv + (t & 63) * (int)KV_TILE;
                u32x2 kb = {ONE2, ONE2};
                if (!hh) kb = LD8(r32 * 8, kt + 1024);
                f32x16 sc = MFMA32(BF8(LD16(lane * 16, kt)), BF8(qa0), f32x16{});
                sc = MFMA32H(kb, qr, sc);
                if (t < 64) {
                    mt2 = max16(sc, mt2);
                    if (t == 63) qr = with_ref(qb0, xhalf_max(mt2), hh);
                } else {
                    u32x4 v0 = vfill, v1 = vfill;
                    if (r32 < 20) { v0 = LD16(voff, kt + 1280); v1 = LD16(voff, kt + 1920); }
#pragma unroll
                    for (int r = 0; r < 16; ++r) sc[r] = __builtin_amdgcn_exp2f(sc[r]);
                    o = SMFMAC(BF8(v0), LO8(sc), o);
                    o = SMFMAC(BF8(v1), HI8(sc), o);
                }
            }
            l = row_sum(o);
        }
        store_o(o, gB * __builtin_amdgcn_rcpf(l), wrs, olane, (int)WS_O + (128 + qt * 2) * (int)O_BLK, head, hh);
        STAMP(1, 3);
        RAW_BAR();
        const int mb = wid & 3, kh = wid >> 2;
        const int osrc = (int)WS_O + (128 + omt * 4 + mb) * (int)O_BLK + kh * 10240;
        u32x4 of[10];
#pragma unroll
        for (int s = 0; s < 10; ++s) of[s] = LD16(lane * 16, osrc + s * 1024);
        SB();
        f32x4 acc[5];
        lds_ptr pp = lds + P_OFF + kh * 40960 + mb * 5120 + pslot;
#pragma unroll
        for (int j = 0; j < 5; ++j) acc[j] = f32x4{0.f, 0.f, 0.f, 0.f};
        if (kh) {
#pragma unroll
            for (int j = 0; j < 5; ++j) acc[j] = *(const LDS_AS f32x4*)(pp + j * 1024 + ((r16 + 2 * j + (g >> 1)) & 15) * 16);
        }
        lds_ptr wp = lds + kh * 51200 + lane * 16;
        bf16x8 wf[2][10];
#pragma unroll
        for (int j = 0; j < 10; ++j) wf[0][j] = lds_b128(wp + j * 1024);
        auto bad = [&](int s) {
            const u32x4 a = __builtin_elementwise_max(of[s], of[s + 1]);
            return __any(max(max(a[0], a[1]), max(a[2], a[3])) == ~0u);
        };
#pragma unroll
        for (int s = 0; s < 10; s += 2) {
            if (s < 8) {
#pragma unroll
                for (int j = 0; j < 10; ++j) wf[(s / 2 + 1) & 1][j] = lds_b128(wp + (s + 2) * 5120 + j * 1024);
            }
            if (bad(s)) {
#pragma unroll 1
                for (unsigned spins = 0; spins < (1u << 20); ++spins) {
                    asm volatile("" ::: "memory");
#pragma unroll
                    for (int t = 8; t >= s; t -= 2)
                        if (t == s || bad(t)) { of[t] = LD16(lane * 16, osrc + t * 1024); of[t + 1] = LD16(lane * 16, osrc + (t + 1) * 1024); }
                    if (!bad(s)) break;
                }
            }
            if (s == 0) STAMP(1, 4);
            if (s == 8) STAMP(1, 6);
#pragma unroll
            for (int j = 0; j < 10; ++j) acc[j % 5] = MFMA16(wf[(s / 2) & 1][j], BF8(of[s + j / 5]), acc[j % 5]);
        }
        STAMP(1, 5);
#pragma unroll
        for (int j = 0; j < 5; ++j) *(LDS_AS f32x4*)(pp + j * 1024 + ((r16 + 2 * j + (g >> 1)) & 15) * 16) = acc[j];
    }
    RAW_BAR();
    if (tid < 640) {
        const int row = tid / 10, c = tid - row * 10;
        lds_ptr q = lds + P_OFF + ((row >> 4) * 5 + (c >> 1)) * 1024 + (c & 1) * 512 + (((row & 15) + c) & 15) * 16;
        f32x4 a = {0.f, 0.f, 0.f, 0.f}, b = a;
#pragma unroll
        for (int p = 0; p < 3; ++p) {
            a += *(const LDS_AS f32x4*)(q + p * 20480);
            b += *(const LDS_AS f32x4*)(q + p * 20480 + 256);
        }
        u32x4 v = {pack2(a[0], a[1]), pack2(a[2], a[3]), pack2(b[0], b[1]), pack2(b[2], b[3])};
        *(u32x4*)(out + (size_t)(omt * 64 + row) * DM + ont * 80 + c * 8) = v;
    }
    SPAN_END();
    DIAG_WAIT();
    STAMP(1, 7); FLUSH(1);
}

extern "C" void kernel_launch(void* const* d_in, const int* in_sizes, int n_in, void* d_out, int out_size, void* d_ws, size_t ws_size,
                              hipStream_t stream) {
    gated_mix_kernel<<<256, 768, 0, stream>>>((const u16*)d_in[0], (const u16*)d_in[1], (const u16*)d_in[4], (const u16*)d_in[2],
                                              (const u16*)d_in[5], (const u16*)d_in[6], (char*)d_ws, (u16*)d_out);
}
```

```cpp
#include <hip/hip_runtime.h>
#include <hip/hip_bf16.h>

typedef __attribute__((ext_vector_type(8))) __bf16 bf16x8;
typedef __attribute__((ext_vector_type(16))) __bf16 bf16x16;
typedef __attribute__((ext_vector_type(2))) __bf16 bf16x2;
typedef __attribute__((ext_vector_type(4))) short s16x4;
typedef __attribute__((ext_vector_type(8))) short s16x8;
typedef __attribute__((ext_vector_type(2))) float f32x2;
typedef __attribute__((ext_vector_type(4))) float f32x4;
typedef __attribute__((ext_vector_type(8))) float f32x8;
typedef __attribute__((ext_vector_type(16))) float f32x16;
typedef __attribute__((ext_vector_type(2))) unsigned u32x2;
typedef __attribute__((ext_vector_type(4))) unsigned u32x4;
typedef unsigned short u16;
#define LDS_AS __attribute__((address_space(3)))
typedef LDS_AS char* lds_ptr;

constexpr int SEQ = 2048, DM = 640, NH = 32, DH = 20;
constexpr size_t KV_TILE = 2560, KV_HEAD = 64 * KV_TILE;
constexpr size_t Q_TILE = 1280, Q_HEAD = 64 * Q_TILE;
constexpr size_t WS_KV = 0;
constexpr size_t WS_Q = WS_KV + 2 * NH * KV_HEAD;
constexpr size_t O_BLK = 80 * 256;
constexpr size_t WS_O = WS_Q + 2 * NH * Q_HEAD;
constexpr size_t WS_WO = WS_O + 2 * 128 * O_BLK;
constexpr size_t WS_G = WS_WO + 40 * 40 * 1024;
constexpr size_t WS_F1 = WS_G + SEQ * 4;
constexpr int FS = 16;
constexpr size_t WS_FA = WS_F1 + 512 * 128;
constexpr size_t WS_EPOCH = WS_FA + 256 * 128;
constexpr size_t WS_END = WS_EPOCH + 128;
#define SPAN_BEGIN()
#define SPAN_END()
#define STAMP_DECL
#define STAMP(k, i)
#define FLUSH(k)
#define DIAG_WAIT()
constexpr float QSCALE = 0.22360679774997896f * 1.4426950408889634f;

#define K1_PF 3
#define PF_DONE() asm volatile("s_waitcnt vmcnt(0)" : "+v"(d0) :: "memory")
#define PRIO_SW 5
#define ABL 0
#define B_DELAY 8
#define G6_AT 9
#define POLL1 2
#define W_AT 9
#define TAILW 0
#define OUT_SC1 1
#define RETRY 0
#define THIN 1
#define FORCE_FALLBACK 0
#define WAIT_V(n) asm volatile("s_waitcnt vmcnt(" #n ")" ::: "memory")
#define RAW_BAR() asm volatile("s_waitcnt lgkmcnt(0)\n\ts_barrier" ::: "memory")
#define MFMA16(a, b, c) __builtin_amdgcn_mfma_f32_16x16x32_bf16(a, b, c, 0, 0, 0)
#define MFMA32(a, b, c) __builtin_amdgcn_mfma_f32_32x32x16_bf16(a, b, c, 0, 0, 0)
#define MFMA32H(a, b, c) __builtin_amdgcn_mfma_f32_32x32x8bf16_1k(__builtin_bit_cast(s16x4, a), __builtin_bit_cast(s16x4, b), c, 0, 0, 0)

__device__ __forceinline__ unsigned pack2(float lo, float hi) {
    f32x2 v = {lo, hi};
    return __builtin_bit_cast(unsigned, __builtin_convertvector(v, bf16x2));
}
__device__ __forceinline__ void glds16(const char* sbase, unsigned voff, lds_ptr l) {
    unsigned keep;
    asm volatile("s_mov_b32 %0, m0\n\ts_mov_b32 m0, %3\n\ts_nop 0\n\tglobal_load_lds_dwordx4 %1, %2\n\ts_mov_b32 m0, %0"
                 : "=&s"(keep) : "v"(voff), "s"(sbase), "s"((unsigned)(size_t)l) : "memory");
}
__device__ __forceinline__ void glds16x3(const char* s0, unsigned v0, unsigned l0, const char* s1, const char* s2, unsigned v1, unsigned l1, unsigned l2) {
    unsigned keep;
    asm volatile("s_mov_b32 %0, m0\n\ts_mov_b32 m0, %3\n\ts_nop 0\n\tglobal_load_lds_dwordx4 %1, %2\n\t"
                 "s_mov_b32 m0, %7\n\ts_nop 0\n\tglobal_load_lds_dwordx4 %4, %5\n\t"
                 "s_mov_b32 m0, %8\n\ts_nop 0\n\tglobal_load_lds_dwordx4 %4, %6\n\ts_mov_b32 m0, %0"
                 : "=&s"(keep) : "v"(v0), "s"(s0), "s"(l0), "v"(v1), "s"(s1), "s"(s2), "s"(l1), "s"(l2) : "memory");
}
__device__ __forceinline__ bf16x8 lds_b128(lds_ptr p) { return *(const LDS_AS bf16x8*)p; }
__device__ __forceinline__ s16x4 lds_tr(lds_ptr p) { return __builtin_amdgcn_ds_read_tr16_b64_v4i16((LDS_AS s16x4*)p); }
__device__ __forceinline__ bf16x8 cat8(s16x4 a, s16x4 b) {
    return __builtin_bit_cast(bf16x8, __builtin_shufflevector(a, b, 0, 1, 2, 3, 4, 5, 6, 7));
}
typedef __amdgpu_buffer_rsrc_t rsrc_t;
typedef unsigned long long u64;
typedef __attribute__((address_space(1))) u64 gu64;
#define RLX_AGENT __ATOMIC_RELAXED, __HIP_MEMORY_SCOPE_AGENT
constexpr int SC1 = 16;
#define LD16(voff, soff) __builtin_amdgcn_raw_buffer_load_b128(wrs, voff, soff, SC1)
#define LD8(voff, soff) __builtin_amdgcn_raw_buffer_load_b64(wrs, voff, soff, SC1)
#define LD4(voff, soff) __builtin_amdgcn_raw_buffer_load_b32(wrs, voff, soff, SC1)
#define ST16(v, voff, soff) __builtin_amdgcn_raw_buffer_store_b128(v, wrs, (voff) + (soff), 0, SC1)
#define ST8(v, voff, soff) __builtin_amdgcn_raw_buffer_store_b64(v, wrs, voff, soff, SC1)
#define ST4(v, voff, soff) __builtin_amdgcn_raw_buffer_store_b32(v, wrs, voff, soff, SC1)
#define BF8(x) __builtin_bit_cast(bf16x8, x)
__device__ __forceinline__ void glds16_dev(const char* sbase, unsigned voff, lds_ptr l) {
    unsigned keep;
    asm volatile("s_mov_b32 %0, m0\n\ts_mov_b32 m0, %3\n\ts_nop 0\n\tglobal_load_lds_dwordx4 %1, %2 sc1\n\ts_mov_b32 m0, %0"
                 : "=&s"(keep) : "v"(voff), "s"(sbase), "s"((unsigned)(size_t)l) : "memory");
}
template <int SLEEP, int N>
__device__ __forceinline__ void wait_flags(gu64* const (&w)[N], u64 tag) {
#pragma unroll 1
    for (unsigned spins = 0; spins < (1u << 20); ++spins) {
        bool ok = true;
#pragma unroll
        for (int i = 0; i < N; ++i) ok &= __hip_atomic_load(w[i], RLX_AGENT) == tag;
        if (__all(ok)) break;
        __builtin_amdgcn_s_sleep(SLEEP);
    }
    __builtin_amdgcn_fence(__ATOMIC_ACQUIRE, "wavefront");
}
constexpr int K1_WP = 1024 + 32;
constexpr int K1_STAGE = 16384 + 32 * K1_WP;
constexpr int L_Q = 112 * 1024, L_STASH = 122 * 1024, L_CNT = 163824;
#define SB() __builtin_amdgcn_sched_barrier(0)
__device__ __forceinline__ void put_qkv(const u32x4 (&pv)[2], unsigned off0, unsigned off1, int ps, rsrc_t wrs) {
#pragma unroll
    for (int i = 0; i < 2; ++i) {
        ST8((u32x2{pv[i][0], pv[i][1]}), off0, i * ps);
        ST8((u32x2{pv[i][2], pv[i][3]}), off1, i * ps);
    }
}

template <bool VT>
__device__ __forceinline__ void k1_main(f32x4 (&acc)[4][4], lds_ptr lds, int xa, int wa, const char* tp, int tstep, unsigned& d0) {
#define TOUCH() asm volatile("global_load_dword %0, %1, off" : "+v"(d0) : "v"(tp) : "memory")
    bf16x8 xf[2][4], wf[2][4];
    auto rd = [&](int so, int kk) {
#pragma unroll
        for (int j = 0; j < 4; ++j) {
            lds_ptr p = lds + so + wa + kk * 512 + j * 32;
            wf[kk][j] = cat8(lds_tr(p), lds_tr(p + 256));
        }
#pragma unroll
        for (int i = 0; i < 4; ++i) xf[kk][i] = lds_b128(lds + so + (xa ^ (kk * 64)) + i * 2048);
    };
    auto mm = [&](int kk) {
#pragma unroll
        for (int i = 0; i < 4; ++i)
#pragma unroll
            for (int j = 0; j < 4; ++j)
                acc[i][j] = VT ? MFMA16(xf[kk][i], wf[kk][j], acc[i][j]) : MFMA16(wf[kk][j], xf[kk][i], acc[i][j]);
    };
#define ILV() do { _Pragma("unroll") for (int q_ = 0; q_ < 12; ++q_) { __builtin_amdgcn_sched_group_barrier(0x100, 1, 0); __builtin_amdgcn_sched_group_barrier(0x8, 1, 0); } __builtin_amdgcn_sched_group_barrier(0x8, 4, 0); SB(); } while (0)
    if (K1_PF && tstep < 0) TOUCH();
    RAW_BAR();
    if (K1_PF && tstep == 0) TOUCH();
    rd(0, 0); SB();
    rd(0, 1);
    mm(0); ILV();
    int so = K1_STAGE;
#pragma unroll 1
    for (int t = 1; t < 10; ++t) {
        RAW_BAR();
        if (K1_PF && tstep == t) TOUCH();
        rd(so, 0);
        mm(1); ILV();
        rd(so, 1);
        mm(0); ILV();
        so = so == 2 * K1_STAGE ? 0 : so + K1_STAGE;
    }
    mm(1);
}

#define SMFMAC(a, b8, c) __builtin_amdgcn_smfmac_f32_32x32x32_bf16(a, __builtin_bit_cast(bf16x16, b8), c, (int)0xDDDDDDDD, 0, 0)
#define LO8(s) __builtin_shufflevector(s, s, 0, 1, 2, 3, 4, 5, 6, 7)
#define HI8(s) __builtin_shufflevector(s, s, 8, 9, 10, 11, 12, 13, 14, 15)
constexpr unsigned ONE2 = 0x3F803F80u;

__device__ __forceinline__ float xhalf_max(float m) {
    auto r = __builtin_amdgcn_permlane32_swap(__float_as_uint(m), __float_as_uint(m), false, false);
    return fmaxf(__uint_as_float(r[0]), __uint_as_float(r[1]));
}
__device__ __forceinline__ float max3f(float a, float b, float c) { return __builtin_fmaxf(__builtin_fmaxf(a, b), c); }
__device__ __forceinline__ float max16(const f32x16& s, float m) {
#pragma unroll
    for (int r = 0; r < 16; r += 2) m = max3f(m, s[r], s[r + 1]);
    return m;
}
__device__ __forceinline__ float row_sum(const f32x16& o) {
    auto r = __builtin_amdgcn_permlane32_swap(__float_as_uint(o[8]), __float_as_uint(o[8]), false, false);
    return __uint_as_float(r[1]);
}
__device__ __forceinline__ u32x2 with_ref(u32x2 q, float m, int hh) {
    const unsigned h1 = __float_as_uint(-m) & 0xffff0000u;
    const float r1 = -m - __uint_as_float(h1);
    const unsigned h2 = __float_as_uint(r1) & 0xffff0000u;
    const unsigned h3 = __float_as_uint(r1 - __uint_as_float(h2)) >> 16;
    return hh ? u32x2{h1 | (h2 >> 16), h3} : q;
}
__device__ __forceinline__ void put_o(u32x4 v, u32x2 t, rsrc_t wrs, unsigned voff, int soff, int head, int hh) {
    const int c0 = (head >> 1) * 5;
    ST16(v, voff + hh * 256, soff + (c0 + (head & 1) * 2) * 256);
    if (!hh) ST8(t, voff, soff + (c0 + 4) * 256 + (head & 1) * 8);
}
__device__ __forceinline__ unsigned pack_o(float a, float b) { return min(pack2(a, b), 0xFFFFFFFEu); }
__device__ __forceinline__ void store_o(const f32x16& o, float f, rsrc_t wrs, unsigned voff, int soff, int head, int hh) {
    put_o(u32x4{pack_o(o[0] * f, o[1] * f), pack_o(o[2] * f, o[3] * f), pack_o(o[4] * f, o[5] * f), pack_o(o[6] * f, o[7] * f)},
          u32x2{pack_o(o[8] * f, o[9] * f), pack_o(o[10] * f, o[11] * f)}, wrs, voff, soff, head, hh);
}

#define EXP4(C, a) "v_exp_f32 v[" #C "+" #a "], v[" #C "+" #a "]\n\tv_exp_f32 v[" #C "+" #a "+1], v[" #C "+" #a "+1]\n\t"         \
                   "v_exp_f32 v[" #C "+" #a "+2], v[" #C "+" #a "+2]\n\tv_exp_f32 v[" #C "+" #a "+3], v[" #C "+" #a "+3]\n\t"
#define PV(O, V, P) "v_smfmac_f32_32x32x32_bf16 v[" #O ":" #O "+15], v[" #V ":" #V "+3], v[" #P ":" #P "+7], v40\n\t"
#define QK0(S, Q) "v_mfma_f32_32x32x16_bf16 v[" #S ":" #S "+15], v[24:27], v[" #Q ":" #Q "+3], 0\n\t"
#define QK1(S, Q, K) "v_mfma_f32_32x32x8bf16_1k v[" #S ":" #S "+15], v[" #K ":" #K "+1], v[" #Q "+4:" #Q "+5], v[" #S ":" #S "+15]\n\t"
#define VLOAD(T) "s_mov_b64 exec, %[vm]\n\tds_read_b128 v[32:35], v43 offset:" #T "*5120+1280\n\t"                                \
                 "ds_read_b128 v[36:39], v43 offset:" #T "*5120+1920\n\t"
#define KBLOAD(T, K) "s_mov_b64 exec, %[km]\n\tds_read_b64 v[" #K ":" #K "+1], v42 offset:(" #T ")*5120+1024\n\ts_mov_b64 exec, -1\n\t"
#define PAIR(T, KC, KN, MID)                                                                       \
    PV(96, 36, 72) VLOAD(T) KBLOAD(T + 1, KN) EXP4(48, 0)                                          \
    QK0(64, 16) "ds_read_b128 v[24:27], v41 offset:(" #T "+1)*5120\n\t" EXP4(48, 4)                \
    QK1(64, 16, KC) EXP4(48, 8)                                                                    \
    "s_waitcnt lgkmcnt(2)\n\t" PV(80, 32, 48) EXP4(48, 12) MID "s_nop 1\n\t"                       \
    PV(80, 36, 56) EXP4(64, 0)                                                                     \
    "s_waitcnt lgkmcnt(0)\n\t" QK0(48, 8) EXP4(64, 4)                                              \
    QK1(48, 8, KN) EXP4(64, 8)                                                                     \
    PV(96, 32, 64) EXP4(64, 12) "s_nop 1\n\t"
#define PAIR_LAST(T, KC)                                                                           \
    PV(96, 36, 72) VLOAD(T) "s_mov_b64 exec, -1\n\t" EXP4(48, 0)                                   \
    QK0(64, 16) EXP4(48, 4)                                                                        \
    QK1(64, 16, KC) EXP4(48, 8)                                                                    \
    "s_waitcnt lgkmcnt(0)\n\t" PV(80, 32, 48) EXP4(48, 12) "s_nop 1\n\t"                           \
    PV(80, 36, 56) EXP4(64, 0) EXP4(64, 4) EXP4(64, 8)                                             \
    PV(96, 32, 64) EXP4(64, 12) "s_nop 1\n\t"                                                      \
    PV(96, 36, 72) "s_nop 15\n\t"
#define ZERO4(R) "v_mov_b64 v[" #R ":" #R "+1], 0\n\tv_mov_b64 v[" #R "+2:" #R "+3], 0\n\t"
#define MAX2(i) "v_max3_f32 v14, v14, v[116+" #i "], v[117+" #i "]\n\tv_max3_f32 v22, v22, v[132+" #i "], v[133+" #i "]\n\t"
#define PRIO(n, L) "s_cmp_ge_u32 %[psw], 4\n\ts_cbranch_scc1 " #L "f\n\ts_setprio " #n "\n\t" #L ":\n\t"

__global__ __launch_bounds__(768, 3) void gated_mix_kernel(const u16* __restrict__ x, const u16* __restrict__ wA, const u16* __restrict__ wB,
                                                            const u16* __restrict__ woA, const u16* __restrict__ woB,
                                                            const u16* __restrict__ gate_w, char* __restrict__ ws, u16* __restrict__ out) {
    __shared__ __attribute__((aligned(16))) char smem[KV_HEAD];
    lds_ptr lds = (lds_ptr)smem;
    const int tid = threadIdx.x, lane = tid & 63, wid = __builtin_amdgcn_readfirstlane(tid >> 6);
    const int g = lane >> 4, r16 = lane & 15;
    const rsrc_t wrs = __builtin_amdgcn_make_buffer_rsrc(ws, 0, (int)WS_END, 0x00020000);
    SPAN_BEGIN();
    STAMP_DECL STAMP(0, 0);
    const int xcd = blockIdx.x & 7, idx = blockIdx.x >> 3;
    const int mt = (idx & 7) * 2 + (xcd & 1), nt = (xcd >> 1) + (idx >> 3) * 4, m0 = mt * 128;
    if (wid == 8) *(LDS_AS u32x2*)(lds + L_CNT) = u32x2{0u, 0u};
    if (wid >= 8 && nt < 15) {
        const int lw = wid - 8;
        const unsigned xoff = (lw * 8 + (lane >> 3)) * (DM * 2) + (((lane & 7) ^ ((lane >> 3) & 7)) << 4);
        const unsigned woff = (32 * (g >> 1) + 4 * (g & 1) + lw) * 3840 + r16 * 16;
        const char* xs = (const char*)(x + (size_t)m0 * DM);
        const char* bs = (const char*)(wB + nt * 128);
        const char* as = (const char*)(wA + nt * 128);
        unsigned so = (unsigned)(size_t)lds;
#pragma unroll 1
        for (int t = -2; t < 9; ++t) {
            if (t >= 0) {
                WAIT_V(12);
                if (t == 0) STAMP(0, 1);
                RAW_BAR();
            }
            if (t < 8) {
#pragma unroll 1
                for (int i = 0; i < 4; ++i) {
                    const unsigned lx = so + (lw + 4 * i) * 1024, lb = so + 16384 + (lw + 4 * i) * K1_WP;
                    glds16x3(xs + i * (32 * DM * 2), xoff, lx, bs + i * (8 * 3840), as + i * (8 * 3840), woff, lb, lb + 16 * K1_WP);
                }
                xs += 128; bs += 64 * 3840; as += 64 * 3840;
            }
            so = so == (unsigned)(size_t)lds + 2 * K1_STAGE ? (unsigned)(size_t)lds : so + K1_STAGE;
        }
        WAIT_V(0);
        STAMP(0, 2);
        RAW_BAR();
    }
    const int head = (blockIdx.x & 7) * 4 + ((blockIdx.x >> 3) & 3), qb = blockIdx.x >> 5;
    const int omt = head, ont = qb;
    const int r32 = lane & 31, hh = lane >> 5;
    const u32x4 vfill = r32 == 20 ? u32x4{ONE2, ONE2, ONE2, ONE2} : u32x4{0u, 0u, 0u, 0u};
    const int voff = hh * 320 + (r32 < 20 ? r32 : 0) * 16;
    const unsigned olane = (r32 >> 4) * (int)O_BLK + (r32 & 15) * 16;
    const int ps = nt < 5 ? (int)Q_TILE : (int)KV_TILE;
    const int wimg = (int)WS_WO + ont * 5120;
    gu64* const f1 = (gu64*)(ws + WS_F1);
    constexpr int P_OFF = 102400;
    const int pslot = g * 256;
    if (nt == 15) {
        const int task = mt * 10 + wid, ks = task >> 2, cg = task & 3;
        lds_ptr st = lds + wid * 10240;
        if (wid < 10) {
            const char* wsrc = (const char*)((ks >= 20 ? woB : woA) + cg * 160);
#pragma unroll
            for (int i = 0; i < 10; ++i) {
                const int ci = i * 64 + lane, slot = ci / 20, cc = ci - slot * 20;
                const int c = (ks >= 20 ? ks - 20 : ks) * 4 + (slot >> 3), e = slot & 7, p = c / 5, r = c - p * 5;
                const int k = r < 4 ? (2 * p + (r >> 1)) * 20 + 4 * (r & 1) + (e & 3) + 8 * (e >> 2) : (2 * p + (e >> 2)) * 20 + 16 + (e & 3);
                glds16(wsrc, k * (DM * 2) + cc * 16, st + i * 1024);
            }
        }
        u32x4 gv[2];
        gv[0] = *(const u32x4*)(gate_w + lane * 8);
        gv[1] = lane < 16 ? *(const u32x4*)(gate_w + 512 + lane * 8) : u32x4{0u, 0u, 0u, 0u};
        float s[11];
#pragma unroll
        for (int u = 0; u < 11; ++u) {
            const int row = wid + 12 * u < 128 ? wid + 12 * u : wid;
            const u16* xr = x + (size_t)(m0 + row) * DM;
            const u32x4 xv[2] = {*(const u32x4*)(xr + lane * 8), *(const u32x4*)(xr + 512 + r16 * 8)};
            float a = 0.f;
#pragma unroll
            for (int c = 0; c < 2; ++c)
#pragma unroll
                for (int e = 0; e < 4; ++e) {
                    a = fmaf(__uint_as_float(xv[c][e] << 16), __uint_as_float(gv[c][e] << 16), a);
                    a = fmaf(__uint_as_float(xv[c][e] & 0xffff0000u), __uint_as_float(gv[c][e] & 0xffff0000u), a);
                }
            s[u] = a;
        }
#pragma unroll
        for (int o = 32; o; o >>= 1)
#pragma unroll
            for (int u = 0; u < 11; ++u) s[u] += __shfl_xor(s[u], o);
        float z = s[0];
#pragma unroll
        for (int u = 1; u < 11; ++u) z = lane == u ? s[u] : z;
        if (lane < 11 && wid + 12 * lane < 128) ST4(__float_as_uint(1.f / (1.f + __expf(-z))), (wid + 12 * lane) * 4, (int)WS_G + m0 * 4);
        WAIT_V(0);
        if (wid < 10) {
            lds_ptr rp = st + (8 * g + (r16 >> 2)) * 320 + (lane & 3) * 8;
#pragma unroll
            for (int j = 0; j < 10; ++j)
                ST16(__builtin_bit_cast(u32x4, cat8(lds_tr(rp + j * 32), lds_tr(rp + j * 32 + 4 * 320))), lane * 16, (int)WS_WO + (ks * 40 + cg * 10 + j) * 1024);
        }
        STAMP(0, 4);
    }
    if (wid >= 8) {
        u64 tag = __hip_atomic_load((gu64*)(ws + WS_EPOCH), RLX_AGENT) + 1;
        f32x16 c0, c1;
        {
            const unsigned long long msk = ~(0x15554ull << r32) >> (4 * hh);
            const unsigned mlo = (unsigned)msk, mhi = (unsigned)(msk >> 32);
#pragma unroll
            for (int r = 0; r < 16; ++r) {
                const int u = (r & 3) + 8 * (r >> 2);
                c0[r] = r < 8 ? 0.f : __uint_as_float(__builtin_amdgcn_sbfe(mlo, (u - 16) & 31, 1) & 0xff800000u);
                c1[r] = __uint_as_float(__builtin_amdgcn_sbfe(u < 16 ? mlo : mhi, (u + 16) & 31, 1) & 0xff800000u);
            }
        }
        asm volatile("" : "+v"(c0), "+v"(c1));
        const int hw = wid - 8;
        __builtin_amdgcn_s_setprio(3);
        STAMP(0, 3);
        RAW_BAR();
        STAMP(1, 0);
        const int fc = head * 20 + (g & 1) * 19;
        gu64* const fkv = f1 + (((g < 2 ? 640 : 1280) + fc) >> 7) * 16 * FS;
        auto go = [&](int n, gu64* w0) {
            volatile LDS_AS int* gw = (volatile LDS_AS int*)(lds + L_CNT + 4);
            if (hw == 3) {
                gu64* const w[1] = {w0};
                wait_flags<POLL1>(w, tag);
                *gw = n;
            } else {
#pragma unroll 1
                while (__builtin_amdgcn_readfirstlane(*gw) < n) __builtin_amdgcn_s_sleep(1);
                asm volatile("" ::: "memory");
            }
        };
        go(1, r16 < 3 ? fkv + r16 * FS : f1 + ((fc >> 7) * 16 + qb * 2 + (lane & 1)) * FS);
        STAMP(1, 1);
        const char* src = ws + WS_KV + (size_t)(NH + head) * KV_HEAD + hw * 1024;
#define LOADS(a, b) _Pragma("unroll 1") for (int i = a; i < b; ++i) glds16_dev(src + i * 4096, lane * 16, lds + hw * 1024 + i * 4096)
#pragma unroll 1
        for (int p = hw; p < 10; p += 4) glds16_dev(ws + WS_Q + (size_t)((NH + head) * 64 + qb * 8) * Q_TILE + p * 1024, lane * 16, lds + L_Q + p * 1024);
        LOADS(0, 7);
        WAIT_V(0); RAW_BAR();
        go(2, fkv + r16 * FS);
        LOADS(7, 28);
        WAIT_V(16); RAW_BAR();
        *(LDS_AS int*)lds = 0;
        if (nt < 15) {
            lds_ptr sp = lds + L_STASH + hw * 9216 + lane * 16;
            const u32x4 po = *(const LDS_AS u32x4*)(sp + 8192);
#pragma unroll
            for (int j = 0; j < 4; ++j) {
                const u32x4 pv[2] = {*(const LDS_AS u32x4*)(sp + j * 2048), *(const LDS_AS u32x4*)(sp + j * 2048 + 1024)};
                put_qkv(pv, po[j] & ~7u, (po[j] & ~7u) + (po[j] & 7u) * 128, ps, wrs);
            }
        }
        WAIT_V(0);
        STAMP(1, 6);
        RAW_BAR();
        if (hw == 0 && lane == 0) __hip_atomic_store(f1 + (256 + nt * 16 + mt) * FS, tag, RLX_AGENT);
        LOADS(28, 40);
#pragma unroll
        for (int i = 0; i < 2; ++i)
            put_o(u32x4{~0u, ~0u, ~0u, ~0u}, u32x2{~0u, ~0u}, wrs, olane, (int)WS_O + (128 + (qb * 8 + hw * 2 + i) * 2) * (int)O_BLK, head, hh);
        {
            const int sec = r16 >> 2, fm = max(qb * 2 - 1 + (r16 & 3), 0);
            gu64* const w[1] = {f1 + (256 + (g < 2 && sec < 3 && (r16 & 3) < 3 ? ((sec * 640 + fc) >> 7) * 16 + fm : 240 + r16)) * FS};
            wait_flags<8>(w, tag);
        }
        STAMP(1, 2);
        if (!(ABL & 1)) {
            const int q0 = qb * 8 + hw * 2;
            const int qA = (int)WS_Q + (head * 64 + q0) * (int)Q_TILE, k1 = (int)WS_KV + (head * 64 + q0) * (int)KV_TILE;
            u32x4 aqa[2], ka[3], vf[5];
            u32x2 aqb[2] = {{0u, 0u}, {0u, 0u}}, kb[3];
            float gv[2];
#pragma unroll
            for (int i = 0; i < 3; ++i) {
                const int kt = k1 + (i == 0 ? (q0 ? -(int)KV_TILE : 0) : (i - 1) * (int)KV_TILE);
                ka[i] = LD16(lane * 16, kt);
                kb[i] = LD8(r32 * 8, kt + 1024);
            }
#pragma unroll
            for (int i = 0; i < 2; ++i) aqa[i] = LD16(lane * 16, qA + i * (int)Q_TILE);
            if (!hh) {
#pragma unroll
                for (int i = 0; i < 2; ++i) aqb[i] = LD8(r32 * 8, qA + i * (int)Q_TILE + 1024);
            }
#pragma unroll
            for (int i = 0; i < 5; ++i) vf[i] = vfill;
            if (r32 < 20) {
                vf[0] = LD16(voff, k1 + (q0 ? -(int)KV_TILE : 0) + 1920);
#pragma unroll
                for (int i = 1; i < 5; ++i) vf[i] = LD16(voff, k1 + ((i - 1) >> 1) * (int)KV_TILE + 1280 + ((i - 1) & 1) * 640);
            }
#pragma unroll
            for (int i = 0; i < 2; ++i) gv[i] = __uint_as_float(LD4(r32 * 4, (int)WS_G + (q0 + i) * 128));
#pragma unroll
            for (int i = 1; i >= 0; --i) {
                if (i == 0 && q0 == 0) {
#pragma unroll
                    for (int r = 8; r < 16; ++r) c0[r] = -INFINITY;
                }
                f32x16 s0 = MFMA32(BF8(ka[i]), BF8(aqa[i]), c0);
                f32x16 s1 = MFMA32(BF8(ka[i + 1]), BF8(aqa[i]), c1);
                s0 = MFMA32H(kb[i], aqb[i], s0);
                s1 = MFMA32H(kb[i + 1], aqb[i], s1);
                float m = max3f(s0[8], s0[9], s0[10]);
                m = max3f(m, s0[11], s0[12]); m = max3f(m, s0[13], s0[14]);
                m = xhalf_max(max16(s1, fmaxf(m, s0[15])));
                const u32x2 ref = with_ref(u32x2{0u, 0u}, m, hh);
                s0 = MFMA32H((u32x2{ONE2, ONE2}), ref, s0);
                s1 = MFMA32H((u32x2{ONE2, ONE2}), ref, s1);
#pragma unroll
                for (int r = 8; r < 16; ++r) s0[r] = __builtin_amdgcn_exp2f(s0[r]);
#pragma unroll
                for (int r = 0; r < 16; ++r) s1[r] = __builtin_amdgcn_exp2f(s1[r]);
                f32x16 o = SMFMAC(BF8(vf[2 * i]), HI8(s0), f32x16{});
                o = SMFMAC(BF8(vf[2 * i + 1]), LO8(s1), o);
                o = SMFMAC(BF8(vf[2 * i + 2]), HI8(s1), o);
                const float f = gv[i] * __builtin_amdgcn_rcpf(row_sum(o));
                put_o(u32x4{pack2(o[0] * f, o[1] * f), pack2(o[2] * f, o[3] * f), pack2(o[4] * f, o[5] * f), pack2(o[6] * f, o[7] * f)},
                      u32x2{pack2(o[8] * f, o[9] * f), pack2(o[10] * f, o[11] * f)}, wrs, olane, (int)WS_O + (q0 + i) * 2 * (int)O_BLK, head, hh);
            }
        }
        WAIT_V(0);
        if (lane == 0 && __hip_atomic_fetch_add((LDS_AS int*)lds, 1, __ATOMIC_RELAXED, __HIP_MEMORY_SCOPE_WORKGROUP) == 3)
            __hip_atomic_store((gu64*)(ws + WS_FA) + (qb * 32 + head) * FS, tag, RLX_AGENT);
        STAMP(1, 3);
        f32x4 acc[2][5];
        bf16x8 of[3][2], wf[3][5];
        const int osrc = (int)WS_O + (omt * 4 + (hw & 1) * 2) * (int)O_BLK + (hw >> 1) * 10240, wsrc = wimg + (hw >> 1) * 409600;
        auto ldo = [&](int ks) {
#pragma unroll
            for (int i = 0; i < 2; ++i) of[ks % 3][i] = BF8(LD16(lane * 16, osrc + i * (int)O_BLK + ks * 1024));
        };
        auto ldw = [&](int ks) {
#pragma unroll
            for (int j = 0; j < 5; ++j) wf[ks % 3][j] = BF8(LD16(lane * 16, wsrc + ks * 40960 + j * 1024));
        };
        ldw(0); ldw(1); ldw(2);
        SB();
        RAW_BAR();
        {
            gu64* const w[1] = {(gu64*)(ws + WS_FA) + ((omt >> 2) * 32 + r32) * FS};
            wait_flags<8>(w, tag);
        }
        ldo(0); ldo(1); ldo(2);
        SB();
#pragma unroll
        for (int ks = 0; ks < 10; ++ks) {
#pragma unroll
            for (int j = 0; j < 5; ++j) {
#pragma unroll
                for (int i = 0; i < 2; ++i) acc[i][j] = MFMA16(wf[ks % 3][j], of[ks % 3][i], ks ? acc[i][j] : (f32x4{0.f, 0.f, 0.f, 0.f}));
                SB();
                if (THIN) __builtin_amdgcn_s_sleep(THIN);
                SB();
            }
            if (ks < 7) { ldo(ks + 3); ldw(ks + 3); SB(); }
            if (ks == W_AT) {
#define WLOAD(ks) _Pragma("unroll") for (int j = 0; j < 5; ++j) glds16_dev(ws + wimg + (20 + (ks)) * 40960 + j * 1024, lane * 16, lds + (ks) * 5120 + j * 1024)
#pragma unroll 1
                for (int i = 0; i < 5; ++i) WLOAD(hw * 5 + i);
            }
            if (ks == G6_AT) {
                STAMP(1, 4);
                RAW_BAR();
            }
        }
        if (blockIdx.x == 0 && hw == 0) {
            gu64* const w[4] = {f1 + (256 + lane) * FS, f1 + (320 + lane) * FS, f1 + (384 + lane) * FS, f1 + (448 + lane) * FS};
            wait_flags<8>(w, tag);
            if (lane == 0) __hip_atomic_store((gu64*)(ws + WS_EPOCH), tag, RLX_AGENT);
        }
        WAIT_V(0);
        STAMP(1, 5);
        auto put = [&]() {
#pragma unroll
            for (int i = 0; i < 2; ++i)
#pragma unroll
                for (int j = 0; j < 5; ++j)
                    *(LDS_AS f32x4*)(lds + P_OFF + (1 + (hw >> 1)) * 20480 + (((hw & 1) * 2 + i) * 5 + j) * 1024 + pslot + ((r16 + 2 * j + (g >> 1)) & 15) * 16) = acc[i][j];
        };
        RAW_BAR();
        if (hw >= 2) put();
        RAW_BAR();
        if (hw < 2) put();
    } else {
        if (nt < 15) {
            const u64 tv = __hip_atomic_load((gu64*)(ws + WS_EPOCH), RLX_AGENT) + 1;
            const char* tp;
            if (lane < 16) {
                tp = (const char*)((lane & 2 ? wA : wB) + nt * 128 + (lane & 1) * 64) + (size_t)((wid + 2) * 64 + mt * 4 + (lane >> 2)) * 3840;
            } else {
                const int r = nt * 9 + (lane < 25 ? lane - 16 : 0);
                tp = (const char*)(x + (size_t)(m0 + (r < 127 ? r : 127)) * DM) + (wid + 2) * 128;
            }
            unsigned d0 = 0;
            const int br = wid < 4, wm = (wid >> 1) & 1, wn = wid & 1;
            const int xa = (wm * 64 + r16) * 128 + ((g ^ (lane & 7)) << 4);
            const int wa = 16384 + ((wid >> 2) * 16 + 4 * g + (r16 >> 2)) * K1_WP + (wn * 64 + 4 * (lane & 3)) * 2;
            const int tile0 = mt * 4 + wm * 2, cw = nt * 128 + wn * 64;
            unsigned poff[4], poff1[4];
#pragma unroll
            for (int j = 0; j < 4; ++j) {
                if (nt < 10) {
                    const int cp = (cw - (nt < 5 ? 0 : 640)) / 8 + j * 2 + (g >> 1), p = cp / 5, r = cp - p * 5;
                    const unsigned hoff = (int)(nt < 5 ? WS_Q : WS_KV) + ((br * NH + 2 * p) * 64 + tile0) * ps;
                    poff[j] = r == 2 ? hoff + (g & 1) * 64 * ps + 1024 + r16 * 8 : hoff + (r > 2 ? 64 * ps + (r - 3) * 512 : r * 512) + r16 * 16 + (g & 1) * 8;
                    poff1[j] = poff[j] + (r == 2 ? 128 : 256);
                } else {
                    const int c = cw - 1280 + j * 16 + r16, h = c / 20, d = c - h * 20;
                    poff[j] = (int)WS_KV + ((br * NH + h) * 64 + tile0) * (int)KV_TILE + 1280 + (g >> 1) * 320 + d * 16 + (g & 1) * 8;
                    poff1[j] = poff[j] + 640;
                }
            }
            asm volatile("" : "+v"(poff[0]), "+v"(poff[1]), "+v"(poff[2]), "+v"(poff[3]), "+v"(poff1[0]), "+v"(poff1[1]), "+v"(poff1[2]), "+v"(poff1[3]));
            const u64 tag = (u64)__builtin_amdgcn_readfirstlane((unsigned)(tv >> 32)) << 32 | (unsigned)__builtin_amdgcn_readfirstlane((unsigned)tv);
            f32x4 acc[4][4];
#pragma unroll
            for (int i = 0; i < 4; ++i)
#pragma unroll
                for (int j = 0; j < 4; ++j) acc[i][j] = f32x4{0.f, 0.f, 0.f, 0.f};
            if (br) __builtin_amdgcn_s_setprio(1);
            if (nt < 10) k1_main<false>(acc, lds, xa, wa, tp, wid + 2 - K1_PF, d0); else k1_main<true>(acc, lds, xa, wa, tp, wid + 2 - K1_PF, d0);
            STAMP(0, 1);
            PF_DONE();
            const bool urgent = br && (nt < 5 || mt < 3);
            if (!urgent) RAW_BAR();
            if (br && !urgent) __builtin_amdgcn_s_sleep(B_DELAY);
            if (nt < 5) {
#pragma unroll
                for (int i = 0; i < 4; ++i)
#pragma unroll
                    for (int j = 0; j < 4; ++j) acc[i][j] *= QSCALE;
            }
            auto packed = [&](int i, int j) {
                const f32x4 a = acc[2 * i][j], b = acc[2 * i + 1][j];
                return u32x4{pack2(a[0], a[1]), pack2(a[2], a[3]), pack2(b[0], b[1]), pack2(b[2], b[3])};
            };
            if (br) {
#pragma unroll
                for (int j = 0; j < 4; ++j) {
                    const u32x4 pv[2] = {packed(0, j), packed(1, j)};
                    put_qkv(pv, poff[j], poff1[j], ps, wrs);
                }
            } else {
#pragma unroll
                for (int j = 0; j < 4; ++j)
#pragma unroll
                    for (int i = 0; i < 2; ++i) *(LDS_AS u32x4*)(lds + L_STASH + (wid - 4) * 9216 + j * 2048 + i * 1024 + lane * 16) = packed(i, j);
            }
            STAMP(0, 2);
            if (urgent) RAW_BAR();
            if (br) {
                WAIT_V(0);
                if (lane == 0 && __hip_atomic_fetch_add((LDS_AS int*)(lds + L_CNT), 1, __ATOMIC_RELAXED, __HIP_MEMORY_SCOPE_WORKGROUP) == 3)
                    __hip_atomic_store(f1 + (nt * 16 + mt) * FS, tag, RLX_AGENT);
            } else {
                *(LDS_AS u32x4*)(lds + L_STASH + (wid - 4) * 9216 + 8192 + lane * 16) =
                    u32x4{poff[0] + ((poff1[0] - poff[0]) >> 7), poff[1] + ((poff1[1] - poff[1]) >> 7), poff[2] + ((poff1[2] - poff[2]) >> 7), poff[3] + ((poff1[3] - poff[3]) >> 7)};
            }
            STAMP(0, 3);
        } else {
            RAW_BAR();
            WAIT_V(0);
        }
        const int qt = qb * 8 + wid;
        const unsigned long long vmask = 0x000fffff000fffffull;
        f32x16 o, o2;
        float gate;
        u32x4 qa0, vlo = vfill, vhi = vfill;
        u32x2 qb0;
        const unsigned l0 = (unsigned)(size_t)lds, kap = (wid >> 2) * (int)KV_TILE;
        unsigned a_ka = l0 + kap + lane * 16, a_kb = l0 + kap + r32 * 8, a_v = l0 + kap + voff;
        const unsigned qs0 = L_Q + wid * (int)Q_TILE - kap, qs1 = L_Q + (wid ^ 4) * (int)Q_TILE - kap;
        int nbar = 0x53, it = 7;
        const int psw = PRIO_SW ? wid : 4;
        __builtin_amdgcn_s_setprio(1);
        asm volatile(
            "v_mov_b32 v28, %[one]\n\tv_mov_b32 v29, %[one]\n\tv_mov_b32 v30, %[one]\n\tv_mov_b32 v31, %[one]\n\tv_mov_b32 v40, %[sidx]\n\t"
            ZERO4(72) ZERO4(76) ZERO4(80) ZERO4(84) ZERO4(88) ZERO4(92) ZERO4(96) ZERO4(100) ZERO4(104) ZERO4(108)
            "v_mov_b64 v[12:13], 0\n\tv_mov_b64 v[20:21], 0\n\t"
            "v_add_u32 v48, %[qs0], v41\n\tv_add_u32 v49, %[qs1], v41\n\tv_subrev_u32 v50, %[kap], v41\n\t"
            "v_add_u32 v51, %[qs0], v42\n\tv_add_u32 v52, %[qs1], v42\n\tv_subrev_u32 v53, %[kap], v42\n\t"
            "s_waitcnt lgkmcnt(0)\n\ts_barrier\n\t"
            "ds_read_b128 v[112:115], v50\n\tds_read_b128 v[8:11], v48\n\tds_read_b128 v[16:19], v49\n\t"
            "s_mov_b64 exec, %[km]\n\t"
            "ds_read_b64 v[30:31], v53 offset:1024\n\tds_read_b64 v[12:13], v51 offset:1024\n\tds_read_b64 v[20:21], v52 offset:1024\n\t"
            "ds_read_b64 v[28:29], v42 offset:1024\n\t"
            "s_mov_b64 exec, -1\n\t"
            "ds_read_b128 v[24:27], v41\n\t"
            "s_waitcnt lgkmcnt(5)\n\t"
            "v_mfma_f32_32x32x16_bf16 v[116:131], v[112:115], v[8:11], 0\n\t"
            "v_mfma_f32_32x32x16_bf16 v[132:147], v[112:115], v[16:19], 0\n\t"
            "s_waitcnt lgkmcnt(0)\n\t"
            QK1(116, 8, 30) QK1(132, 16, 30) QK0(48, 8)
            "s_nop 10\n\t"
            "v_max_f32 v14, v116, v117\n\tv_max_f32 v22, v132, v133\n\t"
            MAX2(2) MAX2(4) MAX2(6) MAX2(8) MAX2(10) MAX2(12) MAX2(14)
            "v_mov_b32 v15, v14\n\tv_mov_b32 v23, v22\n\ts_nop 1\n\t"
            "v_permlane32_swap_b32 v14, v15\n\tv_permlane32_swap_b32 v22, v23\n\t"
            "s_not_b64 exec, %[km]\n\t"
            "v_min_f32 v14, -v14, -v15\n\tv_min_f32 v22, -v22, -v23\n\t"
            "v_and_b32 v12, 0xffff0000, v14\n\tv_and_b32 v20, 0xffff0000, v22\n\t"
            "s_mov_b64 exec, -1\n\ts_nop 1\n\t"
            QK1(48, 8, 28)
            "s_nop 5\n\t"
            "1:\n\t"
            PRIO(2, 3) PAIR(0, 28, 30, "") PAIR(1, 30, 28, "") PAIR(2, 28, 30, PRIO(0, 4)) PAIR(3, 30, 28, "")
            "v_add_u32 v41, 20480, v41\n\tv_add_u32 v42, 20480, v42\n\tv_add_u32 v43, 20480, v43\n\t"
            "s_bitcmp0_b32 %[nbar], 0\n\t"
            "s_cbranch_scc1 2f\n\t"
            "s_barrier\n\t"
            "2:\n\t"
            "s_lshr_b32 %[nbar], %[nbar], 1\n\t"
            "s_sub_i32 %[it], %[it], 1\n\t"
            "s_cmp_lg_u32 %[it], 0\n\t"
            "s_cbranch_scc1 1b\n\t"
            "global_load_dword v44, %[goff], %[gb] sc1\n\t"
            PRIO(2, 3) PAIR(0, 28, 30, "") PAIR(1, 30, 28, "") PAIR(2, 28, 30, PRIO(0, 4)) PAIR_LAST(3, 30)
            "s_waitcnt vmcnt(0)"
            : "=&{v44}"(gate), "=&{v[80:95]}"(o), "=&{v[96:111]}"(o2), "=&{v[8:11]}"(qa0), "=&{v[12:13]}"(qb0), "+{v[32:35]}"(vlo),
              "+{v[36:39]}"(vhi), "+{v41}"(a_ka), "+{v42}"(a_kb), "+{v43}"(a_v), [nbar] "+s"(nbar), [it] "+s"(it)
            : [vm] "s"(vmask), [km] "s"(0xffffffffull), [one] "s"(ONE2), [sidx] "s"(0xDDDDDDDDu), [psw] "s"(psw), [goff] "v"(r32 * 4),
              [gb] "s"(ws + WS_G + qt * 128), [qs0] "s"(qs0), [qs1] "s"(qs1), [kap] "s"(kap)
            : "v14", "v15", "v16", "v17", "v18", "v19", "v20", "v21", "v22", "v23", "v24", "v25", "v26", "v27", "v28", "v29", "v30",
              "v31", "v40", "v48", "v49", "v50", "v51", "v52", "v53", "v54", "v55", "v56", "v57", "v58", "v59", "v60", "v61", "v62",
              "v63", "v64", "v65", "v66", "v67", "v68", "v69", "v70", "v71", "v72", "v73", "v74", "v75", "v76", "v77", "v78", "v79",
              "v112", "v113", "v114", "v115", "v116", "v117", "v118", "v119", "v120", "v121", "v122", "v123", "v124", "v125", "v126",
              "v127", "v128", "v129", "v130", "v131", "v132", "v133", "v134", "v135", "v136", "v137", "v138", "v139", "v140", "v141",
              "v142", "v143", "v144", "v145", "v146", "v147", "scc", "memory");
        STAMP(1, 2);
        __builtin_amdgcn_s_setprio(3);
#pragma unroll
        for (int i = 0; i < 3; ++i)
            *(LDS_AS f32x4*)(lds + P_OFF + (wid ^ 4) * 3072 + i * 1024 + lane * 16) = f32x4{o2[4 * i], o2[4 * i + 1], o2[4 * i + 2], o2[4 * i + 3]};
        RAW_BAR();
#pragma unroll
        for (int i = 0; i < 3; ++i) {
            const f32x4 t = *(const LDS_AS f32x4*)(lds + P_OFF + wid * 3072 + i * 1024 + lane * 16);
#pragma unroll
            for (int e = 0; e < 4; ++e) o[4 * i + e] += t[e];
        }
        const float gB = 1.f - gate;
        float l = row_sum(o);
        float amax = hh ? 0.f : fmaxf(fmaxf(fabsf(o[8]), fabsf(o[9])), fmaxf(fabsf(o[10]), fabsf(o[11])));
#pragma unroll
        for (int i = 0; i < 8; ++i) amax = fmaxf(amax, fabsf(o[i]));
        if (FORCE_FALLBACK || !__all(l >= 0x1p-64f && l <= 0x1p64f && amax <= 0x1p126f)) {
            const int kv = (int)WS_KV + (NH + head) * (int)KV_HEAD;
            u32x2 qr = hh ? u32x2{0u, 0u} : qb0;
            float mt2 = -INFINITY;
            o = f32x16{};
#pragma unroll 1
            for (int t = 0; t < 128; ++t) {
                const int kt = kv + (t & 63) * (int)KV_TILE;
                u32x2 kb = {ONE2, ONE2};
                if (!hh) kb = LD8(r32 * 8, kt + 1024);
                f32x16 sc = MFMA32(BF8(LD16(lane * 16, kt)), BF8(qa0), f32x16{});
                sc = MFMA32H(kb, qr, sc);
                if (t < 64) {
                    mt2 = max16(sc, mt2);
                    if (t == 63) qr = with_ref(qb0, xhalf_max(mt2), hh);
                } else {
                    u32x4 v0 = vfill, v1 = vfill;
                    if (r32 < 20) { v0 = LD16(voff, kt + 1280); v1 = LD16(voff, kt + 1920); }
#pragma unroll
                    for (int r = 0; r < 16; ++r) sc[r] = __builtin_amdgcn_exp2f(sc[r]);
                    o = SMFMAC(BF8(v0), LO8(sc), o);
                    o = SMFMAC(BF8(v1), HI8(sc), o);
                }
            }
            l = row_sum(o);
        }
        store_o(o, gB * __builtin_amdgcn_rcpf(l), wrs, olane, (int)WS_O + (128 + qt * 2) * (int)O_BLK, head, hh);
        STAMP(1, 3);
        if (TAILW == 1) WAIT_V(0);
        RAW_BAR();
        if (TAILW == 2) WAIT_V(0);
        if (TAILW > 2) __builtin_amdgcn_s_sleep(TAILW);
        const int mb = wid & 3, kh = wid >> 2;
        const int osrc = (int)WS_O + (128 + omt * 4 + mb) * (int)O_BLK + kh * 10240;
        u32x4 of[10];
#pragma unroll
        for (int s = 0; s < 10; ++s) of[s] = LD16(lane * 16, osrc + s * 1024);
        SB();
        f32x4 acc[5];
        lds_ptr pp = lds + P_OFF + kh * 40960 + mb * 5120 + pslot;
#pragma unroll
        for (int j = 0; j < 5; ++j) acc[j] = f32x4{0.f, 0.f, 0.f, 0.f};
        if (kh) {
#pragma unroll
            for (int j = 0; j < 5; ++j) acc[j] = *(const LDS_AS f32x4*)(pp + j * 1024 + ((r16 + 2 * j + (g >> 1)) & 15) * 16);
        }
        lds_ptr wp = lds + kh * 51200 + lane * 16;
        bf16x8 wf[2][10];
#pragma unroll
        for (int j = 0; j < 10; ++j) wf[0][j] = lds_b128(wp + j * 1024);
        auto bad = [&](int s) {
            const u32x4 a = __builtin_elementwise_max(of[s], of[s + 1]);
            return __any(max(max(a[0], a[1]), max(a[2], a[3])) == ~0u);
        };
#pragma unroll
        for (int s = 0; s < 10; s += 2) {
            if (s < 8) {
#pragma unroll
                for (int j = 0; j < 10; ++j) wf[(s / 2 + 1) & 1][j] = lds_b128(wp + (s + 2) * 5120 + j * 1024);
            }
            if (bad(s)) {
#pragma unroll 1
                for (unsigned spins = 0; spins < (1u << 20); ++spins) {
                    __builtin_amdgcn_s_sleep(RETRY);
                    asm volatile("" ::: "memory");
#pragma unroll
                    for (int t = 8; t >= s; t -= 2)
                        if (t == s || bad(t)) { of[t] = LD16(lane * 16, osrc + t * 1024); of[t + 1] = LD16(lane * 16, osrc + (t + 1) * 1024); }
                    if (!bad(s)) break;
                }
            }
            if (s == 0) STAMP(1, 4);
            if (s == 8) STAMP(1, 6);
#pragma unroll
            for (int j = 0; j < 10; ++j) acc[j % 5] = MFMA16(wf[(s / 2) & 1][j], BF8(of[s + j / 5]), acc[j % 5]);
        }
        STAMP(1, 5);
#pragma unroll
        for (int j = 0; j < 5; ++j) *(LDS_AS f32x4*)(pp + j * 1024 + ((r16 + 2 * j + (g >> 1)) & 15) * 16) = acc[j];
    }
    RAW_BAR();
    if (tid < 640) {
        const int row = tid / 10, c = tid - row * 10;
        lds_ptr q = lds + P_OFF + ((row >> 4) * 5 + (c >> 1)) * 1024 + (c & 1) * 512 + (((row & 15) + c) & 15) * 16;
        f32x4 a = {0.f, 0.f, 0.f, 0.f}, b = a;
#pragma unroll
        for (int p = 0; p < 3; ++p) {
            a += *(const LDS_AS f32x4*)(q + p * 20480);
            b += *(const LDS_AS f32x4*)(q + p * 20480 + 256);
        }
        u32x4 v = {pack2(a[0], a[1]), pack2(a[2], a[3]), pack2(b[0], b[1]), pack2(b[2], b[3])};
        if (OUT_SC1) __builtin_amdgcn_raw_buffer_store_b128(v, __builtin_amdgcn_make_buffer_rsrc(out, 0, SEQ * DM * 2, 0x00020000), ((omt * 64 + row) * DM + ont * 80 + c * 8) * 2, 0, SC1);
        else *(u32x4*)(out + (size_t)(omt * 64 + row) * DM + ont * 80 + c * 8) = v;
    }
    SPAN_END();
    DIAG_WAIT();
    STAMP(1, 7); FLUSH(1);
}

extern "C" void kernel_launch(void* const* d_in, const int* in_sizes, int n_in, void* d_out, int out_size, void* d_ws, size_t ws_size,
                              hipStream_t stream) {
    gated_mix_kernel<<<256, 768, 0, stream>>>((const u16*)d_in[0], (const u16*)d_in[1], (const u16*)d_in[4], (const u16*)d_in[2],
                                              (const u16*)d_in[5], (const u16*)d_in[6], (char*)d_ws, (u16*)d_out);
}
```

```cpp
#include <hip/hip_runtime.h>
#include <hip/hip_bf16.h>

typedef __attribute__((ext_vector_type(8))) __bf16 bf16x8;
typedef __attribute__((ext_vector_type(16))) __bf16 bf16x16;
typedef __attribute__((ext_vector_type(2))) __bf16 bf16x2;
typedef __attribute__((ext_vector_type(4))) short s16x4;
typedef __attribute__((ext_vector_type(2))) float f32x2;
typedef __attribute__((ext_vector_type(4))) float f32x4;
typedef __attribute__((ext_vector_type(16))) float f32x16;
typedef __attribute__((ext_vector_type(2))) unsigned u32x2;
typedef __attribute__((ext_vector_type(4))) unsigned u32x4;
typedef unsigned short u16;
#define LDS_AS __attribute__((address_space(3)))
typedef LDS_AS char* lds_ptr;

constexpr int SEQ = 2048, DM = 640, NH = 32;
constexpr size_t KV_TILE = 2560, KV_HEAD = 64 * KV_TILE;
constexpr size_t Q_TILE = 1280, Q_HEAD = 64 * Q_TILE;
constexpr size_t WS_KV = 0;
constexpr size_t WS_Q = WS_KV + 2 * NH * KV_HEAD;
constexpr size_t O_BLK = 80 * 256;
constexpr size_t WS_O = WS_Q + 2 * NH * Q_HEAD;
constexpr size_t WS_WO = WS_O + 2 * 128 * O_BLK;
constexpr size_t WS_G = WS_WO + 40 * 40 * 1024;
constexpr size_t WS_F1 = WS_G + SEQ * 4;
constexpr int FS = 16;
constexpr size_t WS_FA = WS_F1 + 512 * 128;
constexpr size_t WS_EPOCH = WS_FA + 256 * 128;
constexpr size_t WS_END = WS_EPOCH + 128;
constexpr float QSCALE = 0.22360679774997896f * 1.4426950408889634f;

#define WAIT_V(n) asm volatile("s_waitcnt vmcnt(" #n ")" ::: "memory")
#define RAW_BAR() asm volatile("s_waitcnt lgkmcnt(0)\n\ts_barrier" ::: "memory")
#define MFMA16(a, b, c) __builtin_amdgcn_mfma_f32_16x16x32_bf16(a, b, c, 0, 0, 0)
#define MFMA32(a, b, c) __builtin_amdgcn_mfma_f32_32x32x16_bf16(a, b, c, 0, 0, 0)
#define MFMA32H(a, b, c) __builtin_amdgcn_mfma_f32_32x32x8bf16_1k(__builtin_bit_cast(s16x4, a), __builtin_bit_cast(s16x4, b), c, 0, 0, 0)

__device__ __forceinline__ unsigned pack2(float lo, float hi) {
    f32x2 v = {lo, hi};
    return __builtin_bit_cast(unsigned, __builtin_convertvector(v, bf16x2));
}
__device__ __forceinline__ void glds16(const char* sbase, unsigned voff, lds_ptr l) {
    unsigned keep;
    asm volatile("s_mov_b32 %0, m0\n\ts_mov_b32 m0, %3\n\ts_nop 0\n\tglobal_load_lds_dwordx4 %1, %2\n\ts_mov_b32 m0, %0"
                 : "=&s"(keep) : "v"(voff), "s"(sbase), "s"((unsigned)(size_t)l) : "memory");
}
__device__ __forceinline__ void glds16x3(const char* s0, unsigned v0, unsigned l0, const char* s1, const char* s2, unsigned v1, unsigned l1, unsigned l2) {
    unsigned keep;
    asm volatile("s_mov_b32 %0, m0\n\ts_mov_b32 m0, %3\n\ts_nop 0\n\tglobal_load_lds_dwordx4 %1, %2\n\t"
                 "s_mov_b32 m0, %7\n\ts_nop 0\n\tglobal_load_lds_dwordx4 %4, %5\n\t"
                 "s_mov_b32 m0, %8\n\ts_nop 0\n\tglobal_load_lds_dwordx4 %4, %6\n\ts_mov_b32 m0, %0"
                 : "=&s"(keep) : "v"(v0), "s"(s0), "s"(l0), "v"(v1), "s"(s1), "s"(s2), "s"(l1), "s"(l2) : "memory");
}
__device__ __forceinline__ bf16x8 lds_b128(lds_ptr p) { return *(const LDS_AS bf16x8*)p; }
__device__ __forceinline__ s16x4 lds_tr(lds_ptr p) { return __builtin_amdgcn_ds_read_tr16_b64_v4i16((LDS_AS s16x4*)p); }
__device__ __forceinline__ bf16x8 cat8(s16x4 a, s16x4 b) {
    return __builtin_bit_cast(bf16x8, __builtin_shufflevector(a, b, 0, 1, 2, 3, 4, 5, 6, 7));
}
typedef __amdgpu_buffer_rsrc_t rsrc_t;
typedef unsigned long long u64;
typedef __attribute__((address_space(1))) u64 gu64;
#define RLX_AGENT __ATOMIC_RELAXED, __HIP_MEMORY_SCOPE_AGENT
constexpr int SC1 = 16;
#define LD16(voff, soff) __builtin_amdgcn_raw_buffer_load_b128(wrs, voff, soff, SC1)
#define LD8(voff, soff) __builtin_amdgcn_raw_buffer_load_b64(wrs, voff, soff, SC1)
#define LD4(voff, soff) __builtin_amdgcn_raw_buffer_load_b32(wrs, voff, soff, SC1)
#define ST16(v, voff, soff) __builtin_amdgcn_raw_buffer_store_b128(v, wrs, (voff) + (soff), 0, SC1)
#define ST8(v, voff, soff) __builtin_amdgcn_raw_buffer_store_b64(v, wrs, voff, soff, SC1)
#define ST4(v, voff, soff) __builtin_amdgcn_raw_buffer_store_b32(v, wrs, voff, soff, SC1)
#define BF8(x) __builtin_bit_cast(bf16x8, x)
__device__ __forceinline__ void glds16_dev(const char* sbase, unsigned voff, lds_ptr l) {
    unsigned keep;
    asm volatile("s_mov_b32 %0, m0\n\ts_mov_b32 m0, %3\n\ts_nop 0\n\tglobal_load_lds_dwordx4 %1, %2 sc1\n\ts_mov_b32 m0, %0"
                 : "=&s"(keep) : "v"(voff), "s"(sbase), "s"((unsigned)(size_t)l) : "memory");
}
#define GL_(v, s, o) "global_load_lds_dwordx4 " v ", " s " offset:" #o " sc1\n\t"
__device__ __forceinline__ void glds_first(unsigned voff, const char* s0, unsigned l0, const char* s1, unsigned l1, const char* s2, unsigned l2) {
    unsigned keep;
    asm volatile("s_mov_b32 %0, m0\n\ts_mov_b32 m0, %3\n\ts_nop 0\n\t" GL_("%1", "%2", 0) GL_("%1", "%2", 1024) GL_("%1", "%2", 2048)
                 "s_mov_b32 m0, %5\n\ts_nop 0\n\t" GL_("%1", "%4", 0) GL_("%1", "%4", 1024) GL_("%1", "%4", 2048) GL_("%1", "%4", 3072)
                 "s_mov_b32 m0, %7\n\ts_nop 0\n\t" GL_("%1", "%6", 0) GL_("%1", "%6", 1024) GL_("%1", "%6", 2048)
                 "s_mov_b32 m0, %0"
                 : "=&s"(keep) : "v"(voff), "s"(s0), "s"(l0), "s"(s1), "s"(l1), "s"(s2), "s"(l2) : "memory");
}
__device__ __forceinline__ void arrive(lds_ptr cnt, gu64* flag, u64 tag) {
    unsigned r, sr;
    asm volatile("s_waitcnt vmcnt(0)\n\ts_mov_b64 exec, 1\n\tds_add_rtn_u32 %0, %2, %3\n\ts_waitcnt lgkmcnt(0)\n\t"
                 "v_readfirstlane_b32 %1, %0\n\ts_cmp_lg_u32 %1, 3\n\ts_cbranch_scc1 1f\n\t"
                 "global_store_dwordx2 %4, %5, %6 sc1\n\t1:\n\ts_mov_b64 exec, -1"
                 : "=&v"(r), "=&s"(sr) : "v"((unsigned)(size_t)cnt), "v"(1u), "v"(0u), "v"(tag), "s"(flag) : "scc", "memory");
}
template <int SLEEP, int N>
__device__ __forceinline__ void wait_flags(gu64* const (&w)[N], u64 tag) {
#pragma unroll 1
    for (unsigned spins = 0; spins < (1u << 18); ++spins) {
        bool ok = true;
#pragma unroll
        for (int i = 0; i < N; ++i) ok &= __hip_atomic_load(w[i], RLX_AGENT) == tag;
        if (__all(ok)) break;
        __builtin_amdgcn_s_sleep(SLEEP);
    }
    __builtin_amdgcn_fence(__ATOMIC_ACQUIRE, "wavefront");
}
constexpr int K1_WP = 1024 + 32;
constexpr int K1_STAGE = 16384 + 32 * K1_WP;
constexpr int L_Q = 112 * 1024, L_STASH = 122 * 1024, L_CNT = 163824;
#define SB() __builtin_amdgcn_sched_barrier(0)
__device__ __forceinline__ void put_qkv(const u32x4 (&pv)[2], unsigned off0, unsigned off1, int ps, rsrc_t wrs) {
#pragma unroll
    for (int i = 0; i < 2; ++i) {
        ST8((u32x2{pv[i][0], pv[i][1]}), off0, i * ps);
        ST8((u32x2{pv[i][2], pv[i][3]}), off1, i * ps);
    }
}

template <bool VT, class M, class F>
__device__ __forceinline__ void k1_main(f32x4 (&acc)[4][4], lds_ptr lds, int xa, int wa, M mid, F fin) {
    bf16x8 xf[2][4], wf[2][4];
    auto rd = [&](int so, int kk) {
#pragma unroll
        for (int j = 0; j < 4; ++j) {
            lds_ptr p = lds + so + wa + kk * 512 + j * 32;
            wf[kk][j] = cat8(lds_tr(p), lds_tr(p + 256));
        }
#pragma unroll
        for (int i = 0; i < 4; ++i) xf[kk][i] = lds_b128(lds + so + (xa ^ (kk * 64)) + i * 2048);
    };
    auto mm = [&](int kk) {
#pragma unroll
        for (int i = 0; i < 4; ++i)
#pragma unroll
            for (int j = 0; j < 4; ++j)
                acc[i][j] = VT ? MFMA16(xf[kk][i], wf[kk][j], acc[i][j]) : MFMA16(wf[kk][j], xf[kk][i], acc[i][j]);
    };
#define ILV() do { _Pragma("unroll") for (int q_ = 0; q_ < 12; ++q_) { __builtin_amdgcn_sched_group_barrier(0x100, 1, 0); __builtin_amdgcn_sched_group_barrier(0x8, 1, 0); } __builtin_amdgcn_sched_group_barrier(0x8, 4, 0); SB(); } while (0)
    RAW_BAR();
    rd(0, 0); SB();
    rd(0, 1);
    mm(0); ILV();
    int so = K1_STAGE;
#pragma unroll 1
    for (int t = 1; t < 10; ++t) {
        RAW_BAR();
        rd(so, 0);
        mm(1); ILV();
        rd(so, 1);
        mm(0); ILV();
        so = so == 2 * K1_STAGE ? 0 : so + K1_STAGE;
    }
    mid();
#pragma unroll
    for (int j = 0; j < 5; ++j) {
        if (j < 4) {
#pragma unroll
            for (int i = 0; i < 4; ++i) acc[i][j] = VT ? MFMA16(xf[1][i], wf[1][j], acc[i][j]) : MFMA16(wf[1][j], xf[1][i], acc[i][j]);
        }
        SB();
        if (j) fin(j - 1);
        SB();
    }
}

#define SMFMAC(a, b8, c) __builtin_amdgcn_smfmac_f32_32x32x32_bf16(a, __builtin_bit_cast(bf16x16, b8), c, (int)0xDDDDDDDD, 0, 0)
#define LO8(s) __builtin_shufflevector(s, s, 0, 1, 2, 3, 4, 5, 6, 7)
#define HI8(s) __builtin_shufflevector(s, s, 8, 9, 10, 11, 12, 13, 14, 15)
constexpr unsigned ONE2 = 0x3F803F80u;

__device__ __forceinline__ float xhalf_max(float m) {
    auto r = __builtin_amdgcn_permlane32_swap(__float_as_uint(m), __float_as_uint(m), false, false);
    return fmaxf(__uint_as_float(r[0]), __uint_as_float(r[1]));
}
__device__ __forceinline__ float max3f(float a, float b, float c) { return __builtin_fmaxf(__builtin_fmaxf(a, b), c); }
__device__ __forceinline__ float max16(const f32x16& s, float m) {
#pragma unroll
    for (int r = 0; r < 16; r += 2) m = max3f(m, s[r], s[r + 1]);
    return m;
}
__device__ __forceinline__ float row_sum(const f32x16& o) {
    auto r = __builtin_amdgcn_permlane32_swap(__float_as_uint(o[8]), __float_as_uint(o[8]), false, false);
    return __uint_as_float(r[1]);
}
__device__ __forceinline__ u32x2 with_ref(u32x2 q, float m, int hh) {
    const unsigned h1 = __float_as_uint(-m) & 0xffff0000u;
    const float r1 = -m - __uint_as_float(h1);
    const unsigned h2 = __float_as_uint(r1) & 0xffff0000u;
    const unsigned h3 = __float_as_uint(r1 - __uint_as_float(h2)) >> 16;
    return hh ? u32x2{h1 | (h2 >> 16), h3} : q;
}
__device__ __forceinline__ void put_o(u32x4 v, u32x2 t, rsrc_t wrs, unsigned voff, int soff, int head, int hh) {
    const int c0 = (head >> 1) * 5;
    ST16(v, voff + hh * 256, soff + (c0 + (head & 1) * 2) * 256);
    if (!hh) ST8(t, voff, soff + (c0 + 4) * 256 + (head & 1) * 8);
}
__device__ __forceinline__ unsigned pack_o(float a, float b) { return min(pack2(a, b), 0xFFFFFFFEu); }
__device__ __forceinline__ void store_o(const f32x16& o, float f, rsrc_t wrs, unsigned voff, int soff, int head, int hh) {
    put_o(u32x4{pack_o(o[0] * f, o[1] * f), pack_o(o[2] * f, o[3] * f), pack_o(o[4] * f, o[5] * f), pack_o(o[6] * f, o[7] * f)},
          u32x2{pack_o(o[8] * f, o[9] * f), pack_o(o[10] * f, o[11] * f)}, wrs, voff, soff, head, hh);
}

#define EXP4(C, a) "v_exp_f32 v[" #C "+" #a "], v[" #C "+" #a "]\n\tv_exp_f32 v[" #C "+" #a "+1], v[" #C "+" #a "+1]\n\t"         \
                   "v_exp_f32 v[" #C "+" #a "+2], v[" #C "+" #a "+2]\n\tv_exp_f32 v[" #C "+" #a "+3], v[" #C "+" #a "+3]\n\t"
#define PV(O, V, P) "v_smfmac_f32_32x32x32_bf16 v[" #O ":" #O "+15], v[" #V ":" #V "+3], v[" #P ":" #P "+7], v40\n\t"
#define QK0(S, Q) "v_mfma_f32_32x32x16_bf16 v[" #S ":" #S "+15], v[24:27], v[" #Q ":" #Q "+3], 0\n\t"
#define QK1(S, Q, K) "v_mfma_f32_32x32x8bf16_1k v[" #S ":" #S "+15], v[" #K ":" #K "+1], v[" #Q "+4:" #Q "+5], v[" #S ":" #S "+15]\n\t"
#define VLOAD(T) "s_mov_b64 exec, %[vm]\n\tds_read_b128 v[32:35], v43 offset:" #T "*5120+1280\n\t"                                \
                 "ds_read_b128 v[36:39], v43 offset:" #T "*5120+1920\n\t"
#define KBLOAD(T, K) "s_mov_b64 exec, %[km]\n\tds_read_b64 v[" #K ":" #K "+1], v42 offset:(" #T ")*5120+1024\n\ts_mov_b64 exec, -1\n\t"
#define PAIR(T, KC, KN, MID)                                                                       \
    PV(96, 36, 72) VLOAD(T) KBLOAD(T + 1, KN) EXP4(48, 0)                                          \
    QK0(64, 16) "ds_read_b128 v[24:27], v41 offset:(" #T "+1)*5120\n\t" EXP4(48, 4)                \
    QK1(64, 16, KC) EXP4(48, 8)                                                                    \
    "s_waitcnt lgkmcnt(2)\n\t" PV(80, 32, 48) EXP4(48, 12) MID "s_nop 1\n\t"                       \
    PV(80, 36, 56) EXP4(64, 0)                                                                     \
    "s_waitcnt lgkmcnt(0)\n\t" QK0(48, 8) EXP4(64, 4)                                              \
    QK1(48, 8, KN) EXP4(64, 8)                                                                     \
    PV(96, 32, 64) EXP4(64, 12) "s_nop 1\n\t"
#define PAIR_LAST(T, KC)                                                                           \
    PV(96, 36, 72) VLOAD(T) "s_mov_b64 exec, -1\n\t" EXP4(48, 0)                                   \
    QK0(64, 16) EXP4(48, 4)                                                                        \
    QK1(64, 16, KC) EXP4(48, 8)                                                                    \
    "s_waitcnt lgkmcnt(0)\n\t" PV(80, 32, 48) EXP4(48, 12) "s_nop 1\n\t"                           \
    PV(80, 36, 56) EXP4(64, 0) EXP4(64, 4) EXP4(64, 8)                                             \
    PV(96, 32, 64) EXP4(64, 12) "s_nop 1\n\t"                                                      \
    PV(96, 36, 72) "s_nop 15\n\t"
#define ZERO4(R) "v_mov_b64 v[" #R ":" #R "+1], 0\n\tv_mov_b64 v[" #R "+2:" #R "+3], 0\n\t"
#define MAX2(i) "v_max3_f32 v14, v14, v[116+" #i "], v[117+" #i "]\n\tv_max3_f32 v22, v22, v[132+" #i "], v[133+" #i "]\n\t"
#define PRIO(n, L) "s_cmp_ge_u32 %[psw], 4\n\ts_cbranch_scc1 " #L "f\n\ts_setprio " #n "\n\t" #L ":\n\t"

__global__ __launch_bounds__(768, 3) void gated_mix_kernel(const u16* __restrict__ x, const u16* __restrict__ wA, const u16* __restrict__ wB,
                                                            const u16* __restrict__ woA, const u16* __restrict__ woB,
                                                            const u16* __restrict__ gate_w, char* __restrict__ ws, u16* out) {
    __shared__ __attribute__((aligned(16))) char smem[KV_HEAD];
    lds_ptr lds = (lds_ptr)smem;
    const int tid = threadIdx.x, lane = tid & 63, wid = __builtin_amdgcn_readfirstlane(tid >> 6);
    const int g = lane >> 4, r16 = lane & 15;
    const rsrc_t wrs = __builtin_amdgcn_make_buffer_rsrc(ws, 0, (int)WS_END, 0x00020000);
    const int xcd = blockIdx.x & 7, idx = blockIdx.x >> 3;
    const int mt = (idx & 7) * 2 + (xcd & 1), nt = (xcd >> 1) + (idx >> 3) * 4, m0 = mt * 128;
    if (wid == 8) *(LDS_AS u32x2*)(lds + L_CNT) = u32x2{0u, 0u};
    if (wid >= 8 && nt < 15) {
        const int lw = wid - 8;
        const unsigned xoff = (lw * 8 + (lane >> 3)) * (DM * 2) + (((lane & 7) ^ ((lane >> 3) & 7)) << 4);
        const unsigned woff = (32 * (g >> 1) + 4 * (g & 1) + lw) * 3840 + r16 * 16;
        const char* xs = (const char*)(x + (size_t)m0 * DM);
        const char* bs = (const char*)(wB + nt * 128);
        const char* as = (const char*)(wA + nt * 128);
        unsigned so = (unsigned)(size_t)lds;
#pragma unroll 1
        for (int t = -2; t < 9; ++t) {
            if (t >= 0) {
                WAIT_V(12);
                RAW_BAR();
            }
            if (t < 8) {
#pragma unroll 1
                for (int i = 0; i < 4; ++i) {
                    const unsigned lx = so + (lw + 4 * i) * 1024, lb = so + 16384 + (lw + 4 * i) * K1_WP;
                    glds16x3(xs + i * (32 * DM * 2), xoff, lx, bs + i * (8 * 3840), as + i * (8 * 3840), woff, lb, lb + 16 * K1_WP);
                }
                xs += 128; bs += 64 * 3840; as += 64 * 3840;
            }
            so = so == (unsigned)(size_t)lds + 2 * K1_STAGE ? (unsigned)(size_t)lds : so + K1_STAGE;
        }
        WAIT_V(0);
        RAW_BAR();
    }
    const int head = (blockIdx.x & 7) * 4 + ((blockIdx.x >> 3) & 3), qb = blockIdx.x >> 5;
    const int omt = head, ont = qb;
    const int r32 = lane & 31, hh = lane >> 5;
    const u32x4 vfill = r32 == 20 ? u32x4{ONE2, ONE2, ONE2, ONE2} : u32x4{0u, 0u, 0u, 0u};
    const int voff = hh * 320 + (r32 < 20 ? r32 : 0) * 16;
    const unsigned olane = (r32 >> 4) * (int)O_BLK + (r32 & 15) * 16;
    const int ps = nt < 5 ? (int)Q_TILE : (int)KV_TILE;
    const int wimg = (int)WS_WO + ont * 5120;
    gu64* const f1 = (gu64*)(ws + WS_F1);
    constexpr int P_OFF = 102400;
    const int pslot = g * 256;
    asm volatile("" : "+s"(out));
    unsigned fq, fo;
    auto fin_addr = [&]() {
        const int row = tid / 10, c = tid - row * 10;
        fq = P_OFF + ((row >> 4) * 5 + (c >> 1)) * 1024 + (c & 1) * 512 + (((row & 15) + c) & 15) * 16;
        fo = ((omt * 64 + row) * DM + ont * 80 + c * 8) * 2;
        asm volatile("" : "+v"(fq), "+v"(fo));
    };
    if (nt == 15) {
        const int task = mt * 10 + wid, ks = task >> 2, cg = task & 3;
        lds_ptr st = lds + wid * 10240;
        if (wid < 10) {
            const char* wsrc = (const char*)((ks >= 20 ? woB : woA) + cg * 160);
#pragma unroll
            for (int i = 0; i < 10; ++i) {
                const int ci = i * 64 + lane, slot = ci / 20, cc = ci - slot * 20;
                const int c = (ks >= 20 ? ks - 20 : ks) * 4 + (slot >> 3), e = slot & 7, p = c / 5, r = c - p * 5;
                const int k = r < 4 ? (2 * p + (r >> 1)) * 20 + 4 * (r & 1) + (e & 3) + 8 * (e >> 2) : (2 * p + (e >> 2)) * 20 + 16 + (e & 3);
                glds16(wsrc, k * (DM * 2) + cc * 16, st + i * 1024);
            }
        }
        u32x4 gv[2];
        gv[0] = *(const u32x4*)(gate_w + lane * 8);
        gv[1] = lane < 16 ? *(const u32x4*)(gate_w + 512 + lane * 8) : u32x4{0u, 0u, 0u, 0u};
        float s[11];
#pragma unroll
        for (int u = 0; u < 11; ++u) {
            const int row = wid + 12 * u < 128 ? wid + 12 * u : wid;
            const u16* xr = x + (size_t)(m0 + row) * DM;
            const u32x4 xv[2] = {*(const u32x4*)(xr + lane * 8), *(const u32x4*)(xr + 512 + r16 * 8)};
            float a = 0.f;
#pragma unroll
            for (int c = 0; c < 2; ++c)
#pragma unroll
                for (int e = 0; e < 4; ++e) {
                    a = fmaf(__uint_as_float(xv[c][e] << 16), __uint_as_float(gv[c][e] << 16), a);
                    a = fmaf(__uint_as_float(xv[c][e] & 0xffff0000u), __uint_as_float(gv[c][e] & 0xffff0000u), a);
                }
            s[u] = a;
        }
#pragma unroll
        for (int o = 32; o; o >>= 1)
#pragma unroll
            for (int u = 0; u < 11; ++u) s[u] += __shfl_xor(s[u], o);
        float z = s[0];
#pragma unroll
        for (int u = 1; u < 11; ++u) z = lane == u ? s[u] : z;
        if (lane < 11 && wid + 12 * lane < 128) ST4(__float_as_uint(1.f / (1.f + __expf(-z))), (wid + 12 * lane) * 4, (int)WS_G + m0 * 4);
        WAIT_V(0);
        if (wid < 10) {
            lds_ptr rp = st + (8 * g + (r16 >> 2)) * 320 + (lane & 3) * 8;
#pragma unroll
            for (int j = 0; j < 10; ++j)
                ST16(__builtin_bit_cast(u32x4, cat8(lds_tr(rp + j * 32), lds_tr(rp + j * 32 + 4 * 320))), lane * 16, (int)WS_WO + (ks * 40 + cg * 10 + j) * 1024);
        }
    }
    if (wid >= 8) {
        u64 tag = __hip_atomic_load((gu64*)(ws + WS_EPOCH), RLX_AGENT) + 1;
        const int hw = wid - 8;
        __builtin_amdgcn_s_setprio(3);
        RAW_BAR();
        f32x16 c0, c1;
        {
            const unsigned long long msk = ~(0x15554ull << r32) >> (4 * hh);
            const unsigned mlo = (unsigned)msk, mhi = (unsigned)(msk >> 32);
#pragma unroll
            for (int r = 0; r < 16; ++r) {
                const int u = (r & 3) + 8 * (r >> 2);
                c0[r] = r < 8 ? 0.f : __uint_as_float(__builtin_amdgcn_sbfe(mlo, (u - 16) & 31, 1) & 0xff800000u);
                c1[r] = __uint_as_float(__builtin_amdgcn_sbfe(u < 16 ? mlo : mhi, (u + 16) & 31, 1) & 0xff800000u);
            }
        }
        asm volatile("" : "+v"(c0), "+v"(c1));
        const int fc = head * 20 + (g & 1) * 19;
        gu64* const fkv = f1 + (((g < 2 ? 640 : 1280) + fc) >> 7) * 16 * FS;
        auto go = [&](int n, gu64* w0) {
            volatile LDS_AS int* gw = (volatile LDS_AS int*)(lds + L_CNT + 4);
            if (hw == 3) {
                gu64* const w[1] = {w0};
                wait_flags<2>(w, tag);
                *gw = n;
            } else {
#pragma unroll 1
                while (__builtin_amdgcn_readfirstlane(*gw) < n) __builtin_amdgcn_s_sleep(1);
                asm volatile("" ::: "memory");
            }
        };
        const int qp = (hw < 3 ? hw * 3 : 7) * 1024;
        const char* q0s = ws + WS_Q + (size_t)((NH + head) * 64 + qb * 8) * Q_TILE + qp;
        const char* k0s = ws + WS_KV + (size_t)(NH + head) * KV_HEAD + hw * 7168;
        const char* k1s = k0s + 4096;
        unsigned q0l = (unsigned)(size_t)lds + L_Q + qp, k0l = (unsigned)(size_t)lds + hw * 7168, k1l = k0l + 4096;
        asm volatile("" : "+s"(q0s), "+s"(k0s), "+s"(k1s), "+s"(q0l), "+s"(k0l), "+s"(k1l));
        go(1, r16 < 3 ? fkv + r16 * FS : f1 + ((fc >> 7) * 16 + qb * 2 + (lane & 1)) * FS);
        glds_first(lane * 16, q0s, q0l, k0s, k0l, k1s, k1l);
        const char* src = ws + WS_KV + (size_t)(NH + head) * KV_HEAD + hw * 1024;
#define LOADS(a, b) _Pragma("unroll 1") for (int i = a; i < b; ++i) glds16_dev(src + i * 4096, lane * 16, lds + hw * 1024 + i * 4096)
        WAIT_V(0); RAW_BAR();
        go(2, fkv + r16 * FS);
        LOADS(7, 28);
        WAIT_V(16); RAW_BAR();
        *(LDS_AS int*)lds = 0;
        if (nt < 15) {
            lds_ptr sp = lds + L_STASH + hw * 9216 + lane * 16;
            const u32x4 po = *(const LDS_AS u32x4*)(sp + 8192);
#pragma unroll
            for (int j = 0; j < 4; ++j) {
                const u32x4 pv[2] = {*(const LDS_AS u32x4*)(sp + j * 2048), *(const LDS_AS u32x4*)(sp + j * 2048 + 1024)};
                put_qkv(pv, po[j] & ~7u, (po[j] & ~7u) + (po[j] & 7u) * 128, ps, wrs);
            }
        }
        WAIT_V(0);
        RAW_BAR();
        if (hw == 0 && lane == 0) __hip_atomic_store(f1 + (256 + nt * 16 + mt) * FS, tag, RLX_AGENT);
        LOADS(28, 40);
#pragma unroll
        for (int i = 0; i < 2; ++i)
            put_o(u32x4{~0u, ~0u, ~0u, ~0u}, u32x2{~0u, ~0u}, wrs, olane, (int)WS_O + (128 + (qb * 8 + hw * 2 + i) * 2) * (int)O_BLK, head, hh);
        {
            const int sec = r16 >> 2, fm = max(qb * 2 - 1 + (r16 & 3), 0);
            gu64* const w[1] = {f1 + (256 + (g < 2 && sec < 3 && (r16 & 3) < 3 ? ((sec * 640 + fc) >> 7) * 16 + fm : 240 + r16)) * FS};
            wait_flags<8>(w, tag);
        }
        {
            const int q0 = qb * 8 + hw * 2;
            const int qA = (int)WS_Q + (head * 64 + q0) * (int)Q_TILE, k1 = (int)WS_KV + (head * 64 + q0) * (int)KV_TILE;
            u32x4 aqa[2], ka[3], vf[5];
            u32x2 aqb[2] = {{0u, 0u}, {0u, 0u}}, kb[3];
            float gv[2];
#pragma unroll
            for (int i = 0; i < 3; ++i) {
                const int kt = k1 + (i == 0 ? (q0 ? -(int)KV_TILE : 0) : (i - 1) * (int)KV_TILE);
                ka[i] = LD16(lane * 16, kt);
                kb[i] = LD8(r32 * 8, kt + 1024);
            }
#pragma unroll
            for (int i = 0; i < 2; ++i) aqa[i] = LD16(lane * 16, qA + i * (int)Q_TILE);
            if (!hh) {
#pragma unroll
                for (int i = 0; i < 2; ++i) aqb[i] = LD8(r32 * 8, qA + i * (int)Q_TILE + 1024);
            }
#pragma unroll
            for (int i = 0; i < 5; ++i) vf[i] = vfill;
            if (r32 < 20) {
                vf[0] = LD16(voff, k1 + (q0 ? -(int)KV_TILE : 0) + 1920);
#pragma unroll
                for (int i = 1; i < 5; ++i) vf[i] = LD16(voff, k1 + ((i - 1) >> 1) * (int)KV_TILE + 1280 + ((i - 1) & 1) * 640);
            }
#pragma unroll
            for (int i = 0; i < 2; ++i) gv[i] = __uint_as_float(LD4(r32 * 4, (int)WS_G + (q0 + i) * 128));
#pragma unroll
            for (int i = 1; i >= 0; --i) {
                if (i == 0 && q0 == 0) {
#pragma unroll
                    for (int r = 8; r < 16; ++r) c0[r] = -INFINITY;
                }
                f32x16 s0 = MFMA32(BF8(ka[i]), BF8(aqa[i]), c0);
                f32x16 s1 = MFMA32(BF8(ka[i + 1]), BF8(aqa[i]), c1);
                s0 = MFMA32H(kb[i], aqb[i], s0);
                s1 = MFMA32H(kb[i + 1], aqb[i], s1);
                float m = max3f(s0[8], s0[9], s0[10]);
                m = max3f(m, s0[11], s0[12]); m = max3f(m, s0[13], s0[14]);
                m = xhalf_max(max16(s1, fmaxf(m, s0[15])));
                const u32x2 ref = with_ref(u32x2{0u, 0u}, m, hh);
                s0 = MFMA32H((u32x2{ONE2, ONE2}), ref, s0);
                s1 = MFMA32H((u32x2{ONE2, ONE2}), ref, s1);
#pragma unroll
                for (int r = 8; r < 16; ++r) s0[r] = __builtin_amdgcn_exp2f(s0[r]);
#pragma unroll
                for (int r = 0; r < 16; ++r) s1[r] = __builtin_amdgcn_exp2f(s1[r]);
                f32x16 o = SMFMAC(BF8(vf[2 * i]), HI8(s0), f32x16{});
                o = SMFMAC(BF8(vf[2 * i + 1]), LO8(s1), o);
                o = SMFMAC(BF8(vf[2 * i + 2]), HI8(s1), o);
                const float f = gv[i] * __builtin_amdgcn_rcpf(row_sum(o));
                put_o(u32x4{pack2(o[0] * f, o[1] * f), pack2(o[2] * f, o[3] * f), pack2(o[4] * f, o[5] * f), pack2(o[6] * f, o[7] * f)},
                      u32x2{pack2(o[8] * f, o[9] * f), pack2(o[10] * f, o[11] * f)}, wrs, olane, (int)WS_O + (q0 + i) * 2 * (int)O_BLK, head, hh);
            }
        }
        arrive(lds, (gu64*)(ws + WS_FA) + (qb * 32 + head) * FS, tag);
        f32x4 acc[2][5];
        bf16x8 of[3][2], wf[3][5];
        const int osrc = (int)WS_O + (omt * 4 + (hw & 1) * 2) * (int)O_BLK + (hw >> 1) * 10240, wsrc = wimg + (hw >> 1) * 409600;
        auto ldo = [&](int ks) {
#pragma unroll
            for (int i = 0; i < 2; ++i) of[ks % 3][i] = BF8(LD16(lane * 16, osrc + i * (int)O_BLK + ks * 1024));
        };
        auto ldw = [&](int ks) {
#pragma unroll
            for (int j = 0; j < 5; ++j) wf[ks % 3][j] = BF8(LD16(lane * 16, wsrc + ks * 40960 + j * 1024));
        };
        ldw(0); ldw(1); ldw(2);
        SB();
        RAW_BAR();
        {
            gu64* const w[1] = {(gu64*)(ws + WS_FA) + ((omt >> 2) * 32 + r32) * FS};
            wait_flags<8>(w, tag);
        }
        ldo(0); ldo(1); ldo(2);
        SB();
#pragma unroll
        for (int ks = 0; ks < 10; ++ks) {
#pragma unroll
            for (int j = 0; j < 5; ++j) {
#pragma unroll
                for (int i = 0; i < 2; ++i) acc[i][j] = MFMA16(wf[ks % 3][j], of[ks % 3][i], ks ? acc[i][j] : (f32x4{0.f, 0.f, 0.f, 0.f}));
                SB();
                __builtin_amdgcn_s_sleep(1);
                SB();
            }
            if (ks < 7) { ldo(ks + 3); ldw(ks + 3); SB(); }
        }
#pragma unroll 1
        for (int ks = hw * 5; ks < hw * 5 + 5; ++ks)
#pragma unroll
            for (int j = 0; j < 5; ++j) glds16_dev(ws + wimg + (20 + ks) * 40960 + j * 1024, lane * 16, lds + ks * 5120 + j * 1024);
        RAW_BAR();
        if (blockIdx.x == 0 && hw == 0) {
            gu64* const w[4] = {f1 + (256 + lane) * FS, f1 + (320 + lane) * FS, f1 + (384 + lane) * FS, f1 + (448 + lane) * FS};
            wait_flags<8>(w, tag);
            if (lane == 0) __hip_atomic_store((gu64*)(ws + WS_EPOCH), tag, RLX_AGENT);
        }
        WAIT_V(0);
        auto put = [&]() {
#pragma unroll
            for (int i = 0; i < 2; ++i)
#pragma unroll
                for (int j = 0; j < 5; ++j)
                    *(LDS_AS f32x4*)(lds + P_OFF + (1 + (hw >> 1)) * 20480 + (((hw & 1) * 2 + i) * 5 + j) * 1024 + pslot + ((r16 + 2 * j + (g >> 1)) & 15) * 16) = acc[i][j];
        };
        fin_addr();
        RAW_BAR();
        if (hw >= 2) put();
        RAW_BAR();
        if (hw < 2) put();
    } else {
        if (nt < 15) {
            const u64 tv = __hip_atomic_load((gu64*)(ws + WS_EPOCH), RLX_AGENT) + 1;
            const int br = wid < 4, wm = (wid >> 1) & 1, wn = wid & 1;
            const int xa = (wm * 64 + r16) * 128 + ((g ^ (lane & 7)) << 4);
            const int wa = 16384 + ((wid >> 2) * 16 + 4 * g + (r16 >> 2)) * K1_WP + (wn * 64 + 4 * (lane & 3)) * 2;
            const int tile0 = mt * 4 + wm * 2, cw = nt * 128 + wn * 64;
            unsigned poff[4], poff1[4];
#pragma unroll
            for (int j = 0; j < 4; ++j) {
                if (nt < 10) {
                    const int cp = (cw - (nt < 5 ? 0 : 640)) / 8 + j * 2 + (g >> 1), p = cp / 5, r = cp - p * 5;
                    const unsigned hoff = (int)(nt < 5 ? WS_Q : WS_KV) + ((br * NH + 2 * p) * 64 + tile0) * ps;
                    poff[j] = r == 2 ? hoff + (g & 1) * 64 * ps + 1024 + r16 * 8 : hoff + (r > 2 ? 64 * ps + (r - 3) * 512 : r * 512) + r16 * 16 + (g & 1) * 8;
                    poff1[j] = poff[j] + (r == 2 ? 128 : 256);
                } else {
                    const int c = cw - 1280 + j * 16 + r16, h = c / 20, d = c - h * 20;
                    poff[j] = (int)WS_KV + ((br * NH + h) * 64 + tile0) * (int)KV_TILE + 1280 + (g >> 1) * 320 + d * 16 + (g & 1) * 8;
                    poff1[j] = poff[j] + 640;
                }
            }
            asm volatile("" : "+v"(poff[0]), "+v"(poff[1]), "+v"(poff[2]), "+v"(poff[3]), "+v"(poff1[0]), "+v"(poff1[1]), "+v"(poff1[2]), "+v"(poff1[3]));
            const u64 tag = (u64)__builtin_amdgcn_readfirstlane((unsigned)(tv >> 32)) << 32 | (unsigned)__builtin_amdgcn_readfirstlane((unsigned)tv);
            f32x4 acc[4][4];
#pragma unroll
            for (int i = 0; i < 4; ++i)
#pragma unroll
                for (int j = 0; j < 4; ++j) acc[i][j] = f32x4{0.f, 0.f, 0.f, 0.f};
            if (br) __builtin_amdgcn_s_setprio(1);
            const bool urgent = br && (nt < 5 || mt < 3);
            gu64* bflag = f1 + (nt * 16 + mt) * FS;
            asm volatile("" : "+s"(bflag));
            auto mid = [&]() {
                if (!urgent) RAW_BAR();
                if (br && !urgent) __builtin_amdgcn_s_sleep(16);
            };
            auto fin = [&](int j) {
                if (nt < 5) {
#pragma unroll
                    for (int i = 0; i < 4; ++i) acc[i][j] *= QSCALE;
                }
                u32x4 pv[2];
#pragma unroll
                for (int i = 0; i < 2; ++i) {
                    const f32x4 a = acc[2 * i][j], b = acc[2 * i + 1][j];
                    pv[i] = u32x4{pack2(a[0], a[1]), pack2(a[2], a[3]), pack2(b[0], b[1]), pack2(b[2], b[3])};
                }
                if (br) {
                    put_qkv(pv, poff[j], poff1[j], ps, wrs);
                } else {
#pragma unroll
                    for (int i = 0; i < 2; ++i) *(LDS_AS u32x4*)(lds + L_STASH + (wid - 4) * 9216 + j * 2048 + i * 1024 + lane * 16) = pv[i];
                }
            };
            if (nt < 10) k1_main<false>(acc, lds, xa, wa, mid, fin);
            else k1_main<true>(acc, lds, xa, wa, mid, fin);
            if (urgent) RAW_BAR();
            if (br) {
                arrive(lds + L_CNT, bflag, tag);
            } else {
                *(LDS_AS u32x4*)(lds + L_STASH + (wid - 4) * 9216 + 8192 + lane * 16) =
                    u32x4{poff[0] + ((poff1[0] - poff[0]) >> 7), poff[1] + ((poff1[1] - poff[1]) >> 7), poff[2] + ((poff1[2] - poff[2]) >> 7), poff[3] + ((poff1[3] - poff[3]) >> 7)};
            }
        } else {
            RAW_BAR();
            WAIT_V(0);
        }
        const int qt = qb * 8 + wid;
        const unsigned long long vmask = 0x000fffff000fffffull;
        f32x16 o, o2;
        float gate;
        u32x4 qa0, vlo = vfill, vhi = vfill;
        u32x2 qb0;
        const unsigned l0 = (unsigned)(size_t)lds, kap = (wid >> 2) * (int)KV_TILE;
        unsigned a_ka = l0 + kap + lane * 16, a_kb = l0 + kap + r32 * 8, a_v = l0 + kap + voff;
        const unsigned qs0 = L_Q + wid * (int)Q_TILE - kap, qs1 = L_Q + (wid ^ 4) * (int)Q_TILE - kap;
        int nbar = 0x53, it = 7;
        __builtin_amdgcn_s_setprio(1);
        asm volatile(
            "v_mov_b32 v28, %[one]\n\tv_mov_b32 v29, %[one]\n\tv_mov_b32 v30, %[one]\n\tv_mov_b32 v31, %[one]\n\tv_mov_b32 v40, %[sidx]\n\t"
            ZERO4(72) ZERO4(76) ZERO4(80) ZERO4(84) ZERO4(88) ZERO4(92) ZERO4(96) ZERO4(100) ZERO4(104) ZERO4(108)
            "v_mov_b64 v[12:13], 0\n\tv_mov_b64 v[20:21], 0\n\t"
            "v_add_u32 v48, %[qs0], v41\n\tv_add_u32 v49, %[qs1], v41\n\tv_subrev_u32 v50, %[kap], v41\n\t"
            "v_add_u32 v51, %[qs0], v42\n\tv_add_u32 v52, %[qs1], v42\n\tv_subrev_u32 v53, %[kap], v42\n\t"
            "s_waitcnt lgkmcnt(0)\n\ts_barrier\n\t"
            "ds_read_b128 v[112:115], v50\n\tds_read_b128 v[8:11], v48\n\tds_read_b128 v[16:19], v49\n\t"
            "s_mov_b64 exec, %[km]\n\t"
            "ds_read_b64 v[30:31], v53 offset:1024\n\tds_read_b64 v[12:13], v51 offset:1024\n\tds_read_b64 v[20:21], v52 offset:1024\n\t"
            "ds_read_b64 v[28:29], v42 offset:1024\n\t"
            "s_mov_b64 exec, -1\n\t"
            "ds_read_b128 v[24:27], v41\n\t"
            "s_waitcnt lgkmcnt(5)\n\t"
            "v_mfma_f32_32x32x16_bf16 v[116:131], v[112:115], v[8:11], 0\n\t"
            "v_mfma_f32_32x32x16_bf16 v[132:147], v[112:115], v[16:19], 0\n\t"
            "s_waitcnt lgkmcnt(0)\n\t"
            QK1(116, 8, 30) QK1(132, 16, 30) QK0(48, 8)
            "s_nop 10\n\t"
            "v_max_f32 v14, v116, v117\n\tv_max_f32 v22, v132, v133\n\t"
            MAX2(2) MAX2(4) MAX2(6) MAX2(8) MAX2(10) MAX2(12) MAX2(14)
            "v_mov_b32 v15, v14\n\tv_mov_b32 v23, v22\n\ts_nop 1\n\t"
            "v_permlane32_swap_b32 v14, v15\n\tv_permlane32_swap_b32 v22, v23\n\t"
            "s_not_b64 exec, %[km]\n\t"
            "v_min_f32 v14, -v14, -v15\n\tv_min_f32 v22, -v22, -v23\n\t"
            "v_and_b32 v12, 0xffff0000, v14\n\tv_and_b32 v20, 0xffff0000, v22\n\t"
            "s_mov_b64 exec, -1\n\ts_nop 1\n\t"
            QK1(48, 8, 28)
            "s_nop 5\n\t"
            "1:\n\t"
            PRIO(2, 3) PAIR(0, 28, 30, "") PAIR(1, 30, 28, "") PAIR(2, 28, 30, PRIO(0, 4)) PAIR(3, 30, 28, "")
            "v_add_u32 v41, 20480, v41\n\tv_add_u32 v42, 20480, v42\n\tv_add_u32 v43, 20480, v43\n\t"
            "s_bitcmp0_b32 %[nbar], 0\n\t"
            "s_cbranch_scc1 2f\n\t"
            "s_barrier\n\t"
            "2:\n\t"
            "s_lshr_b32 %[nbar], %[nbar], 1\n\t"
            "s_sub_i32 %[it], %[it], 1\n\t"
            "s_cmp_lg_u32 %[it], 0\n\t"
            "s_cbranch_scc1 1b\n\t"
            "global_load_dword v44, %[goff], %[gb] sc1\n\t"
            PRIO(2, 3) PAIR(0, 28, 30, "") PAIR(1, 30, 28, "") PAIR(2, 28, 30, PRIO(0, 4)) PAIR_LAST(3, 30)
            "s_waitcnt vmcnt(0)"
            : "=&{v44}"(gate), "=&{v[80:95]}"(o), "=&{v[96:111]}"(o2), "=&{v[8:11]}"(qa0), "=&{v[12:13]}"(qb0), "+{v[32:35]}"(vlo),
              "+{v[36:39]}"(vhi), "+{v41}"(a_ka), "+{v42}"(a_kb), "+{v43}"(a_v), [nbar] "+s"(nbar), [it] "+s"(it)
            : [vm] "s"(vmask), [km] "s"(0xffffffffull), [one] "s"(ONE2), [sidx] "s"(0xDDDDDDDDu), [psw] "s"(wid), [goff] "v"(r32 * 4),
              [gb] "s"(ws + WS_G + qt * 128), [qs0] "s"(qs0), [qs1] "s"(qs1), [kap] "s"(kap)
            : "v14", "v15", "v16", "v17", "v18", "v19", "v20", "v21", "v22", "v23", "v24", "v25", "v26", "v27", "v28", "v29", "v30",
              "v31", "v40", "v48", "v49", "v50", "v51", "v52", "v53", "v54", "v55", "v56", "v57", "v58", "v59", "v60", "v61", "v62",
              "v63", "v64", "v65", "v66", "v67", "v68", "v69", "v70", "v71", "v72", "v73", "v74", "v75", "v76", "v77", "v78", "v79",
              "v112", "v113", "v114", "v115", "v116", "v117", "v118", "v119", "v120", "v121", "v122", "v123", "v124", "v125", "v126",
              "v127", "v128", "v129", "v130", "v131", "v132", "v133", "v134", "v135", "v136", "v137", "v138", "v139", "v140", "v141",
              "v142", "v143", "v144", "v145", "v146", "v147", "scc", "memory");
        __builtin_amdgcn_s_setprio(3);
#pragma unroll
        for (int i = 0; i < 3; ++i)
            *(LDS_AS f32x4*)(lds + P_OFF + (wid ^ 4) * 3072 + i * 1024 + lane * 16) = f32x4{o2[4 * i], o2[4 * i + 1], o2[4 * i + 2], o2[4 * i + 3]};
        RAW_BAR();
#pragma unroll
        for (int i = 0; i < 3; ++i) {
            const f32x4 t = *(const LDS_AS f32x4*)(lds + P_OFF + wid * 3072 + i * 1024 + lane * 16);
#pragma unroll
            for (int e = 0; e < 4; ++e) o[4 * i + e] += t[e];
        }
        const float gB = 1.f - gate;
        float l = row_sum(o);
        float asum = 0.f;
#pragma unroll
        for (int i = 0; i < 12; ++i) asum += fabsf(o[i]);
        if (__builtin_expect(__ballot(l >= 0x1p-64f && l <= 0x1p64f && asum <= 0x1p126f) == ~0ull, 1)) {
            store_o(o, gB * __builtin_amdgcn_rcpf(l), wrs, olane, (int)WS_O + (128 + qt * 2) * (int)O_BLK, head, hh);
        } else {
            const int kv = (int)WS_KV + (NH + head) * (int)KV_HEAD;
            u32x2 qr = hh ? u32x2{0u, 0u} : qb0;
            float mt2 = -INFINITY;
            o = f32x16{};
#pragma unroll 1
            for (int t = 0; t < 128; ++t) {
                const int kt = kv + (t & 63) * (int)KV_TILE;
                u32x2 kb = {ONE2, ONE2};
                if (!hh) kb = LD8(r32 * 8, kt + 1024);
                f32x16 sc = MFMA32(BF8(LD16(lane * 16, kt)), BF8(qa0), f32x16{});
                sc = MFMA32H(kb, qr, sc);
                if (t < 64) {
                    mt2 = max16(sc, mt2);
                    if (t == 63) qr = with_ref(qb0, xhalf_max(mt2), hh);
                } else {
                    u32x4 v0 = vfill, v1 = vfill;
                    if (r32 < 20) { v0 = LD16(voff, kt + 1280); v1 = LD16(voff, kt + 1920); }
#pragma unroll
                    for (int r = 0; r < 16; ++r) sc[r] = __builtin_amdgcn_exp2f(sc[r]);
                    o = SMFMAC(BF8(v0), LO8(sc), o);
                    o = SMFMAC(BF8(v1), HI8(sc), o);
                }
            }
            l = row_sum(o);
            asm volatile("" : "+v"(l));
            store_o(o, gB * __builtin_amdgcn_rcpf(l), wrs, olane, (int)WS_O + (128 + qt * 2) * (int)O_BLK, head, hh);
        }
        RAW_BAR();
        const int mb = wid & 3, kh = wid >> 2;
        const int osrc = (int)WS_O + (128 + omt * 4 + mb) * (int)O_BLK + kh * 10240;
        u32x4 of[10];
#pragma unroll
        for (int s = 0; s < 10; ++s) of[s] = LD16(lane * 16, osrc + s * 1024);
        SB();
        f32x4 acc[5];
        lds_ptr pp = lds + P_OFF + kh * 40960 + mb * 5120 + pslot;
#pragma unroll
        for (int j = 0; j < 5; ++j) acc[j] = f32x4{0.f, 0.f, 0.f, 0.f};
        if (kh) {
#pragma unroll
            for (int j = 0; j < 5; ++j) acc[j] = *(const LDS_AS f32x4*)(pp + j * 1024 + ((r16 + 2 * j + (g >> 1)) & 15) * 16);
        }
        lds_ptr wp = lds + kh * 51200 + lane * 16;
        bf16x8 wf[2][10];
#pragma unroll
        for (int j = 0; j < 10; ++j) wf[0][j] = lds_b128(wp + j * 1024);
        auto bad = [&](int s) {
            const u32x4 a = __builtin_elementwise_max(of[s], of[s + 1]);
            return __any(max(max(a[0], a[1]), max(a[2], a[3])) == ~0u);
        };
#pragma unroll
        for (int s = 0; s < 10; s += 2) {
            if (s < 8) {
#pragma unroll
                for (int j = 0; j < 10; ++j) wf[(s / 2 + 1) & 1][j] = lds_b128(wp + (s + 2) * 5120 + j * 1024);
            }
            if (bad(s)) {
#pragma unroll 1
                for (unsigned spins = 0; spins < (1u << 18); ++spins) {
                    asm volatile("" ::: "memory");
#pragma unroll
                    for (int t = 8; t >= s; t -= 2)
                        if (t == s || bad(t)) { of[t] = LD16(lane * 16, osrc + t * 1024); of[t + 1] = LD16(lane * 16, osrc + (t + 1) * 1024); }
                    if (!bad(s)) break;
                }
            }
            if (s == 2) fin_addr();
#pragma unroll
            for (int j = 0; j < 10; ++j) acc[j % 5] = MFMA16(wf[(s / 2) & 1][j], BF8(of[s + j / 5]), acc[j % 5]);
        }
#pragma unroll
        for (int j = 0; j < 5; ++j) *(LDS_AS f32x4*)(pp + j * 1024 + ((r16 + 2 * j + (g >> 1)) & 15) * 16) = acc[j];
    }
    RAW_BAR();
    if (tid < 640) {
        f32x4 t[6];
#pragma unroll
        for (int p = 0; p < 6; ++p) t[p] = *(const LDS_AS f32x4*)(lds + fq + (p >> 1) * 20480 + (p & 1) * 256);
        SB();
        const f32x4 a = t[0] + t[2] + t[4], b = t[1] + t[3] + t[5];
        const u32x4 v = {pack2(a[0], a[1]), pack2(a[2], a[3]), pack2(b[0], b[1]), pack2(b[2], b[3])};
        __builtin_amdgcn_raw_buffer_store_b128(v, __builtin_amdgcn_make_buffer_rsrc(out, 0, SEQ * DM * 2, 0x00020000), fo, 0, SC1);
    }
}

extern "C" void kernel_launch(void* const* d_in, const int* in_sizes, int n_in, void* d_out, int out_size, void* d_ws, size_t ws_size,
                              hipStream_t stream) {
    gated_mix_kernel<<<256, 768, 0, stream>>>((const u16*)d_in[0], (const u16*)d_in[1], (const u16*)d_in[4], (const u16*)d_in[2],
                                              (const u16*)d_in[5], (const u16*)d_in[6], (char*)d_ws, (u16*)d_out);
}
```

```cpp
#include <hip/hip_runtime.h>
#include <hip/hip_bf16.h>

typedef __attribute__((ext_vector_type(8))) __bf16 bf16x8;
typedef __attribute__((ext_vector_type(16))) __bf16 bf16x16;
typedef __attribute__((ext_vector_type(2))) __bf16 bf16x2;
typedef __attribute__((ext_vector_type(4))) short s16x4;
typedef __attribute__((ext_vector_type(2))) float f32x2;
typedef __attribute__((ext_vector_type(4))) float f32x4;
typedef __attribute__((ext_vector_type(16))) float f32x16;
typedef __attribute__((ext_vector_type(2))) unsigned u32x2;
typedef __attribute__((ext_vector_type(4))) unsigned u32x4;
typedef unsigned short u16;
#define LDS_AS __attribute__((address_space(3)))
typedef LDS_AS char* lds_ptr;

constexpr int SEQ = 2048, DM = 640, NH = 32;
constexpr size_t KV_TILE = 2560, KV_HEAD = 64 * KV_TILE;
constexpr size_t Q_TILE = 1280, Q_HEAD = 64 * Q_TILE;
constexpr size_t WS_KV = 0;
constexpr size_t WS_Q = WS_KV + 2 * NH * KV_HEAD;
constexpr size_t O_BLK = 80 * 256;
constexpr size_t WS_O = WS_Q + 2 * NH * Q_HEAD;
constexpr size_t WS_WO = WS_O + 2 * 128 * O_BLK;
constexpr size_t WS_G = WS_WO + 40 * 40 * 1024;
constexpr size_t WS_F1 = WS_G + SEQ * 4;
constexpr int FS = 16;
constexpr size_t WS_FA = WS_F1 + 512 * 128;
constexpr size_t WS_EPOCH = WS_FA + 256 * 128;
constexpr size_t WS_END = WS_EPOCH + 128;
constexpr float QSCALE = 0.22360679774997896f * 1.4426950408889634f;

#define WAIT_V(n) asm volatile("s_waitcnt vmcnt(" #n ")" ::: "memory")
#define RAW_BAR() asm volatile("s_waitcnt lgkmcnt(0)\n\ts_barrier" ::: "memory")
#define MFMA16(a, b, c) __builtin_amdgcn_mfma_f32_16x16x32_bf16(a, b, c, 0, 0, 0)
#define MFMA32(a, b, c) __builtin_amdgcn_mfma_f32_32x32x16_bf16(a, b, c, 0, 0, 0)
#define MFMA32H(a, b, c) __builtin_amdgcn_mfma_f32_32x32x8bf16_1k(__builtin_bit_cast(s16x4, a), __builtin_bit_cast(s16x4, b), c, 0, 0, 0)

__device__ __forceinline__ unsigned pack2(float lo, float hi) {
    f32x2 v = {lo, hi};
    return __builtin_bit_cast(unsigned, __builtin_convertvector(v, bf16x2));
}
__device__ __forceinline__ void glds16(const char* sbase, unsigned voff, lds_ptr l) {
    unsigned keep;
    asm volatile("s_mov_b32 %0, m0\n\ts_mov_b32 m0, %3\n\ts_nop 0\n\tglobal_load_lds_dwordx4 %1, %2\n\ts_mov_b32 m0, %0"
                 : "=&s"(keep) : "v"(voff), "s"(sbase), "s"((unsigned)(size_t)l) : "memory");
}
__device__ __forceinline__ void glds16x3(const char* s0, unsigned v0, unsigned l0, const char* s1, const char* s2, unsigned v1, unsigned l1, unsigned l2) {
    unsigned keep;
    asm volatile("s_mov_b32 %0, m0\n\ts_mov_b32 m0, %3\n\ts_nop 0\n\tglobal_load_lds_dwordx4 %1, %2\n\t"
                 "s_mov_b32 m0, %7\n\ts_nop 0\n\tglobal_load_lds_dwordx4 %4, %5\n\t"
                 "s_mov_b32 m0, %8\n\ts_nop 0\n\tglobal_load_lds_dwordx4 %4, %6\n\ts_mov_b32 m0, %0"
                 : "=&s"(keep) : "v"(v0), "s"(s0), "s"(l0), "v"(v1), "s"(s1), "s"(s2), "s"(l1), "s"(l2) : "memory");
}
__device__ __forceinline__ bf16x8 lds_b128(lds_ptr p) { return *(const LDS_AS bf16x8*)p; }
__device__ __forceinline__ s16x4 lds_tr(lds_ptr p) { return __builtin_amdgcn_ds_read_tr16_b64_v4i16((LDS_AS s16x4*)p); }
__device__ __forceinline__ bf16x8 cat8(s16x4 a, s16x4 b) {
    return __builtin_bit_cast(bf16x8, __builtin_shufflevector(a, b, 0, 1, 2, 3, 4, 5, 6, 7));
}
typedef __amdgpu_buffer_rsrc_t rsrc_t;
typedef unsigned long long u64;
typedef __attribute__((address_space(1))) u64 gu64;
#define RLX_AGENT __ATOMIC_RELAXED, __HIP_MEMORY_SCOPE_AGENT
constexpr int SC1 = 16;
#define LD16(voff, soff) __builtin_amdgcn_raw_buffer_load_b128(wrs, voff, soff, SC1)
#define LD8(voff, soff) __builtin_amdgcn_raw_buffer_load_b64(wrs, voff, soff, SC1)
#define LD4(voff, soff) __builtin_amdgcn_raw_buffer_load_b32(wrs, voff, soff, SC1)
#define ST16(v, voff, soff) __builtin_amdgcn_raw_buffer_store_b128(v, wrs, (voff) + (soff), 0, SC1)
#define ST8(v, voff, soff) __builtin_amdgcn_raw_buffer_store_b64(v, wrs, voff, soff, SC1)
#define ST4(v, voff, soff) __builtin_amdgcn_raw_buffer_store_b32(v, wrs, voff, soff, SC1)
#define BF8(x) __builtin_bit_cast(bf16x8, x)
__device__ __forceinline__ void glds16_dev(const char* sbase, unsigned voff, lds_ptr l) {
    unsigned keep;
    asm volatile("s_mov_b32 %0, m0\n\ts_mov_b32 m0, %3\n\ts_nop 0\n\tglobal_load_lds_dwordx4 %1, %2 sc1\n\ts_mov_b32 m0, %0"
                 : "=&s"(keep) : "v"(voff), "s"(sbase), "s"((unsigned)(size_t)l) : "memory");
}
#define GL_(v, s, o) "global_load_lds_dwordx4 " v ", " s " offset:" #o " sc1\n\t"
__device__ __forceinline__ void glds_first(unsigned voff, const char* s0, unsigned l0, const char* s1, unsigned l1, const char* s2, unsigned l2) {
    unsigned keep;
    asm volatile("s_mov_b32 %0, m0\n\ts_mov_b32 m0, %3\n\ts_nop 0\n\t" GL_("%1", "%2", 0) GL_("%1", "%2", 1024) GL_("%1", "%2", 2048)
                 "s_mov_b32 m0, %5\n\ts_nop 0\n\t" GL_("%1", "%4", 0) GL_("%1", "%4", 1024) GL_("%1", "%4", 2048) GL_("%1", "%4", 3072)
                 "s_mov_b32 m0, %7\n\ts_nop 0\n\t" GL_("%1", "%6", 0) GL_("%1", "%6", 1024) GL_("%1", "%6", 2048)
                 "s_mov_b32 m0, %0"
                 : "=&s"(keep) : "v"(voff), "s"(s0), "s"(l0), "s"(s1), "s"(l1), "s"(s2), "s"(l2) : "memory");
}
__device__ __forceinline__ void arrive(lds_ptr cnt, gu64* flag, u64 tag) {
    unsigned r, sr;
    asm volatile("s_waitcnt vmcnt(0)\n\ts_mov_b64 exec, 1\n\tds_add_rtn_u32 %0, %2, %3\n\ts_waitcnt lgkmcnt(0)\n\t"
                 "v_readfirstlane_b32 %1, %0\n\ts_cmp_lg_u32 %1, 3\n\ts_cbranch_scc1 1f\n\t"
                 "global_store_dwordx2 %4, %5, %6 sc1\n\t1:\n\ts_mov_b64 exec, -1"
                 : "=&v"(r), "=&s"(sr) : "v"((unsigned)(size_t)cnt), "v"(1u), "v"(0u), "v"(tag), "s"(flag) : "scc", "memory");
}
template <int SLEEP, int N>
__device__ __forceinline__ void wait_flags(gu64* const (&w)[N], u64 tag) {
#pragma unroll 1
    for (unsigned spins = 0; spins < (1u << 18); ++spins) {
        bool ok = true;
#pragma unroll
        for (int i = 0; i < N; ++i) ok &= __hip_atomic_load(w[i], RLX_AGENT) == tag;
        if (__all(ok)) break;
        __builtin_amdgcn_s_sleep(SLEEP);
    }
    __builtin_amdgcn_fence(__ATOMIC_ACQUIRE, "wavefront");
}
constexpr int K1_WP = 1024 + 32;
constexpr int K1_STAGE = 16384 + 32 * K1_WP;
constexpr int L_Q = 112 * 1024, L_STASH = 122 * 1024, L_CNT = 163824;
#define SB() __builtin_amdgcn_sched_barrier(0)
__device__ __forceinline__ void put_qkv(const u32x4 (&pv)[2], unsigned off0, unsigned off1, int ps, rsrc_t wrs) {
#pragma unroll
    for (int i = 0; i < 2; ++i) {
        ST8((u32x2{pv[i][0], pv[i][1]}), off0, i * ps);
        ST8((u32x2{pv[i][2], pv[i][3]}), off1, i * ps);
    }
}

template <bool VT, class M, class F>
__device__ __forceinline__ void k1_main(f32x4 (&acc)[4][4], lds_ptr lds, int xa, int wa, const char* tp, int tstep, M mid, F fin) {
    unsigned d0 = 0;
#define TOUCH() asm volatile("global_load_dword %0, %1, off" : "+v"(d0) : "v"(tp) : "memory")
    bf16x8 xf[2][4], wf[2][4];
    auto rd = [&](int so, int kk) {
#pragma unroll
        for (int j = 0; j < 4; ++j) {
            lds_ptr p = lds + so + wa + kk * 512 + j * 32;
            wf[kk][j] = cat8(lds_tr(p), lds_tr(p + 256));
        }
#pragma unroll
        for (int i = 0; i < 4; ++i) xf[kk][i] = lds_b128(lds + so + (xa ^ (kk * 64)) + i * 2048);
    };
    auto mm = [&](int kk) {
#pragma unroll
        for (int i = 0; i < 4; ++i)
#pragma unroll
            for (int j = 0; j < 4; ++j)
                acc[i][j] = VT ? MFMA16(xf[kk][i], wf[kk][j], acc[i][j]) : MFMA16(wf[kk][j], xf[kk][i], acc[i][j]);
    };
#define ILV() do { _Pragma("unroll") for (int q_ = 0; q_ < 12; ++q_) { __builtin_amdgcn_sched_group_barrier(0x100, 1, 0); __builtin_amdgcn_sched_group_barrier(0x8, 1, 0); } __builtin_amdgcn_sched_group_barrier(0x8, 4, 0); SB(); } while (0)
    if (tstep < 0) TOUCH();
    RAW_BAR();
    if (tstep == 0) TOUCH();
    rd(0, 0); SB();
    rd(0, 1);
    mm(0); ILV();
    int so = K1_STAGE;
#pragma unroll 1
    for (int t = 1; t < 10; ++t) {
        RAW_BAR();
        if (tstep == t) TOUCH();
        rd(so, 0);
        mm(1); ILV();
        rd(so, 1);
        mm(0); ILV();
        so = so == 2 * K1_STAGE ? 0 : so + K1_STAGE;
    }
    asm volatile("s_waitcnt vmcnt(0)" : "+v"(d0) :: "memory");
    mid();
#pragma unroll
    for (int j = 0; j < 5; ++j) {
        if (j < 4) {
#pragma unroll
            for (int i = 0; i < 4; ++i) acc[i][j] = VT ? MFMA16(xf[1][i], wf[1][j], acc[i][j]) : MFMA16(wf[1][j], xf[1][i], acc[i][j]);
        }
        SB();
        if (j) fin(j - 1);
        SB();
    }
}

#define SMFMAC(a, b8, c) __builtin_amdgcn_smfmac_f32_32x32x32_bf16(a, __builtin_bit_cast(bf16x16, b8), c, (int)0xDDDDDDDD, 0, 0)
#define LO8(s) __builtin_shufflevector(s, s, 0, 1, 2, 3, 4, 5, 6, 7)
#define HI8(s) __builtin_shufflevector(s, s, 8, 9, 10, 11, 12, 13, 14, 15)
constexpr unsigned ONE2 = 0x3F803F80u;

__device__ __forceinline__ float xhalf_max(float m) {
    auto r = __builtin_amdgcn_permlane32_swap(__float_as_uint(m), __float_as_uint(m), false, false);
    return fmaxf(__uint_as_float(r[0]), __uint_as_float(r[1]));
}
__device__ __forceinline__ float max3f(float a, float b, float c) { return __builtin_fmaxf(__builtin_fmaxf(a, b), c); }
__device__ __forceinline__ float max16(const f32x16& s, float m) {
#pragma unroll
    for (int r = 0; r < 16; r += 2) m = max3f(m, s[r], s[r + 1]);
    return m;
}
__device__ __forceinline__ float row_sum(const f32x16& o) {
    auto r = __builtin_amdgcn_permlane32_swap(__float_as_uint(o[8]), __float_as_uint(o[8]), false, false);
    return __uint_as_float(r[1]);
}
__device__ __forceinline__ u32x2 with_ref(u32x2 q, float m, int hh) {
    const unsigned h1 = __float_as_uint(-m) & 0xffff0000u;
    const float r1 = -m - __uint_as_float(h1);
    const unsigned h2 = __float_as_uint(r1) & 0xffff0000u;
    const unsigned h3 = __float_as_uint(r1 - __uint_as_float(h2)) >> 16;
    return hh ? u32x2{h1 | (h2 >> 16), h3} : q;
}
__device__ __forceinline__ void put_o(u32x4 v, u32x2 t, rsrc_t wrs, unsigned voff, int soff, int head, int hh) {
    const int c0 = (head >> 1) * 5;
    ST16(v, voff + hh * 256, soff + (c0 + (head & 1) * 2) * 256);
    if (!hh) ST8(t, voff, soff + (c0 + 4) * 256 + (head & 1) * 8);
}
__device__ __forceinline__ unsigned pack_o(float a, float b) { return min(pack2(a, b), 0xFFFFFFFEu); }
__device__ __forceinline__ void store_o(const f32x16& o, float f, rsrc_t wrs, unsigned voff, int soff, int head, int hh) {
    put_o(u32x4{pack_o(o[0] * f, o[1] * f), pack_o(o[2] * f, o[3] * f), pack_o(o[4] * f, o[5] * f), pack_o(o[6] * f, o[7] * f)},
          u32x2{pack_o(o[8] * f, o[9] * f), pack_o(o[10] * f, o[11] * f)}, wrs, voff, soff, head, hh);
}

#define EXP4(C, a) "v_exp_f32 v[" #C "+" #a "], v[" #C "+" #a "]\n\tv_exp_f32 v[" #C "+" #a "+1], v[" #C "+" #a "+1]\n\t"         \
                   "v_exp_f32 v[" #C "+" #a "+2], v[" #C "+" #a "+2]\n\tv_exp_f32 v[" #C "+" #a "+3], v[" #C "+" #a "+3]\n\t"
#define PV(O, V, P) "v_smfmac_f32_32x32x32_bf16 v[" #O ":" #O "+15], v[" #V ":" #V "+3], v[" #P ":" #P "+7], v40\n\t"
#define QK0(S, Q) "v_mfma_f32_32x32x16_bf16 v[" #S ":" #S "+15], v[24:27], v[" #Q ":" #Q "+3], 0\n\t"
#define QK1(S, Q, K) "v_mfma_f32_32x32x8bf16_1k v[" #S ":" #S "+15], v[" #K ":" #K "+1], v[" #Q "+4:" #Q "+5], v[" #S ":" #S "+15]\n\t"
#define VLOAD(T) "s_mov_b64 exec, %[vm]\n\tds_read_b128 v[32:35], v43 offset:" #T "*5120+1280\n\t"                                \
                 "ds_read_b128 v[36:39], v43 offset:" #T "*5120+1920\n\t"
#define KBLOAD(T, K) "s_mov_b64 exec, %[km]\n\tds_read_b64 v[" #K ":" #K "+1], v42 offset:(" #T ")*5120+1024\n\ts_mov_b64 exec, -1\n\t"
#define PAIR(T, KC, KN, MID)                                                                       \
    PV(96, 36, 72) VLOAD(T) KBLOAD(T + 1, KN) EXP4(48, 0)                                          \
    QK0(64, 16) "ds_read_b128 v[24:27], v41 offset:(" #T "+1)*5120\n\t" EXP4(48, 4)                \
    QK1(64, 16, KC) EXP4(48, 8)                                                                    \
    "s_waitcnt lgkmcnt(2)\n\t" PV(80, 32, 48) EXP4(48, 12) MID "s_nop 1\n\t"                       \
    PV(80, 36, 56) EXP4(64, 0)                                                                     \
    "s_waitcnt lgkmcnt(0)\n\t" QK0(48, 8) EXP4(64, 4)                                              \
    QK1(48, 8, KN) EXP4(64, 8)                                                                     \
    PV(96, 32, 64) EXP4(64, 12) "s_nop 1\n\t"
#define PAIR_LAST(T, KC)                                                                           \
    PV(96, 36, 72) VLOAD(T) "s_mov_b64 exec, -1\n\t" EXP4(48, 0)                                   \
    QK0(64, 16) EXP4(48, 4)                                                                        \
    QK1(64, 16, KC) EXP4(48, 8)                                                                    \
    "s_waitcnt lgkmcnt(0)\n\t" PV(80, 32, 48) EXP4(48, 12) "s_nop 1\n\t"                           \
    PV(80, 36, 56) EXP4(64, 0) EXP4(64, 4) EXP4(64, 8)                                             \
    PV(96, 32, 64) EXP4(64, 12) "s_nop 1\n\t"                                                      \
    PV(96, 36, 72) "s_nop 15\n\t"
#define ZERO4(R) "v_mov_b64 v[" #R ":" #R "+1], 0\n\tv_mov_b64 v[" #R "+2:" #R "+3], 0\n\t"
#define MAX2(i) "v_max3_f32 v14, v14, v[116+" #i "], v[117+" #i "]\n\tv_max3_f32 v22, v22, v[132+" #i "], v[133+" #i "]\n\t"
#define PRIO(n, L) "s_cmp_ge_u32 %[psw], 4\n\ts_cbranch_scc1 " #L "f\n\ts_setprio " #n "\n\t" #L ":\n\t"

__global__ __launch_bounds__(768, 3) void gated_mix_kernel(const u16* __restrict__ x, const u16* __restrict__ wA, const u16* __restrict__ wB,
                                                            const u16* __restrict__ woA, const u16* __restrict__ woB,
                                                            const u16* __restrict__ gate_w, char* __restrict__ ws, u16* out) {
    __shared__ __attribute__((aligned(16))) char smem[KV_HEAD];
    lds_ptr lds = (lds_ptr)smem;
    const int tid = threadIdx.x, lane = tid & 63, wid = __builtin_amdgcn_readfirstlane(tid >> 6);
    const int g = lane >> 4, r16 = lane & 15;
    const rsrc_t wrs = __builtin_amdgcn_make_buffer_rsrc(ws, 0, (int)WS_END, 0x00020000);
    const int xcd = blockIdx.x & 7, idx = blockIdx.x >> 3;
    const int mt = (idx & 7) * 2 + (xcd & 1), nt = (xcd >> 1) + (idx >> 3) * 4, m0 = mt * 128;
    if (wid == 8) *(LDS_AS u32x2*)(lds + L_CNT) = u32x2{0u, 0u};
    if (wid >= 8 && nt < 15) {
        const int lw = wid - 8;
        const unsigned xoff = (lw * 8 + (lane >> 3)) * (DM * 2) + (((lane & 7) ^ ((lane >> 3) & 7)) << 4);
        const unsigned woff = (32 * (g >> 1) + 4 * (g & 1) + lw) * 3840 + r16 * 16;
        const char* xs = (const char*)(x + (size_t)m0 * DM);
        const char* bs = (const char*)(wB + nt * 128);
        const char* as = (const char*)(wA + nt * 128);
        unsigned so = (unsigned)(size_t)lds;
#pragma unroll 1
        for (int t = -2; t < 9; ++t) {
            if (t >= 0) {
                WAIT_V(12);
                RAW_BAR();
            }
            if (t < 8) {
#pragma unroll 1
                for (int i = 0; i < 4; ++i) {
                    const unsigned lx = so + (lw + 4 * i) * 1024, lb = so + 16384 + (lw + 4 * i) * K1_WP;
                    glds16x3(xs + i * (32 * DM * 2), xoff, lx, bs + i * (8 * 3840), as + i * (8 * 3840), woff, lb, lb + 16 * K1_WP);
                }
                xs += 128; bs += 64 * 3840; as += 64 * 3840;
            }
            so = so == (unsigned)(size_t)lds + 2 * K1_STAGE ? (unsigned)(size_t)lds : so + K1_STAGE;
        }
        WAIT_V(0);
        RAW_BAR();
    }
    const int head = (blockIdx.x & 7) * 4 + ((blockIdx.x >> 3) & 3), qb = blockIdx.x >> 5;
    const int omt = head, ont = qb;
    const int r32 = lane & 31, hh = lane >> 5;
    const u32x4 vfill = r32 == 20 ? u32x4{ONE2, ONE2, ONE2, ONE2} : u32x4{0u, 0u, 0u, 0u};
    const int voff = hh * 320 + (r32 < 20 ? r32 : 0) * 16;
    const unsigned olane = (r32 >> 4) * (int)O_BLK + (r32 & 15) * 16;
    const int ps = nt < 5 ? (int)Q_TILE : (int)KV_TILE;
    const int wimg = (int)WS_WO + ont * 5120;
    gu64* const f1 = (gu64*)(ws + WS_F1);
    constexpr int P_OFF = 102400;
    const int pslot = g * 256;
    asm volatile("" : "+s"(out));
    unsigned fq, fo;
    auto fin_addr = [&]() {
        const int row = tid / 10, c = tid - row * 10;
        fq = P_OFF + ((row >> 4) * 5 + (c >> 1)) * 1024 + (c & 1) * 512 + (((row & 15) + c) & 15) * 16;
        fo = ((omt * 64 + row) * DM + ont * 80 + c * 8) * 2;
        asm volatile("" : "+v"(fq), "+v"(fo));
    };
    if (nt == 15) {
        const int task = mt * 10 + wid, ks = task >> 2, cg = task & 3;
        lds_ptr st = lds + wid * 10240;
        if (wid < 10) {
            const char* wsrc = (const char*)((ks >= 20 ? woB : woA) + cg * 160);
#pragma unroll
            for (int i = 0; i < 10; ++i) {
                const int ci = i * 64 + lane, slot = ci / 20, cc = ci - slot * 20;
                const int c = (ks >= 20 ? ks - 20 : ks) * 4 + (slot >> 3), e = slot & 7, p = c / 5, r = c - p * 5;
                const int k = r < 4 ? (2 * p + (r >> 1)) * 20 + 4 * (r & 1) + (e & 3) + 8 * (e >> 2) : (2 * p + (e >> 2)) * 20 + 16 + (e & 3);
                glds16(wsrc, k * (DM * 2) + cc * 16, st + i * 1024);
            }
        }
        u32x4 gv[2];
        gv[0] = *(const u32x4*)(gate_w + lane * 8);
        gv[1] = lane < 16 ? *(const u32x4*)(gate_w + 512 + lane * 8) : u32x4{0u, 0u, 0u, 0u};
        float s[11];
#pragma unroll
        for (int u = 0; u < 11; ++u) {
            const int row = wid + 12 * u < 128 ? wid + 12 * u : wid;
            const u16* xr = x + (size_t)(m0 + row) * DM;
            const u32x4 xv[2] = {*(const u32x4*)(xr + lane * 8), *(const u32x4*)(xr + 512 + r16 * 8)};
            float a = 0.f;
#pragma unroll
            for (int c = 0; c < 2; ++c)
#pragma unroll
                for (int e = 0; e < 4; ++e) {
                    a = fmaf(__uint_as_float(xv[c][e] << 16), __uint_as_float(gv[c][e] << 16), a);
                    a = fmaf(__uint_as_float(xv[c][e] & 0xffff0000u), __uint_as_float(gv[c][e] & 0xffff0000u), a);
                }
            s[u] = a;
        }
#pragma unroll
        for (int o = 32; o; o >>= 1)
#pragma unroll
            for (int u = 0; u < 11; ++u) s[u] += __shfl_xor(s[u], o);
        float z = s[0];
#pragma unroll
        for (int u = 1; u < 11; ++u) z = lane == u ? s[u] : z;
        if (lane < 11 && wid + 12 * lane < 128) ST4(__float_as_uint(1.f / (1.f + __expf(-z))), (wid + 12 * lane) * 4, (int)WS_G + m0 * 4);
        WAIT_V(0);
        if (wid < 10) {
            lds_ptr rp = st + (8 * g + (r16 >> 2)) * 320 + (lane & 3) * 8;
#pragma unroll
            for (int j = 0; j < 10; ++j)
                ST16(__builtin_bit_cast(u32x4, cat8(lds_tr(rp + j * 32), lds_tr(rp + j * 32 + 4 * 320))), lane * 16, (int)WS_WO + (ks * 40 + cg * 10 + j) * 1024);
        }
    }
    if (wid >= 8) {
        u64 tag = __hip_atomic_load((gu64*)(ws + WS_EPOCH), RLX_AGENT) + 1;
        const int hw = wid - 8;
        __builtin_amdgcn_s_setprio(3);
        RAW_BAR();
        f32x16 c0, c1;
        {
            const unsigned long long msk = ~(0x15554ull << r32) >> (4 * hh);
            const unsigned mlo = (unsigned)msk, mhi = (unsigned)(msk >> 32);
#pragma unroll
            for (int r = 0; r < 16; ++r) {
                const int u = (r & 3) + 8 * (r >> 2);
                c0[r] = r < 8 ? 0.f : __uint_as_float(__builtin_amdgcn_sbfe(mlo, (u - 16) & 31, 1) & 0xff800000u);
                c1[r] = __uint_as_float(__builtin_amdgcn_sbfe(u < 16 ? mlo : mhi, (u + 16) & 31, 1) & 0xff800000u);
            }
        }
        asm volatile("" : "+v"(c0), "+v"(c1));
        const int fc = head * 20 + (g & 1) * 19;
        gu64* const fkv = f1 + (((g < 2 ? 640 : 1280) + fc) >> 7) * 16 * FS;
        auto go = [&](int n, gu64* w0) {
            volatile LDS_AS int* gw = (volatile LDS_AS int*)(lds + L_CNT + 4);
            if (hw == 3) {
                gu64* const w[1] = {w0};
                wait_flags<2>(w, tag);
                *gw = n;
            } else {
#pragma unroll 1
                while (__builtin_amdgcn_readfirstlane(*gw) < n) __builtin_amdgcn_s_sleep(1);
                asm volatile("" ::: "memory");
            }
        };
        const int qp = (hw < 3 ? hw * 3 : 7) * 1024;
        const char* q0s = ws + WS_Q + (size_t)((NH + head) * 64 + qb * 8) * Q_TILE + qp;
        const char* k0s = ws + WS_KV + (size_t)(NH + head) * KV_HEAD + hw * 7168;
        const char* k1s = k0s + 4096;
        unsigned q0l = (unsigned)(size_t)lds + L_Q + qp, k0l = (unsigned)(size_t)lds + hw * 7168, k1l = k0l + 4096;
        asm volatile("" : "+s"(q0s), "+s"(k0s), "+s"(k1s), "+s"(q0l), "+s"(k0l), "+s"(k1l));
        go(1, r16 < 3 ? fkv + r16 * FS : f1 + ((fc >> 7) * 16 + qb * 2 + (lane & 1)) * FS);
        glds_first(lane * 16, q0s, q0l, k0s, k0l, k1s, k1l);
        const char* src = ws + WS_KV + (size_t)(NH + head) * KV_HEAD + hw * 1024;
#define LOADS(a, b) _Pragma("unroll 1") for (int i = a; i < b; ++i) glds16_dev(src + i * 4096, lane * 16, lds + hw * 1024 + i * 4096)
        WAIT_V(0); RAW_BAR();
        go(2, fkv + r16 * FS);
        LOADS(7, 28);
        WAIT_V(16); RAW_BAR();
        *(LDS_AS int*)lds = 0;
        if (nt < 15) {
            lds_ptr sp = lds + L_STASH + hw * 9216 + lane * 16;
            const u32x4 po = *(const LDS_AS u32x4*)(sp + 8192);
#pragma unroll
            for (int j = 0; j < 4; ++j) {
                const u32x4 pv[2] = {*(const LDS_AS u32x4*)(sp + j * 2048), *(const LDS_AS u32x4*)(sp + j * 2048 + 1024)};
                put_qkv(pv, po[j] & ~7u, (po[j] & ~7u) + (po[j] & 7u) * 128, ps, wrs);
            }
        }
        WAIT_V(0);
        RAW_BAR();
        if (hw == 0 && lane == 0) __hip_atomic_store(f1 + (256 + nt * 16 + mt) * FS, tag, RLX_AGENT);
        LOADS(28, 40);
#pragma unroll
        for (int i = 0; i < 2; ++i)
            put_o(u32x4{~0u, ~0u, ~0u, ~0u}, u32x2{~0u, ~0u}, wrs, olane, (int)WS_O + (128 + (qb * 8 + hw * 2 + i) * 2) * (int)O_BLK, head, hh);
        {
            const int sec = r16 >> 2, fm = max(qb * 2 - 1 + (r16 & 3), 0);
            gu64* const w[1] = {f1 + (256 + (g < 2 && sec < 3 && (r16 & 3) < 3 ? ((sec * 640 + fc) >> 7) * 16 + fm : 240 + r16)) * FS};
            wait_flags<8>(w, tag);
        }
        {
            const int q0 = qb * 8 + hw * 2;
            const int qA = (int)WS_Q + (head * 64 + q0) * (int)Q_TILE, k1 = (int)WS_KV + (head * 64 + q0) * (int)KV_TILE;
            u32x4 aqa[2], ka[3], vf[5];
            u32x2 aqb[2] = {{0u, 0u}, {0u, 0u}}, kb[3];
            float gv[2];
#pragma unroll
            for (int i = 0; i < 3; ++i) {
                const int kt = k1 + (i == 0 ? (q0 ? -(int)KV_TILE : 0) : (i - 1) * (int)KV_TILE);
                ka[i] = LD16(lane * 16, kt);
                kb[i] = LD8(r32 * 8, kt + 1024);
            }
#pragma unroll
            for (int i = 0; i < 2; ++i) aqa[i] = LD16(lane * 16, qA + i * (int)Q_TILE);
            if (!hh) {
#pragma unroll
                for (int i = 0; i < 2; ++i) aqb[i] = LD8(r32 * 8, qA + i * (int)Q_TILE + 1024);
            }
#pragma unroll
            for (int i = 0; i < 5; ++i) vf[i] = vfill;
            if (r32 < 20) {
                vf[0] = LD16(voff, k1 + (q0 ? -(int)KV_TILE : 0) + 1920);
#pragma unroll
                for (int i = 1; i < 5; ++i) vf[i] = LD16(voff, k1 + ((i - 1) >> 1) * (int)KV_TILE + 1280 + ((i - 1) & 1) * 640);
            }
#pragma unroll
            for (int i = 0; i < 2; ++i) gv[i] = __uint_as_float(LD4(r32 * 4, (int)WS_G + (q0 + i) * 128));
#pragma unroll
            for (int i = 1; i >= 0; --i) {
                if (i == 0 && q0 == 0) {
#pragma unroll
                    for (int r = 8; r < 16; ++r) c0[r] = -INFINITY;
                }
                f32x16 s0 = MFMA32(BF8(ka[i]), BF8(aqa[i]), c0);
                f32x16 s1 = MFMA32(BF8(ka[i + 1]), BF8(aqa[i]), c1);
                s0 = MFMA32H(kb[i], aqb[i], s0);
                s1 = MFMA32H(kb[i + 1], aqb[i], s1);
                float m = max3f(s0[8], s0[9], s0[10]);
                m = max3f(m, s0[11], s0[12]); m = max3f(m, s0[13], s0[14]);
                m = xhalf_max(max16(s1, fmaxf(m, s0[15])));
                const u32x2 ref = with_ref(u32x2{0u, 0u}, m, hh);
                s0 = MFMA32H((u32x2{ONE2, ONE2}), ref, s0);
                s1 = MFMA32H((u32x2{ONE2, ONE2}), ref, s1);
#pragma unroll
                for (int r = 8; r < 16; ++r) s0[r] = __builtin_amdgcn_exp2f(s0[r]);
#pragma unroll
                for (int r = 0; r < 16; ++r) s1[r] = __builtin_amdgcn_exp2f(s1[r]);
                f32x16 o = SMFMAC(BF8(vf[2 * i]), HI8(s0), f32x16{});
                o = SMFMAC(BF8(vf[2 * i + 1]), LO8(s1), o);
                o = SMFMAC(BF8(vf[2 * i + 2]), HI8(s1), o);
                const float f = gv[i] * __builtin_amdgcn_rcpf(row_sum(o));
                put_o(u32x4{pack2(o[0] * f, o[1] * f), pack2(o[2] * f, o[3] * f), pack2(o[4] * f, o[5] * f), pack2(o[6] * f, o[7] * f)},
                      u32x2{pack2(o[8] * f, o[9] * f), pack2(o[10] * f, o[11] * f)}, wrs, olane, (int)WS_O + (q0 + i) * 2 * (int)O_BLK, head, hh);
            }
        }
        arrive(lds, (gu64*)(ws + WS_FA) + (qb * 32 + head) * FS, tag);
        f32x4 acc[2][5];
        bf16x8 of[3][2], wf[3][5];
        const int osrc = (int)WS_O + (omt * 4 + (hw & 1) * 2) * (int)O_BLK + (hw >> 1) * 10240, wsrc = wimg + (hw >> 1) * 409600;
        auto ldo = [&](int ks) {
#pragma unroll
            for (int i = 0; i < 2; ++i) of[ks % 3][i] = BF8(LD16(lane * 16, osrc + i * (int)O_BLK + ks * 1024));
        };
        auto ldw = [&](int ks) {
#pragma unroll
            for (int j = 0; j < 5; ++j) wf[ks % 3][j] = BF8(LD16(lane * 16, wsrc + ks * 40960 + j * 1024));
        };
        ldw(0); ldw(1); ldw(2);
        SB();
        RAW_BAR();
        {
            gu64* const w[1] = {(gu64*)(ws + WS_FA) + ((omt >> 2) * 32 + r32) * FS};
            wait_flags<8>(w, tag);
        }
        ldo(0); ldo(1); ldo(2);
        SB();
#pragma unroll
        for (int ks = 0; ks < 10; ++ks) {
#pragma unroll
            for (int j = 0; j < 5; ++j) {
#pragma unroll
                for (int i = 0; i < 2; ++i) acc[i][j] = MFMA16(wf[ks % 3][j], of[ks % 3][i], ks ? acc[i][j] : (f32x4{0.f, 0.f, 0.f, 0.f}));
                SB();
                __builtin_amdgcn_s_sleep(1);
                SB();
            }
            if (ks < 7) { ldo(ks + 3); ldw(ks + 3); SB(); }
        }
#pragma unroll 1
        for (int ks = hw * 5; ks < hw * 5 + 5; ++ks)
#pragma unroll
            for (int j = 0; j < 5; ++j) glds16_dev(ws + wimg + (20 + ks) * 40960 + j * 1024, lane * 16, lds + ks * 5120 + j * 1024);
        RAW_BAR();
        if (blockIdx.x == 0 && hw == 0) {
            gu64* const w[4] = {f1 + (256 + lane) * FS, f1 + (320 + lane) * FS, f1 + (384 + lane) * FS, f1 + (448 + lane) * FS};
            wait_flags<8>(w, tag);
            if (lane == 0) __hip_atomic_store((gu64*)(ws + WS_EPOCH), tag, RLX_AGENT);
        }
        WAIT_V(0);
        auto put = [&]() {
#pragma unroll
            for (int i = 0; i < 2; ++i)
#pragma unroll
                for (int j = 0; j < 5; ++j)
                    *(LDS_AS f32x4*)(lds + P_OFF + (1 + (hw >> 1)) * 20480 + (((hw & 1) * 2 + i) * 5 + j) * 1024 + pslot + ((r16 + 2 * j + (g >> 1)) & 15) * 16) = acc[i][j];
        };
        fin_addr();
        RAW_BAR();
        if (hw >= 2) put();
        RAW_BAR();
        if (hw < 2) put();
    } else {
        if (nt < 15) {
            const u64 tv = __hip_atomic_load((gu64*)(ws + WS_EPOCH), RLX_AGENT) + 1;
            const char* tp;
            if (lane < 16) {
                tp = (const char*)((lane & 2 ? wA : wB) + nt * 128 + (lane & 1) * 64) + (size_t)((wid + 2) * 64 + mt * 4 + (lane >> 2)) * 3840;
            } else {
                const int r = nt * 9 + (lane < 25 ? lane - 16 : 0);
                tp = (const char*)(x + (size_t)(m0 + (r < 127 ? r : 127)) * DM) + (wid + 2) * 128;
            }
            const int br = wid < 4, wm = (wid >> 1) & 1, wn = wid & 1;
            const int xa = (wm * 64 + r16) * 128 + ((g ^ (lane & 7)) << 4);
            const int wa = 16384 + ((wid >> 2) * 16 + 4 * g + (r16 >> 2)) * K1_WP + (wn * 64 + 4 * (lane & 3)) * 2;
            const int tile0 = mt * 4 + wm * 2, cw = nt * 128 + wn * 64;
            unsigned poff[4], poff1[4];
#pragma unroll
            for (int j = 0; j < 4; ++j) {
                if (nt < 10) {
                    const int cp = (cw - (nt < 5 ? 0 : 640)) / 8 + j * 2 + (g >> 1), p = cp / 5, r = cp - p * 5;
                    const unsigned hoff = (int)(nt < 5 ? WS_Q : WS_KV) + ((br * NH + 2 * p) * 64 + tile0) * ps;
                    poff[j] = r == 2 ? hoff + (g & 1) * 64 * ps + 1024 + r16 * 8 : hoff + (r > 2 ? 64 * ps + (r - 3) * 512 : r * 512) + r16 * 16 + (g & 1) * 8;
                    poff1[j] = poff[j] + (r == 2 ? 128 : 256);
                } else {
                    const int c = cw - 1280 + j * 16 + r16, h = c / 20, d = c - h * 20;
                    poff[j] = (int)WS_KV + ((br * NH + h) * 64 + tile0) * (int)KV_TILE + 1280 + (g >> 1) * 320 + d * 16 + (g & 1) * 8;
                    poff1[j] = poff[j] + 640;
                }
            }
            asm volatile("" : "+v"(poff[0]), "+v"(poff[1]), "+v"(poff[2]), "+v"(poff[3]), "+v"(poff1[0]), "+v"(poff1[1]), "+v"(poff1[2]), "+v"(poff1[3]));
            const u64 tag = (u64)__builtin_amdgcn_readfirstlane((unsigned)(tv >> 32)) << 32 | (unsigned)__builtin_amdgcn_readfirstlane((unsigned)tv);
            f32x4 acc[4][4];
#pragma unroll
            for (int i = 0; i < 4; ++i)
#pragma unroll
                for (int j = 0; j < 4; ++j) acc[i][j] = f32x4{0.f, 0.f, 0.f, 0.f};
            if (br) __builtin_amdgcn_s_setprio(1);
            const bool urgent = br && (nt < 5 || mt < 3);
            gu64* bflag = f1 + (nt * 16 + mt) * FS;
            asm volatile("" : "+s"(bflag));
            auto mid = [&]() {
                if (!urgent) RAW_BAR();
                if (br && !urgent) __builtin_amdgcn_s_sleep(16);
            };
            auto fin = [&](int j) {
                if (nt < 5) {
#pragma unroll
                    for (int i = 0; i < 4; ++i) acc[i][j] *= QSCALE;
                }
                u32x4 pv[2];
#pragma unroll
                for (int i = 0; i < 2; ++i) {
                    const f32x4 a = acc[2 * i][j], b = acc[2 * i + 1][j];
                    pv[i] = u32x4{pack2(a[0], a[1]), pack2(a[2], a[3]), pack2(b[0], b[1]), pack2(b[2], b[3])};
                }
                if (br) {
                    put_qkv(pv, poff[j], poff1[j], ps, wrs);
                } else {
#pragma unroll
                    for (int i = 0; i < 2; ++i) *(LDS_AS u32x4*)(lds + L_STASH + (wid - 4) * 9216 + j * 2048 + i * 1024 + lane * 16) = pv[i];
                }
            };
            if (nt < 10) k1_main<false>(acc, lds, xa, wa, tp, wid - 1, mid, fin);
            else k1_main<true>(acc, lds, xa, wa, tp, wid - 1, mid, fin);
            if (urgent) RAW_BAR();
            if (br) {
                arrive(lds + L_CNT, bflag, tag);
            } else {
                *(LDS_AS u32x4*)(lds + L_STASH + (wid - 4) * 9216 + 8192 + lane * 16) =
                    u32x4{poff[0] + ((poff1[0] - poff[0]) >> 7), poff[1] + ((poff1[1] - poff[1]) >> 7), poff[2] + ((poff1[2] - poff[2]) >> 7), poff[3] + ((poff1[3] - poff[3]) >> 7)};
            }
        } else {
            RAW_BAR();
            WAIT_V(0);
        }
        const int qt = qb * 8 + wid;
        const unsigned long long vmask = 0x000fffff000fffffull;
        f32x16 o, o2;
        float gate;
        u32x4 qa0, vlo = vfill, vhi = vfill;
        u32x2 qb0;
        const unsigned l0 = (unsigned)(size_t)lds, kap = (wid >> 2) * (int)KV_TILE;
        unsigned a_ka = l0 + kap + lane * 16, a_kb = l0 + kap + r32 * 8, a_v = l0 + kap + voff;
        const unsigned qs0 = L_Q + wid * (int)Q_TILE - kap, qs1 = L_Q + (wid ^ 4) * (int)Q_TILE - kap;
        int nbar = 0x53, it = 7;
        __builtin_amdgcn_s_setprio(1);
        asm volatile(
            "v_mov_b32 v28, %[one]\n\tv_mov_b32 v29, %[one]\n\tv_mov_b32 v30, %[one]\n\tv_mov_b32 v31, %[one]\n\tv_mov_b32 v40, %[sidx]\n\t"
            ZERO4(72) ZERO4(76) ZERO4(80) ZERO4(84) ZERO4(88) ZERO4(92) ZERO4(96) ZERO4(100) ZERO4(104) ZERO4(108)
            "v_mov_b64 v[12:13], 0\n\tv_mov_b64 v[20:21], 0\n\t"
            "v_add_u32 v48, %[qs0], v41\n\tv_add_u32 v49, %[qs1], v41\n\tv_subrev_u32 v50, %[kap], v41\n\t"
            "v_add_u32 v51, %[qs0], v42\n\tv_add_u32 v52, %[qs1], v42\n\tv_subrev_u32 v53, %[kap], v42\n\t"
            "s_waitcnt lgkmcnt(0)\n\ts_barrier\n\t"
            "ds_read_b128 v[112:115], v50\n\tds_read_b128 v[8:11], v48\n\tds_read_b128 v[16:19], v49\n\t"
            "s_mov_b64 exec, %[km]\n\t"
            "ds_read_b64 v[30:31], v53 offset:1024\n\tds_read_b64 v[12:13], v51 offset:1024\n\tds_read_b64 v[20:21], v52 offset:1024\n\t"
            "ds_read_b64 v[28:29], v42 offset:1024\n\t"
            "s_mov_b64 exec, -1\n\t"
            "ds_read_b128 v[24:27], v41\n\t"
            "s_waitcnt lgkmcnt(5)\n\t"
            "v_mfma_f32_32x32x16_bf16 v[116:131], v[112:115], v[8:11], 0\n\t"
            "v_mfma_f32_32x32x16_bf16 v[132:147], v[112:115], v[16:19], 0\n\t"
            "s_waitcnt lgkmcnt(0)\n\t"
            QK1(116, 8, 30) QK1(132, 16, 30) QK0(48, 8)
            "s_nop 10\n\t"
            "v_max_f32 v14, v116, v117\n\tv_max_f32 v22, v132, v133\n\t"
            MAX2(2) MAX2(4) MAX2(6) MAX2(8) MAX2(10) MAX2(12) MAX2(14)
            "v_mov_b32 v15, v14\n\tv_mov_b32 v23, v22\n\ts_nop 1\n\t"
            "v_permlane32_swap_b32 v14, v15\n\tv_permlane32_swap_b32 v22, v23\n\t"
            "s_not_b64 exec, %[km]\n\t"
            "v_min_f32 v14, -v14, -v15\n\tv_min_f32 v22, -v22, -v23\n\t"
            "v_and_b32 v12, 0xffff0000, v14\n\tv_and_b32 v20, 0xffff0000, v22\n\t"
            "s_mov_b64 exec, -1\n\ts_nop 1\n\t"
            QK1(48, 8, 28)
            "s_nop 5\n\t"
            "1:\n\t"
            PRIO(2, 3) PAIR(0, 28, 30, "") PAIR(1, 30, 28, "") PAIR(2, 28, 30, PRIO(0, 4)) PAIR(3, 30, 28, "")
            "v_add_u32 v41, 20480, v41\n\tv_add_u32 v42, 20480, v42\n\tv_add_u32 v43, 20480, v43\n\t"
            "s_bitcmp0_b32 %[nbar], 0\n\t"
            "s_cbranch_scc1 2f\n\t"
            "s_barrier\n\t"
            "2:\n\t"
            "s_lshr_b32 %[nbar], %[nbar], 1\n\t"
            "s_sub_i32 %[it], %[it], 1\n\t"
            "s_cmp_lg_u32 %[it], 0\n\t"
            "s_cbranch_scc1 1b\n\t"
            "global_load_dword v44, %[goff], %[gb] sc1\n\t"
            PRIO(2, 3) PAIR(0, 28, 30, "") PAIR(1, 30, 28, "") PAIR(2, 28, 30, PRIO(0, 4)) PAIR_LAST(3, 30)
            "s_waitcnt vmcnt(0)"
            : "=&{v44}"(gate), "=&{v[80:95]}"(o), "=&{v[96:111]}"(o2), "=&{v[8:11]}"(qa0), "=&{v[12:13]}"(qb0), "+{v[32:35]}"(vlo),
              "+{v[36:39]}"(vhi), "+{v41}"(a_ka), "+{v42}"(a_kb), "+{v43}"(a_v), [nbar] "+s"(nbar), [it] "+s"(it)
            : [vm] "s"(vmask), [km] "s"(0xffffffffull), [one] "s"(ONE2), [sidx] "s"(0xDDDDDDDDu), [psw] "s"(wid), [goff] "v"(r32 * 4),
              [gb] "s"(ws + WS_G + qt * 128), [qs0] "s"(qs0), [qs1] "s"(qs1), [kap] "s"(kap)
            : "v14", "v15", "v16", "v17", "v18", "v19", "v20", "v21", "v22", "v23", "v24", "v25", "v26", "v27", "v28", "v29", "v30",
              "v31", "v40", "v48", "v49", "v50", "v51", "v52", "v53", "v54", "v55", "v56", "v57", "v58", "v59", "v60", "v61", "v62",
              "v63", "v64", "v65", "v66", "v67", "v68", "v69", "v70", "v71", "v72", "v73", "v74", "v75", "v76", "v77", "v78", "v79",
              "v112", "v113", "v114", "v115", "v116", "v117", "v118", "v119", "v120", "v121", "v122", "v123", "v124", "v125", "v126",
              "v127", "v128", "v129", "v130", "v131", "v132", "v133", "v134", "v135", "v136", "v137", "v138", "v139", "v140", "v141",
              "v142", "v143", "v144", "v145", "v146", "v147", "scc", "memory");
        __builtin_amdgcn_s_setprio(3);
#pragma unroll
        for (int i = 0; i < 3; ++i)
            *(LDS_AS f32x4*)(lds + P_OFF + (wid ^ 4) * 3072 + i * 1024 + lane * 16) = f32x4{o2[4 * i], o2[4 * i + 1], o2[4 * i + 2], o2[4 * i + 3]};
        RAW_BAR();
#pragma unroll
        for (int i = 0; i < 3; ++i) {
            const f32x4 t = *(const LDS_AS f32x4*)(lds + P_OFF + wid * 3072 + i * 1024 + lane * 16);
#pragma unroll
            for (int e = 0; e < 4; ++e) o[4 * i + e] += t[e];
        }
        const float gB = 1.f - gate;
        float l = row_sum(o);
        float asum = 0.f;
#pragma unroll
        for (int i = 0; i < 12; ++i) asum += fabsf(o[i]);
        if (__builtin_expect(__ballot(l >= 0x1p-64f && l <= 0x1p64f && asum <= 0x1p126f) == ~0ull, 1)) {
            store_o(o, gB * __builtin_amdgcn_rcpf(l), wrs, olane, (int)WS_O + (128 + qt * 2) * (int)O_BLK, head, hh);
        } else {
            const int kv = (int)WS_KV + (NH + head) * (int)KV_HEAD;
            u32x2 qr = hh ? u32x2{0u, 0u} : qb0;
            float mt2 = -INFINITY;
            o = f32x16{};
#pragma unroll 1
            for (int t = 0; t < 128; ++t) {
                const int kt = kv + (t & 63) * (int)KV_TILE;
                u32x2 kb = {ONE2, ONE2};
                if (!hh) kb = LD8(r32 * 8, kt + 1024);
                f32x16 sc = MFMA32(BF8(LD16(lane * 16, kt)), BF8(qa0), f32x16{});
                sc = MFMA32H(kb, qr, sc);
                if (t < 64) {
                    mt2 = max16(sc, mt2);
                    if (t == 63) qr = with_ref(qb0, xhalf_max(mt2), hh);
                } else {
                    u32x4 v0 = vfill, v1 = vfill;
                    if (r32 < 20) { v0 = LD16(voff, kt + 1280); v1 = LD16(voff, kt + 1920); }
#pragma unroll
                    for (int r = 0; r < 16; ++r) sc[r] = __builtin_amdgcn_exp2f(sc[r]);
                    o = SMFMAC(BF8(v0), LO8(sc), o);
                    o = SMFMAC(BF8(v1), HI8(sc), o);
                }
            }
            l = row_sum(o);
            asm volatile("" : "+v"(l));
            store_o(o, gB * __builtin_amdgcn_rcpf(l), wrs, olane, (int)WS_O + (128 + qt * 2) * (int)O_BLK, head, hh);
        }
        RAW_BAR();
        const int mb = wid & 3, kh = wid >> 2;
        const int osrc = (int)WS_O + (128 + omt * 4 + mb) * (int)O_BLK + kh * 10240;
        u32x4 of[10];
#pragma unroll
        for (int s = 0; s < 10; ++s) of[s] = LD16(lane * 16, osrc + s * 1024);
        SB();
        f32x4 acc[5];
        lds_ptr pp = lds + P_OFF + kh * 40960 + mb * 5120 + pslot;
#pragma unroll
        for (int j = 0; j < 5; ++j) acc[j] = f32x4{0.f, 0.f, 0.f, 0.f};
        if (kh) {
#pragma unroll
            for (int j = 0; j < 5; ++j) acc[j] = *(const LDS_AS f32x4*)(pp + j * 1024 + ((r16 + 2 * j + (g >> 1)) & 15) * 16);
        }
        lds_ptr wp = lds + kh * 51200 + lane * 16;
        bf16x8 wf[2][10];
#pragma unroll
        for (int j = 0; j < 10; ++j) wf[0][j] = lds_b128(wp + j * 1024);
        auto bad = [&](int s) {
            const u32x4 a = __builtin_elementwise_max(of[s], of[s + 1]);
            return __any(max(max(a[0], a[1]), max(a[2], a[3])) == ~0u);
        };
#pragma unroll
        for (int s = 0; s < 10; s += 2) {
            if (s < 8) {
#pragma unroll
                for (int j = 0; j < 10; ++j) wf[(s / 2 + 1) & 1][j] = lds_b128(wp + (s + 2) * 5120 + j * 1024);
            }
            if (bad(s)) {
#pragma unroll 1
                for (unsigned spins = 0; spins < (1u << 18); ++spins) {
                    asm volatile("" ::: "memory");
#pragma unroll
                    for (int t = 8; t >= s; t -= 2)
                        if (t == s || bad(t)) { of[t] = LD16(lane * 16, osrc + t * 1024); of[t + 1] = LD16(lane * 16, osrc + (t + 1) * 1024); }
                    if (!bad(s)) break;
                }
            }
            if (s == 2) fin_addr();
#pragma unroll
            for (int j = 0; j < 10; ++j) acc[j % 5] = MFMA16(wf[(s / 2) & 1][j], BF8(of[s + j / 5]), acc[j % 5]);
        }
#pragma unroll
        for (int j = 0; j < 5; ++j) *(LDS_AS f32x4*)(pp + j * 1024 + ((r16 + 2 * j + (g >> 1)) & 15) * 16) = acc[j];
    }
    RAW_BAR();
    if (tid < 640) {
        f32x4 t[6];
#pragma unroll
        for (int p = 0; p < 6; ++p) t[p] = *(const LDS_AS f32x4*)(lds + fq + (p >> 1) * 20480 + (p & 1) * 256);
        SB();
        const f32x4 a = t[0] + t[2] + t[4], b = t[1] + t[3] + t[5];
        const u32x4 v = {pack2(a[0], a[1]), pack2(a[2], a[3]), pack2(b[0], b[1]), pack2(b[2], b[3])};
        __builtin_amdgcn_raw_buffer_store_b128(v, __builtin_amdgcn_make_buffer_rsrc(out, 0, SEQ * DM * 2, 0x00020000), fo, 0, SC1);
    }
}

extern "C" void kernel_launch(void* const* d_in, const int* in_sizes, int n_in, void* d_out, int out_size, void* d_ws, size_t ws_size,
                              hipStream_t stream) {
    gated_mix_kernel<<<256, 768, 0, stream>>>((const u16*)d_in[0], (const u16*)d_in[1], (const u16*)d_in[4], (const u16*)d_in[2],
                                              (const u16*)d_in[5], (const u16*)d_in[6], (char*)d_ws, (u16*)d_out);
}
```

```cpp
#include <hip/hip_runtime.h>
#include <hip/hip_bf16.h>

typedef __attribute__((ext_vector_type(8))) __bf16 bf16x8;
typedef __attribute__((ext_vector_type(16))) __bf16 bf16x16;
typedef __attribute__((ext_vector_type(2))) __bf16 bf16x2;
typedef __attribute__((ext_vector_type(4))) short s16x4;
typedef __attribute__((ext_vector_type(2))) float f32x2;
typedef __attribute__((ext_vector_type(4))) float f32x4;
typedef __attribute__((ext_vector_type(16))) float f32x16;
typedef __attribute__((ext_vector_type(2))) unsigned u32x2;
typedef __attribute__((ext_vector_type(4))) unsigned u32x4;
typedef unsigned short u16;
#define LDS_AS __attribute__((address_space(3)))
typedef LDS_AS char* lds_ptr;

constexpr int SEQ = 2048, DM = 640, NH = 32;
constexpr size_t KV_TILE = 2560, KV_HEAD = 64 * KV_TILE;
constexpr size_t Q_TILE = 1280, Q_HEAD = 64 * Q_TILE;
constexpr size_t WS_KV = 0;
constexpr size_t WS_Q = WS_KV + 2 * NH * KV_HEAD;
constexpr size_t O_BLK = 80 * 256;
constexpr size_t WS_O = WS_Q + 2 * NH * Q_HEAD;
constexpr size_t WS_WO = WS_O + 2 * 128 * O_BLK;
constexpr size_t WS_G = WS_WO + 40 * 40 * 1024;
constexpr size_t WS_F1 = WS_G + SEQ * 4;
constexpr int FS = 16;
constexpr size_t WS_FA = WS_F1 + 512 * 128;
constexpr size_t WS_EPOCH = WS_FA + 256 * 128;
constexpr size_t WS_END = WS_EPOCH + 128;
constexpr float QSCALE = 0.22360679774997896f * 1.4426950408889634f;

#define LOOK 3
#define PFV 0
#define TMIN -1
#define XSTEP -1
#define W0 0
#define WAIT_V(n) asm volatile("s_waitcnt vmcnt(" #n ")" ::: "memory")
#define RAW_BAR() asm volatile("s_waitcnt lgkmcnt(0)\n\ts_barrier" ::: "memory")
#define MFMA16(a, b, c) __builtin_amdgcn_mfma_f32_16x16x32_bf16(a, b, c, 0, 0, 0)
#define MFMA32(a, b, c) __builtin_amdgcn_mfma_f32_32x32x16_bf16(a, b, c, 0, 0, 0)
#define MFMA32H(a, b, c) __builtin_amdgcn_mfma_f32_32x32x8bf16_1k(__builtin_bit_cast(s16x4, a), __builtin_bit_cast(s16x4, b), c, 0, 0, 0)

__device__ __forceinline__ unsigned pack2(float lo, float hi) {
    f32x2 v = {lo, hi};
    return __builtin_bit_cast(unsigned, __builtin_convertvector(v, bf16x2));
}
__device__ __forceinline__ void glds16(const char* sbase, unsigned voff, lds_ptr l) {
    unsigned keep;
    asm volatile("s_mov_b32 %0, m0\n\ts_mov_b32 m0, %3\n\ts_nop 0\n\tglobal_load_lds_dwordx4 %1, %2\n\ts_mov_b32 m0, %0"
                 : "=&s"(keep) : "v"(voff), "s"(sbase), "s"((unsigned)(size_t)l) : "memory");
}
__device__ __forceinline__ void glds16x3(const char* s0, unsigned v0, unsigned l0, const char* s1, const char* s2, unsigned v1, unsigned l1, unsigned l2) {
    unsigned keep;
    asm volatile("s_mov_b32 %0, m0\n\ts_mov_b32 m0, %3\n\ts_nop 0\n\tglobal_load_lds_dwordx4 %1, %2\n\t"
                 "s_mov_b32 m0, %7\n\ts_nop 0\n\tglobal_load_lds_dwordx4 %4, %5\n\t"
                 "s_mov_b32 m0, %8\n\ts_nop 0\n\tglobal_load_lds_dwordx4 %4, %6\n\ts_mov_b32 m0, %0"
                 : "=&s"(keep) : "v"(v0), "s"(s0), "s"(l0), "v"(v1), "s"(s1), "s"(s2), "s"(l1), "s"(l2) : "memory");
}
__device__ __forceinline__ bf16x8 lds_b128(lds_ptr p) { return *(const LDS_AS bf16x8*)p; }
__device__ __forceinline__ s16x4 lds_tr(lds_ptr p) { return __builtin_amdgcn_ds_read_tr16_b64_v4i16((LDS_AS s16x4*)p); }
__device__ __forceinline__ bf16x8 cat8(s16x4 a, s16x4 b) {
    return __builtin_bit_cast(bf16x8, __builtin_shufflevector(a, b, 0, 1, 2, 3, 4, 5, 6, 7));
}
typedef __amdgpu_buffer_rsrc_t rsrc_t;
typedef unsigned long long u64;
typedef __attribute__((address_space(1))) u64 gu64;
#define RLX_AGENT __ATOMIC_RELAXED, __HIP_MEMORY_SCOPE_AGENT
constexpr int SC1 = 16;
#define LD16(voff, soff) __builtin_amdgcn_raw_buffer_load_b128(wrs, voff, soff, SC1)
#define LD8(voff, soff) __builtin_amdgcn_raw_buffer_load_b64(wrs, voff, soff, SC1)
#define LD4(voff, soff) __builtin_amdgcn_raw_buffer_load_b32(wrs, voff, soff, SC1)
#define ST16(v, voff, soff) __builtin_amdgcn_raw_buffer_store_b128(v, wrs, (voff) + (soff), 0, SC1)
#define ST8(v, voff, soff) __builtin_amdgcn_raw_buffer_store_b64(v, wrs, voff, soff, SC1)
#define ST4(v, voff, soff) __builtin_amdgcn_raw_buffer_store_b32(v, wrs, voff, soff, SC1)
#define BF8(x) __builtin_bit_cast(bf16x8, x)
__device__ __forceinline__ void glds16_dev(const char* sbase, unsigned voff, lds_ptr l) {
    unsigned keep;
    asm volatile("s_mov_b32 %0, m0\n\ts_mov_b32 m0, %3\n\ts_nop 0\n\tglobal_load_lds_dwordx4 %1, %2 sc1\n\ts_mov_b32 m0, %0"
                 : "=&s"(keep) : "v"(voff), "s"(sbase), "s"((unsigned)(size_t)l) : "memory");
}
#define GL_(v, s, o) "global_load_lds_dwordx4 " v ", " s " offset:" #o " sc1\n\t"
__device__ __forceinline__ void glds_first(unsigned voff, const char* s0, unsigned l0, const char* s1, unsigned l1, const char* s2, unsigned l2) {
    unsigned keep;
    asm volatile("s_mov_b32 %0, m0\n\ts_mov_b32 m0, %3\n\ts_nop 0\n\t" GL_("%1", "%2", 0) GL_("%1", "%2", 1024) GL_("%1", "%2", 2048)
                 "s_mov_b32 m0, %5\n\ts_nop 0\n\t" GL_("%1", "%4", 0) GL_("%1", "%4", 1024) GL_("%1", "%4", 2048) GL_("%1", "%4", 3072)
                 "s_mov_b32 m0, %7\n\ts_nop 0\n\t" GL_("%1", "%6", 0) GL_("%1", "%6", 1024) GL_("%1", "%6", 2048)
                 "s_mov_b32 m0, %0"
                 : "=&s"(keep) : "v"(voff), "s"(s0), "s"(l0), "s"(s1), "s"(l1), "s"(s2), "s"(l2) : "memory");
}
__device__ __forceinline__ void arrive(lds_ptr cnt, gu64* flag, u64 tag) {
    unsigned r, sr;
    asm volatile("s_waitcnt vmcnt(0)\n\ts_mov_b64 exec, 1\n\tds_add_rtn_u32 %0, %2, %3\n\ts_waitcnt lgkmcnt(0)\n\t"
                 "v_readfirstlane_b32 %1, %0\n\ts_cmp_lg_u32 %1, 3\n\ts_cbranch_scc1 1f\n\t"
                 "global_store_dwordx2 %4, %5, %6 sc1\n\t1:\n\ts_mov_b64 exec, -1"
                 : "=&v"(r), "=&s"(sr) : "v"((unsigned)(size_t)cnt), "v"(1u), "v"(0u), "v"(tag), "s"(flag) : "scc", "memory");
}
template <int SLEEP, int N>
__device__ __forceinline__ void wait_flags(gu64* const (&w)[N], u64 tag) {
#pragma unroll 1
    for (unsigned spins = 0; spins < (1u << 18); ++spins) {
        bool ok = true;
#pragma unroll
        for (int i = 0; i < N; ++i) ok &= __hip_atomic_load(w[i], RLX_AGENT) == tag;
        if (__all(ok)) break;
        __builtin_amdgcn_s_sleep(SLEEP);
    }
    __builtin_amdgcn_fence(__ATOMIC_ACQUIRE, "wavefront");
}
constexpr int K1_WP = 1024 + 32;
constexpr int K1_STAGE = 16384 + 32 * K1_WP;
constexpr int L_Q = 112 * 1024, L_STASH = 122 * 1024, L_CNT = 163824;
#define SB() __builtin_amdgcn_sched_barrier(0)
__device__ __forceinline__ void put_qkv(const u32x4 (&pv)[2], unsigned off0, unsigned off1, int ps, rsrc_t wrs) {
#pragma unroll
    for (int i = 0; i < 2; ++i) {
        ST8((u32x2{pv[i][0], pv[i][1]}), off0, i * ps);
        ST8((u32x2{pv[i][2], pv[i][3]}), off1, i * ps);
    }
}

template <bool VT, class M, class F>
__device__ __forceinline__ void k1_main(f32x4 (&acc)[4][4], lds_ptr lds, int xa, int wa, const char* tp, int tstep, unsigned& d0, M mid, F fin) {
#define TOUCH() asm volatile("global_load_dword %0, %1, off" : "+v"(d0) : "v"(tp) : "memory")
    bf16x8 xf[2][4], wf[2][4];
    auto rd = [&](int so, int kk) {
#pragma unroll
        for (int j = 0; j < 4; ++j) {
            lds_ptr p = lds + so + wa + kk * 512 + j * 32;
            wf[kk][j] = cat8(lds_tr(p), lds_tr(p + 256));
        }
#pragma unroll
        for (int i = 0; i < 4; ++i) xf[kk][i] = lds_b128(lds + so + (xa ^ (kk * 64)) + i * 2048);
    };
    auto mm = [&](int kk) {
#pragma unroll
        for (int i = 0; i < 4; ++i)
#pragma unroll
            for (int j = 0; j < 4; ++j)
                acc[i][j] = VT ? MFMA16(xf[kk][i], wf[kk][j], acc[i][j]) : MFMA16(wf[kk][j], xf[kk][i], acc[i][j]);
    };
#define ILV() do { _Pragma("unroll") for (int q_ = 0; q_ < 12; ++q_) { __builtin_amdgcn_sched_group_barrier(0x100, 1, 0); __builtin_amdgcn_sched_group_barrier(0x8, 1, 0); } __builtin_amdgcn_sched_group_barrier(0x8, 4, 0); SB(); } while (0)
    RAW_BAR();
    if (tstep == 0) TOUCH();
    rd(0, 0); SB();
    rd(0, 1);
    mm(0); ILV();
    int so = K1_STAGE;
#pragma unroll 1
    for (int t = 1; t < 10; ++t) {
        RAW_BAR();
        if (tstep == t) TOUCH();
        rd(so, 0);
        mm(1); ILV();
        rd(so, 1);
        mm(0); ILV();
        so = so == 2 * K1_STAGE ? 0 : so + K1_STAGE;
    }
    asm volatile("s_waitcnt vmcnt(0)" : "+v"(d0) :: "memory");
    mid();
#pragma unroll
    for (int j = 0; j < 5; ++j) {
        if (j < 4) {
#pragma unroll
            for (int i = 0; i < 4; ++i) acc[i][j] = VT ? MFMA16(xf[1][i], wf[1][j], acc[i][j]) : MFMA16(wf[1][j], xf[1][i], acc[i][j]);
        }
        SB();
        if (j) fin(j - 1);
        SB();
    }
}

#define SMFMAC(a, b8, c) __builtin_amdgcn_smfmac_f32_32x32x32_bf16(a, __builtin_bit_cast(bf16x16, b8), c, (int)0xDDDDDDDD, 0, 0)
#define LO8(s) __builtin_shufflevector(s, s, 0, 1, 2, 3, 4, 5, 6, 7)
#define HI8(s) __builtin_shufflevector(s, s, 8, 9, 10, 11, 12, 13, 14, 15)
constexpr unsigned ONE2 = 0x3F803F80u;

__device__ __forceinline__ float xhalf_max(float m) {
    auto r = __builtin_amdgcn_permlane32_swap(__float_as_uint(m), __float_as_uint(m), false, false);
    return fmaxf(__uint_as_float(r[0]), __uint_as_float(r[1]));
}
__device__ __forceinline__ float max3f(float a, float b, float c) { return __builtin_fmaxf(__builtin_fmaxf(a, b), c); }
__device__ __forceinline__ float max16(const f32x16& s, float m) {
#pragma unroll
    for (int r = 0; r < 16; r += 2) m = max3f(m, s[r], s[r + 1]);
    return m;
}
__device__ __forceinline__ float row_sum(const f32x16& o) {
    auto r = __builtin_amdgcn_permlane32_swap(__float_as_uint(o[8]), __float_as_uint(o[8]), false, false);
    return __uint_as_float(r[1]);
}
__device__ __forceinline__ u32x2 with_ref(u32x2 q, float m, int hh) {
    const unsigned h1 = __float_as_uint(-m) & 0xffff0000u;
    const float r1 = -m - __uint_as_float(h1);
    const unsigned h2 = __float_as_uint(r1) & 0xffff0000u;
    const unsigned h3 = __float_as_uint(r1 - __uint_as_float(h2)) >> 16;
    return hh ? u32x2{h1 | (h2 >> 16), h3} : q;
}
__device__ __forceinline__ void put_o(u32x4 v, u32x2 t, rsrc_t wrs, unsigned voff, int soff, int head, int hh) {
    const int c0 = (head >> 1) * 5;
    ST16(v, voff + hh * 256, soff + (c0 + (head & 1) * 2) * 256);
    if (!hh) ST8(t, voff, soff + (c0 + 4) * 256 + (head & 1) * 8);
}
__device__ __forceinline__ unsigned pack_o(float a, float b) { return min(pack2(a, b), 0xFFFFFFFEu); }
__device__ __forceinline__ void store_o(const f32x16& o, float f, rsrc_t wrs, unsigned voff, int soff, int head, int hh) {
    put_o(u32x4{pack_o(o[0] * f, o[1] * f), pack_o(o[2] * f, o[3] * f), pack_o(o[4] * f, o[5] * f), pack_o(o[6] * f, o[7] * f)},
          u32x2{pack_o(o[8] * f, o[9] * f), pack_o(o[10] * f, o[11] * f)}, wrs, voff, soff, head, hh);
}

#define EXP4(C, a) "v_exp_f32 v[" #C "+" #a "], v[" #C "+" #a "]\n\tv_exp_f32 v[" #C "+" #a "+1], v[" #C "+" #a "+1]\n\t"         \
                   "v_exp_f32 v[" #C "+" #a "+2], v[" #C "+" #a "+2]\n\tv_exp_f32 v[" #C "+" #a "+3], v[" #C "+" #a "+3]\n\t"
#define PV(O, V, P) "v_smfmac_f32_32x32x32_bf16 v[" #O ":" #O "+15], v[" #V ":" #V "+3], v[" #P ":" #P "+7], v40\n\t"
#define QK0(S, Q) "v_mfma_f32_32x32x16_bf16 v[" #S ":" #S "+15], v[24:27], v[" #Q ":" #Q "+3], 0\n\t"
#define QK1(S, Q, K) "v_mfma_f32_32x32x8bf16_1k v[" #S ":" #S "+15], v[" #K ":" #K "+1], v[" #Q "+4:" #Q "+5], v[" #S ":" #S "+15]\n\t"
#define VLOAD(T) "s_mov_b64 exec, %[vm]\n\tds_read_b128 v[32:35], v43 offset:" #T "*5120+1280\n\t"                                \
                 "ds_read_b128 v[36:39], v43 offset:" #T "*5120+1920\n\t"
#define KBLOAD(T, K) "s_mov_b64 exec, %[km]\n\tds_read_b64 v[" #K ":" #K "+1], v42 offset:(" #T ")*5120+1024\n\ts_mov_b64 exec, -1\n\t"
#define PAIR(T, KC, KN, MID)                                                                       \
    PV(96, 36, 72) VLOAD(T) KBLOAD(T + 1, KN) EXP4(48, 0)                                          \
    QK0(64, 16) "ds_read_b128 v[24:27], v41 offset:(" #T "+1)*5120\n\t" EXP4(48, 4)                \
    QK1(64, 16, KC) EXP4(48, 8)                                                                    \
    "s_waitcnt lgkmcnt(2)\n\t" PV(80, 32, 48) EXP4(48, 12) MID "s_nop 1\n\t"                       \
    PV(80, 36, 56) EXP4(64, 0)                                                                     \
    "s_waitcnt lgkmcnt(0)\n\t" QK0(48, 8) EXP4(64, 4)                                              \
    QK1(48, 8, KN) EXP4(64, 8)                                                                     \
    PV(96, 32, 64) EXP4(64, 12) "s_nop 1\n\t"
#define PAIR_LAST(T, KC)                                                                           \
    PV(96, 36, 72) VLOAD(T) "s_mov_b64 exec, -1\n\t" EXP4(48, 0)                                   \
    QK0(64, 16) EXP4(48, 4)                                                                        \
    QK1(64, 16, KC) EXP4(48, 8)                                                                    \
    "s_waitcnt lgkmcnt(0)\n\t" PV(80, 32, 48) EXP4(48, 12) "s_nop 1\n\t"                           \
    PV(80, 36, 56) EXP4(64, 0) EXP4(64, 4) EXP4(64, 8)                                             \
    PV(96, 32, 64) EXP4(64, 12) "s_nop 1\n\t"                                                      \
    PV(96, 36, 72) "s_nop 15\n\t"
#define ZERO4(R) "v_mov_b64 v[" #R ":" #R "+1], 0\n\tv_mov_b64 v[" #R "+2:" #R "+3], 0\n\t"
#define MAX2(i) "v_max3_f32 v14, v14, v[116+" #i "], v[117+" #i "]\n\tv_max3_f32 v22, v22, v[132+" #i "], v[133+" #i "]\n\t"
#define PRIO(n, L) "s_cmp_ge_u32 %[psw], 4\n\ts_cbranch_scc1 " #L "f\n\ts_setprio " #n "\n\t" #L ":\n\t"

__global__ __launch_bounds__(768, 3) void gated_mix_kernel(const u16* __restrict__ x, const u16* __restrict__ wA, const u16* __restrict__ wB,
                                                            const u16* __restrict__ woA, const u16* __restrict__ woB,
                                                            const u16* __restrict__ gate_w, char* __restrict__ ws, u16* out) {
    __shared__ __attribute__((aligned(16))) char smem[KV_HEAD];
    lds_ptr lds = (lds_ptr)smem;
    const int tid = threadIdx.x, lane = tid & 63, wid = __builtin_amdgcn_readfirstlane(tid >> 6);
    const int g = lane >> 4, r16 = lane & 15;
    const rsrc_t wrs = __builtin_amdgcn_make_buffer_rsrc(ws, 0, (int)WS_END, 0x00020000);
    const int xcd = blockIdx.x & 7, idx = blockIdx.x >> 3;
    const int mt = (idx & 7) * 2 + (xcd & 1), nt = (xcd >> 1) + (idx >> 3) * 4, m0 = mt * 128;
    if (wid == 8) *(LDS_AS u32x2*)(lds + L_CNT) = u32x2{0u, 0u};
    if (wid >= 8 && nt < 15) {
        const int lw = wid - 8;
        const unsigned xoff = (lw * 8 + (lane >> 3)) * (DM * 2) + (((lane & 7) ^ ((lane >> 3) & 7)) << 4);
        const unsigned woff = (32 * (g >> 1) + 4 * (g & 1) + lw) * 3840 + r16 * 16;
        const char* xs = (const char*)(x + (size_t)m0 * DM);
        const char* bs = (const char*)(wB + nt * 128);
        const char* as = (const char*)(wA + nt * 128);
        unsigned so = (unsigned)(size_t)lds;
#pragma unroll 1
        for (int t = -2; t < 9; ++t) {
            if (t >= 0) {
                WAIT_V(12);
                RAW_BAR();
            }
            if (t < 8) {
#pragma unroll 1
                for (int i = 0; i < 4; ++i) {
                    const unsigned lx = so + (lw + 4 * i) * 1024, lb = so + 16384 + (lw + 4 * i) * K1_WP;
                    glds16x3(xs + i * (32 * DM * 2), xoff, lx, bs + i * (8 * 3840), as + i * (8 * 3840), woff, lb, lb + 16 * K1_WP);
                }
                xs += 128; bs += 64 * 3840; as += 64 * 3840;
            }
            so = so == (unsigned)(size_t)lds + 2 * K1_STAGE ? (unsigned)(size_t)lds : so + K1_STAGE;
        }
        WAIT_V(0);
        RAW_BAR();
    }
    const char* tp = (const char*)x;
    unsigned d0 = 0;
    const int tstep = wid < 5 ? 2 * wid - LOOK : 100;
    if (wid < 5 && nt < 15) {
        const int l = lane & 31, ks = 2 * wid + (lane >> 5);
        if (l < 16) tp = (const char*)((l & 2 ? wA : wB) + nt * 128 + (l & 1) * 64) + (size_t)(ks * 64 + mt * 4 + (l >> 2)) * 3840;
        else tp = (const char*)(x + (size_t)(m0 + min(nt * 9 + (l < 25 ? l - 16 : 0), 127)) * DM) + ks * 128;
        if (tstep < 0 && wid >= W0) asm volatile("global_load_dword %0, %1, off" : "+v"(d0) : "v"(tp) : "memory");
    }
    const int head = (blockIdx.x & 7) * 4 + ((blockIdx.x >> 3) & 3), qb = blockIdx.x >> 5;
    const int omt = head, ont = qb;
    const int r32 = lane & 31, hh = lane >> 5;
    const u32x4 vfill = r32 == 20 ? u32x4{ONE2, ONE2, ONE2, ONE2} : u32x4{0u, 0u, 0u, 0u};
    const int voff = hh * 320 + (r32 < 20 ? r32 : 0) * 16;
    const unsigned olane = (r32 >> 4) * (int)O_BLK + (r32 & 15) * 16;
    const int ps = nt < 5 ? (int)Q_TILE : (int)KV_TILE;
    const int wimg = (int)WS_WO + ont * 5120;
    gu64* const f1 = (gu64*)(ws + WS_F1);
    constexpr int P_OFF = 102400;
    const int pslot = g * 256;
    asm volatile("" : "+s"(out));
    unsigned fq, fo;
    auto fin_addr = [&]() {
        const int row = tid / 10, c = tid - row * 10;
        fq = P_OFF + ((row >> 4) * 5 + (c >> 1)) * 1024 + (c & 1) * 512 + (((row & 15) + c) & 15) * 16;
        fo = ((omt * 64 + row) * DM + ont * 80 + c * 8) * 2;
        asm volatile("" : "+v"(fq), "+v"(fo));
    };
    if (nt == 15) {
        const int task = mt * 10 + wid, ks = task >> 2, cg = task & 3;
        lds_ptr st = lds + wid * 10240;
        if (wid < 10) {
            const char* wsrc = (const char*)((ks >= 20 ? woB : woA) + cg * 160);
#pragma unroll
            for (int i = 0; i < 10; ++i) {
                const int ci = i * 64 + lane, slot = ci / 20, cc = ci - slot * 20;
                const int c = (ks >= 20 ? ks - 20 : ks) * 4 + (slot >> 3), e = slot & 7, p = c / 5, r = c - p * 5;
                const int k = r < 4 ? (2 * p + (r >> 1)) * 20 + 4 * (r & 1) + (e & 3) + 8 * (e >> 2) : (2 * p + (e >> 2)) * 20 + 16 + (e & 3);
                glds16(wsrc, k * (DM * 2) + cc * 16, st + i * 1024);
            }
        }
        u32x4 gv[2];
        gv[0] = *(const u32x4*)(gate_w + lane * 8);
        gv[1] = lane < 16 ? *(const u32x4*)(gate_w + 512 + lane * 8) : u32x4{0u, 0u, 0u, 0u};
        float s[11];
#pragma unroll
        for (int u = 0; u < 11; ++u) {
            const int row = wid + 12 * u < 128 ? wid + 12 * u : wid;
            const u16* xr = x + (size_t)(m0 + row) * DM;
            const u32x4 xv[2] = {*(const u32x4*)(xr + lane * 8), *(const u32x4*)(xr + 512 + r16 * 8)};
            float a = 0.f;
#pragma unroll
            for (int c = 0; c < 2; ++c)
#pragma unroll
                for (int e = 0; e < 4; ++e) {
                    a = fmaf(__uint_as_float(xv[c][e] << 16), __uint_as_float(gv[c][e] << 16), a);
                    a = fmaf(__uint_as_float(xv[c][e] & 0xffff0000u), __uint_as_float(gv[c][e] & 0xffff0000u), a);
                }
            s[u] = a;
        }
#pragma unroll
        for (int o = 32; o; o >>= 1)
#pragma unroll
            for (int u = 0; u < 11; ++u) s[u] += __shfl_xor(s[u], o);
        float z = s[0];
#pragma unroll
        for (int u = 1; u < 11; ++u) z = lane == u ? s[u] : z;
        if (lane < 11 && wid + 12 * lane < 128) ST4(__float_as_uint(1.f / (1.f + __expf(-z))), (wid + 12 * lane) * 4, (int)WS_G + m0 * 4);
        WAIT_V(0);
        if (wid < 10) {
            lds_ptr rp = st + (8 * g + (r16 >> 2)) * 320 + (lane & 3) * 8;
#pragma unroll
            for (int j = 0; j < 10; ++j)
                ST16(__builtin_bit_cast(u32x4, cat8(lds_tr(rp + j * 32), lds_tr(rp + j * 32 + 4 * 320))), lane * 16, (int)WS_WO + (ks * 40 + cg * 10 + j) * 1024);
        }
    }
    if (wid >= 8) {
        u64 tag = __hip_atomic_load((gu64*)(ws + WS_EPOCH), RLX_AGENT) + 1;
        const int hw = wid - 8;
        __builtin_amdgcn_s_setprio(3);
        RAW_BAR();
        f32x16 c0, c1;
        {
            const unsigned long long msk = ~(0x15554ull << r32) >> (4 * hh);
            const unsigned mlo = (unsigned)msk, mhi = (unsigned)(msk >> 32);
#pragma unroll
            for (int r = 0; r < 16; ++r) {
                const int u = (r & 3) + 8 * (r >> 2);
                c0[r] = r < 8 ? 0.f : __uint_as_float(__builtin_amdgcn_sbfe(mlo, (u - 16) & 31, 1) & 0xff800000u);
                c1[r] = __uint_as_float(__builtin_amdgcn_sbfe(u < 16 ? mlo : mhi, (u + 16) & 31, 1) & 0xff800000u);
            }
        }
        asm volatile("" : "+v"(c0), "+v"(c1));
        const int fc = head * 20 + (g & 1) * 19;
        gu64* const fkv = f1 + (((g < 2 ? 640 : 1280) + fc) >> 7) * 16 * FS;
        auto go = [&](int n, gu64* w0) {
            volatile LDS_AS int* gw = (volatile LDS_AS int*)(lds + L_CNT + 4);
            if (hw == 3) {
                gu64* const w[1] = {w0};
                wait_flags<2>(w, tag);
                *gw = n;
            } else {
#pragma unroll 1
                while (__builtin_amdgcn_readfirstlane(*gw) < n) __builtin_amdgcn_s_sleep(1);
                asm volatile("" ::: "memory");
            }
        };
        const int qp = (hw < 3 ? hw * 3 : 7) * 1024;
        const char* q0s = ws + WS_Q + (size_t)((NH + head) * 64 + qb * 8) * Q_TILE + qp;
        const char* k0s = ws + WS_KV + (size_t)(NH + head) * KV_HEAD + hw * 7168;
        const char* k1s = k0s + 4096;
        unsigned q0l = (unsigned)(size_t)lds + L_Q + qp, k0l = (unsigned)(size_t)lds + hw * 7168, k1l = k0l + 4096;
        asm volatile("" : "+s"(q0s), "+s"(k0s), "+s"(k1s), "+s"(q0l), "+s"(k0l), "+s"(k1l));
        go(1, r16 < 3 ? fkv + r16 * FS : f1 + ((fc >> 7) * 16 + qb * 2 + (lane & 1)) * FS);
        glds_first(lane * 16, q0s, q0l, k0s, k0l, k1s, k1l);
        const char* src = ws + WS_KV + (size_t)(NH + head) * KV_HEAD + hw * 1024;
#define LOADS(a, b) _Pragma("unroll 1") for (int i = a; i < b; ++i) glds16_dev(src + i * 4096, lane * 16, lds + hw * 1024 + i * 4096)
        WAIT_V(0); RAW_BAR();
        go(2, fkv + r16 * FS);
        LOADS(7, 28);
        WAIT_V(16); RAW_BAR();
        *(LDS_AS int*)lds = 0;
        if (nt < 15) {
            lds_ptr sp = lds + L_STASH + hw * 9216 + lane * 16;
            const u32x4 po = *(const LDS_AS u32x4*)(sp + 8192);
#pragma unroll
            for (int j = 0; j < 4; ++j) {
                const u32x4 pv[2] = {*(const LDS_AS u32x4*)(sp + j * 2048), *(const LDS_AS u32x4*)(sp + j * 2048 + 1024)};
                put_qkv(pv, po[j] & ~7u, (po[j] & ~7u) + (po[j] & 7u) * 128, ps, wrs);
            }
        }
        WAIT_V(0);
        RAW_BAR();
        if (hw == 0 && lane == 0) __hip_atomic_store(f1 + (256 + nt * 16 + mt) * FS, tag, RLX_AGENT);
        LOADS(28, 40);
#pragma unroll
        for (int i = 0; i < 2; ++i)
            put_o(u32x4{~0u, ~0u, ~0u, ~0u}, u32x2{~0u, ~0u}, wrs, olane, (int)WS_O + (128 + (qb * 8 + hw * 2 + i) * 2) * (int)O_BLK, head, hh);
        {
            const int sec = r16 >> 2, fm = max(qb * 2 - 1 + (r16 & 3), 0);
            gu64* const w[1] = {f1 + (256 + (g < 2 && sec < 3 && (r16 & 3) < 3 ? ((sec * 640 + fc) >> 7) * 16 + fm : 240 + r16)) * FS};
            wait_flags<8>(w, tag);
        }
        {
            const int q0 = qb * 8 + hw * 2;
            const int qA = (int)WS_Q + (head * 64 + q0) * (int)Q_TILE, k1 = (int)WS_KV + (head * 64 + q0) * (int)KV_TILE;
            u32x4 aqa[2], ka[3], vf[5];
            u32x2 aqb[2] = {{0u, 0u}, {0u, 0u}}, kb[3];
            float gv[2];
#pragma unroll
            for (int i = 0; i < 3; ++i) {
                const int kt = k1 + (i == 0 ? (q0 ? -(int)KV_TILE : 0) : (i - 1) * (int)KV_TILE);
                ka[i] = LD16(lane * 16, kt);
                kb[i] = LD8(r32 * 8, kt + 1024);
            }
#pragma unroll
            for (int i = 0; i < 2; ++i) aqa[i] = LD16(lane * 16, qA + i * (int)Q_TILE);
            if (!hh) {
#pragma unroll
                for (int i = 0; i < 2; ++i) aqb[i] = LD8(r32 * 8, qA + i * (int)Q_TILE + 1024);
            }
#pragma unroll
            for (int i = 0; i < 5; ++i) vf[i] = vfill;
            if (r32 < 20) {
                vf[0] = LD16(voff, k1 + (q0 ? -(int)KV_TILE : 0) + 1920);
#pragma unroll
                for (int i = 1; i < 5; ++i) vf[i] = LD16(voff, k1 + ((i - 1) >> 1) * (int)KV_TILE + 1280 + ((i - 1) & 1) * 640);
            }
#pragma unroll
            for (int i = 0; i < 2; ++i) gv[i] = __uint_as_float(LD4(r32 * 4, (int)WS_G + (q0 + i) * 128));
#pragma unroll
            for (int i = 1; i >= 0; --i) {
                if (i == 0 && q0 == 0) {
#pragma unroll
                    for (int r = 8; r < 16; ++r) c0[r] = -INFINITY;
                }
                f32x16 s0 = MFMA32(BF8(ka[i]), BF8(aqa[i]), c0);
                f32x16 s1 = MFMA32(BF8(ka[i + 1]), BF8(aqa[i]), c1);
                s0 = MFMA32H(kb[i], aqb[i], s0);
                s1 = MFMA32H(kb[i + 1], aqb[i], s1);
                float m = max3f(s0[8], s0[9], s0[10]);
                m = max3f(m, s0[11], s0[12]); m = max3f(m, s0[13], s0[14]);
                m = xhalf_max(max16(s1, fmaxf(m, s0[15])));
                const u32x2 ref = with_ref(u32x2{0u, 0u}, m, hh);
                s0 = MFMA32H((u32x2{ONE2, ONE2}), ref, s0);
                s1 = MFMA32H((u32x2{ONE2, ONE2}), ref, s1);
#pragma unroll
                for (int r = 8; r < 16; ++r) s0[r] = __builtin_amdgcn_exp2f(s0[r]);
#pragma unroll
                for (int r = 0; r < 16; ++r) s1[r] = __builtin_amdgcn_exp2f(s1[r]);
                f32x16 o = SMFMAC(BF8(vf[2 * i]), HI8(s0), f32x16{});
                o = SMFMAC(BF8(vf[2 * i + 1]), LO8(s1), o);
                o = SMFMAC(BF8(vf[2 * i + 2]), HI8(s1), o);
                const float f = gv[i] * __builtin_amdgcn_rcpf(row_sum(o));
                put_o(u32x4{pack2(o[0] * f, o[1] * f), pack2(o[2] * f, o[3] * f), pack2(o[4] * f, o[5] * f), pack2(o[6] * f, o[7] * f)},
                      u32x2{pack2(o[8] * f, o[9] * f), pack2(o[10] * f, o[11] * f)}, wrs, olane, (int)WS_O + (q0 + i) * 2 * (int)O_BLK, head, hh);
            }
        }
        arrive(lds, (gu64*)(ws + WS_FA) + (qb * 32 + head) * FS, tag);
        f32x4 acc[2][5];
        bf16x8 of[3][2], wf[3][5];
        const int osrc = (int)WS_O + (omt * 4 + (hw & 1) * 2) * (int)O_BLK + (hw >> 1) * 10240, wsrc = wimg + (hw >> 1) * 409600;
        auto ldo = [&](int ks) {
#pragma unroll
            for (int i = 0; i < 2; ++i) of[ks % 3][i] = BF8(LD16(lane * 16, osrc + i * (int)O_BLK + ks * 1024));
        };
        auto ldw = [&](int ks) {
#pragma unroll
            for (int j = 0; j < 5; ++j) wf[ks % 3][j] = BF8(LD16(lane * 16, wsrc + ks * 40960 + j * 1024));
        };
        ldw(0); ldw(1); ldw(2);
        SB();
        RAW_BAR();
        {
            gu64* const w[1] = {(gu64*)(ws + WS_FA) + ((omt >> 2) * 32 + r32) * FS};
            wait_flags<8>(w, tag);
        }
        ldo(0); ldo(1); ldo(2);
        SB();
#pragma unroll
        for (int ks = 0; ks < 10; ++ks) {
#pragma unroll
            for (int j = 0; j < 5; ++j) {
#pragma unroll
                for (int i = 0; i < 2; ++i) acc[i][j] = MFMA16(wf[ks % 3][j], of[ks % 3][i], ks ? acc[i][j] : (f32x4{0.f, 0.f, 0.f, 0.f}));
                SB();
                __builtin_amdgcn_s_sleep(1);
                SB();
            }
            if (ks < 7) { ldo(ks + 3); ldw(ks + 3); SB(); }
        }
#pragma unroll 1
        for (int ks = hw * 5; ks < hw * 5 + 5; ++ks)
#pragma unroll
            for (int j = 0; j < 5; ++j) glds16_dev(ws + wimg + (20 + ks) * 40960 + j * 1024, lane * 16, lds + ks * 5120 + j * 1024);
        RAW_BAR();
        if (blockIdx.x == 0 && hw == 0) {
            gu64* const w[4] = {f1 + (256 + lane) * FS, f1 + (320 + lane) * FS, f1 + (384 + lane) * FS, f1 + (448 + lane) * FS};
            wait_flags<8>(w, tag);
            if (lane == 0) __hip_atomic_store((gu64*)(ws + WS_EPOCH), tag, RLX_AGENT);
        }
        WAIT_V(0);
        auto put = [&]() {
#pragma unroll
            for (int i = 0; i < 2; ++i)
#pragma unroll
                for (int j = 0; j < 5; ++j)
                    *(LDS_AS f32x4*)(lds + P_OFF + (1 + (hw >> 1)) * 20480 + (((hw & 1) * 2 + i) * 5 + j) * 1024 + pslot + ((r16 + 2 * j + (g >> 1)) & 15) * 16) = acc[i][j];
        };
        fin_addr();
        RAW_BAR();
        if (hw >= 2) put();
        RAW_BAR();
        if (hw < 2) put();
    } else {
        if (nt < 15) {
            const int br = wid < 4, wm = (wid >> 1) & 1, wn = wid & 1;
            const int xa = (wm * 64 + r16) * 128 + ((g ^ (lane & 7)) << 4);
            const int wa = 16384 + ((wid >> 2) * 16 + 4 * g + (r16 >> 2)) * K1_WP + (wn * 64 + 4 * (lane & 3)) * 2;
            const int tile0 = mt * 4 + wm * 2, cw = nt * 128 + wn * 64;
            unsigned poff[4], poff1[4];
#pragma unroll
            for (int j = 0; j < 4; ++j) {
                if (nt < 10) {
                    const int cp = (cw - (nt < 5 ? 0 : 640)) / 8 + j * 2 + (g >> 1), p = cp / 5, r = cp - p * 5;
                    const unsigned hoff = (int)(nt < 5 ? WS_Q : WS_KV) + ((br * NH + 2 * p) * 64 + tile0) * ps;
                    poff[j] = r == 2 ? hoff + (g & 1) * 64 * ps + 1024 + r16 * 8 : hoff + (r > 2 ? 64 * ps + (r - 3) * 512 : r * 512) + r16 * 16 + (g & 1) * 8;
                    poff1[j] = poff[j] + (r == 2 ? 128 : 256);
                } else {
                    const int c = cw - 1280 + j * 16 + r16, h = c / 20, d = c - h * 20;
                    poff[j] = (int)WS_KV + ((br * NH + h) * 64 + tile0) * (int)KV_TILE + 1280 + (g >> 1) * 320 + d * 16 + (g & 1) * 8;
                    poff1[j] = poff[j] + 640;
                }
            }
            asm volatile("" : "+v"(poff[0]), "+v"(poff[1]), "+v"(poff[2]), "+v"(poff[3]), "+v"(poff1[0]), "+v"(poff1[1]), "+v"(poff1[2]), "+v"(poff1[3]));
            u64 tag = 0;
            f32x4 acc[4][4];
#pragma unroll
            for (int i = 0; i < 4; ++i)
#pragma unroll
                for (int j = 0; j < 4; ++j) acc[i][j] = f32x4{0.f, 0.f, 0.f, 0.f};
            if (br) __builtin_amdgcn_s_setprio(1);
            const bool urgent = br && (nt < 5 || mt < 3);
            gu64* bflag = f1 + (nt * 16 + mt) * FS;
            asm volatile("" : "+s"(bflag));
            auto mid = [&]() {
                if (br) tag = __hip_atomic_load((gu64*)(ws + WS_EPOCH), RLX_AGENT);
                if (!urgent) RAW_BAR();
                if (br && !urgent) __builtin_amdgcn_s_sleep(16);
            };
            auto fin = [&](int j) {
                if (nt < 5) {
#pragma unroll
                    for (int i = 0; i < 4; ++i) acc[i][j] *= QSCALE;
                }
                u32x4 pv[2];
#pragma unroll
                for (int i = 0; i < 2; ++i) {
                    const f32x4 a = acc[2 * i][j], b = acc[2 * i + 1][j];
                    pv[i] = u32x4{pack2(a[0], a[1]), pack2(a[2], a[3]), pack2(b[0], b[1]), pack2(b[2], b[3])};
                }
                if (br) {
                    put_qkv(pv, poff[j], poff1[j], ps, wrs);
                } else {
#pragma unroll
                    for (int i = 0; i < 2; ++i) *(LDS_AS u32x4*)(lds + L_STASH + (wid - 4) * 9216 + j * 2048 + i * 1024 + lane * 16) = pv[i];
                }
            };
            if (nt < 10) k1_main<false>(acc, lds, xa, wa, tp, tstep, d0, mid, fin);
            else k1_main<true>(acc, lds, xa, wa, tp, tstep, d0, mid, fin);
            if (urgent) RAW_BAR();
            if (br) {
                arrive(lds + L_CNT, bflag, tag + 1);
            } else {
                *(LDS_AS u32x4*)(lds + L_STASH + (wid - 4) * 9216 + 8192 + lane * 16) =
                    u32x4{poff[0] + ((poff1[0] - poff[0]) >> 7), poff[1] + ((poff1[1] - poff[1]) >> 7), poff[2] + ((poff1[2] - poff[2]) >> 7), poff[3] + ((poff1[3] - poff[3]) >> 7)};
            }
        } else {
            RAW_BAR();
            WAIT_V(0);
        }
        const int qt = qb * 8 + wid;
        const unsigned long long vmask = 0x000fffff000fffffull;
        f32x16 o, o2;
        float gate;
        u32x4 qa0, vlo = vfill, vhi = vfill;
        u32x2 qb0;
        const unsigned l0 = (unsigned)(size_t)lds, kap = (wid >> 2) * (int)KV_TILE;
        unsigned a_ka = l0 + kap + lane * 16, a_kb = l0 + kap + r32 * 8, a_v = l0 + kap + voff;
        const unsigned qs0 = L_Q + wid * (int)Q_TILE - kap, qs1 = L_Q + (wid ^ 4) * (int)Q_TILE - kap;
        int nbar = 0x53, it = 7;
        __builtin_amdgcn_s_setprio(1);
        asm volatile(
            "v_mov_b32 v28, %[one]\n\tv_mov_b32 v29, %[one]\n\tv_mov_b32 v30, %[one]\n\tv_mov_b32 v31, %[one]\n\tv_mov_b32 v40, %[sidx]\n\t"
            ZERO4(72) ZERO4(76) ZERO4(80) ZERO4(84) ZERO4(88) ZERO4(92) ZERO4(96) ZERO4(100) ZERO4(104) ZERO4(108)
            "v_mov_b64 v[12:13], 0\n\tv_mov_b64 v[20:21], 0\n\t"
            "v_add_u32 v48, %[qs0], v41\n\tv_add_u32 v49, %[qs1], v41\n\tv_subrev_u32 v50, %[kap], v41\n\t"
            "v_add_u32 v51, %[qs0], v42\n\tv_add_u32 v52, %[qs1], v42\n\tv_subrev_u32 v53, %[kap], v42\n\t"
            "s_waitcnt lgkmcnt(0)\n\ts_barrier\n\t"
            "ds_read_b128 v[112:115], v50\n\tds_read_b128 v[8:11], v48\n\tds_read_b128 v[16:19], v49\n\t"
            "s_mov_b64 exec, %[km]\n\t"
            "ds_read_b64 v[30:31], v53 offset:1024\n\tds_read_b64 v[12:13], v51 offset:1024\n\tds_read_b64 v[20:21], v52 offset:1024\n\t"
            "ds_read_b64 v[28:29], v42 offset:1024\n\t"
            "s_mov_b64 exec, -1\n\t"
            "ds_read_b128 v[24:27], v41\n\t"
            "s_waitcnt lgkmcnt(5)\n\t"
            "v_mfma_f32_32x32x16_bf16 v[116:131], v[112:115], v[8:11], 0\n\t"
            "v_mfma_f32_32x32x16_bf16 v[132:147], v[112:115], v[16:19], 0\n\t"
            "s_waitcnt lgkmcnt(0)\n\t"
            QK1(116, 8, 30) QK1(132, 16, 30) QK0(48, 8)
            "s_nop 10\n\t"
            "v_max_f32 v14, v116, v117\n\tv_max_f32 v22, v132, v133\n\t"
            MAX2(2) MAX2(4) MAX2(6) MAX2(8) MAX2(10) MAX2(12) MAX2(14)
            "v_mov_b32 v15, v14\n\tv_mov_b32 v23, v22\n\ts_nop 1\n\t"
            "v_permlane32_swap_b32 v14, v15\n\tv_permlane32_swap_b32 v22, v23\n\t"
            "s_not_b64 exec, %[km]\n\t"
            "v_min_f32 v14, -v14, -v15\n\tv_min_f32 v22, -v22, -v23\n\t"
            "v_and_b32 v12, 0xffff0000, v14\n\tv_and_b32 v20, 0xffff0000, v22\n\t"
            "s_mov_b64 exec, -1\n\ts_nop 1\n\t"
            QK1(48, 8, 28)
            "s_nop 5\n\t"
            "1:\n\t"
            PRIO(2, 3) PAIR(0, 28, 30, "") PAIR(1, 30, 28, "") PAIR(2, 28, 30, PRIO(0, 4)) PAIR(3, 30, 28, "")
            "v_add_u32 v41, 20480, v41\n\tv_add_u32 v42, 20480, v42\n\tv_add_u32 v43, 20480, v43\n\t"
            "s_bitcmp0_b32 %[nbar], 0\n\t"
            "s_cbranch_scc1 2f\n\t"
            "s_barrier\n\t"
            "2:\n\t"
            "s_lshr_b32 %[nbar], %[nbar], 1\n\t"
            "s_sub_i32 %[it], %[it], 1\n\t"
            "s_cmp_lg_u32 %[it], 0\n\t"
            "s_cbranch_scc1 1b\n\t"
            "global_load_dword v44, %[goff], %[gb] sc1\n\t"
            PRIO(2, 3) PAIR(0, 28, 30, "") PAIR(1, 30, 28, "") PAIR(2, 28, 30, PRIO(0, 4)) PAIR_LAST(3, 30)
            "s_waitcnt vmcnt(0)"
            : "=&{v44}"(gate), "=&{v[80:95]}"(o), "=&{v[96:111]}"(o2), "=&{v[8:11]}"(qa0), "=&{v[12:13]}"(qb0), "+{v[32:35]}"(vlo),
              "+{v[36:39]}"(vhi), "+{v41}"(a_ka), "+{v42}"(a_kb), "+{v43}"(a_v), [nbar] "+s"(nbar), [it] "+s"(it)
            : [vm] "s"(vmask), [km] "s"(0xffffffffull), [one] "s"(ONE2), [sidx] "s"(0xDDDDDDDDu), [psw] "s"(wid), [goff] "v"(r32 * 4),
              [gb] "s"(ws + WS_G + qt * 128), [qs0] "s"(qs0), [qs1] "s"(qs1), [kap] "s"(kap)
            : "v14", "v15", "v16", "v17", "v18", "v19", "v20", "v21", "v22", "v23", "v24", "v25", "v26", "v27", "v28", "v29", "v30",
              "v31", "v40", "v48", "v49", "v50", "v51", "v52", "v53", "v54", "v55", "v56", "v57", "v58", "v59", "v60", "v61", "v62",
              "v63", "v64", "v65", "v66", "v67", "v68", "v69", "v70", "v71", "v72", "v73", "v74", "v75", "v76", "v77", "v78", "v79",
              "v112", "v113", "v114", "v115", "v116", "v117", "v118", "v119", "v120", "v121", "v122", "v123", "v124", "v125", "v126",
              "v127", "v128", "v129", "v130", "v131", "v132", "v133", "v134", "v135", "v136", "v137", "v138", "v139", "v140", "v141",
              "v142", "v143", "v144", "v145", "v146", "v147", "scc", "memory");
        __builtin_amdgcn_s_setprio(3);
#pragma unroll
        for (int i = 0; i < 3; ++i)
            *(LDS_AS f32x4*)(lds + P_OFF + (wid ^ 4) * 3072 + i * 1024 + lane * 16) = f32x4{o2[4 * i], o2[4 * i + 1], o2[4 * i + 2], o2[4 * i + 3]};
        RAW_BAR();
#pragma unroll
        for (int i = 0; i < 3; ++i) {
            const f32x4 t = *(const LDS_AS f32x4*)(lds + P_OFF + wid * 3072 + i * 1024 + lane * 16);
#pragma unroll
            for (int e = 0; e < 4; ++e) o[4 * i + e] += t[e];
        }
        const float gB = 1.f - gate;
        float l = row_sum(o);
        float asum = 0.f;
#pragma unroll
        for (int i = 0; i < 12; ++i) asum += fabsf(o[i]);
        if (__builtin_expect(__ballot(l >= 0x1p-64f && l <= 0x1p64f && asum <= 0x1p126f) == ~0ull, 1)) {
            store_o(o, gB * __builtin_amdgcn_rcpf(l), wrs, olane, (int)WS_O + (128 + qt * 2) * (int)O_BLK, head, hh);
        } else {
            const int kv = (int)WS_KV + (NH + head) * (int)KV_HEAD;
            u32x2 qr = hh ? u32x2{0u, 0u} : qb0;
            float mt2 = -INFINITY;
            o = f32x16{};
#pragma unroll 1
            for (int t = 0; t < 128; ++t) {
                const int kt = kv + (t & 63) * (int)KV_TILE;
                u32x2 kb = {ONE2, ONE2};
                if (!hh) kb = LD8(r32 * 8, kt + 1024);
                f32x16 sc = MFMA32(BF8(LD16(lane * 16, kt)), BF8(qa0), f32x16{});
                sc = MFMA32H(kb, qr, sc);
                if (t < 64) {
                    mt2 = max16(sc, mt2);
                    if (t == 63) qr = with_ref(qb0, xhalf_max(mt2), hh);
                } else {
                    u32x4 v0 = vfill, v1 = vfill;
                    if (r32 < 20) { v0 = LD16(voff, kt + 1280); v1 = LD16(voff, kt + 1920); }
#pragma unroll
                    for (int r = 0; r < 16; ++r) sc[r] = __builtin_amdgcn_exp2f(sc[r]);
                    o = SMFMAC(BF8(v0), LO8(sc), o);
                    o = SMFMAC(BF8(v1), HI8(sc), o);
                }
            }
            l = row_sum(o);
            asm volatile("" : "+v"(l));
            store_o(o, gB * __builtin_amdgcn_rcpf(l), wrs, olane, (int)WS_O + (128 + qt * 2) * (int)O_BLK, head, hh);
        }
        RAW_BAR();
        const int mb = wid & 3, kh = wid >> 2;
        const int osrc = (int)WS_O + (128 + omt * 4 + mb) * (int)O_BLK + kh * 10240;
        u32x4 of[10];
#pragma unroll
        for (int s = 0; s < 10; ++s) of[s] = LD16(lane * 16, osrc + s * 1024);
        SB();
        f32x4 acc[5];
        lds_ptr pp = lds + P_OFF + kh * 40960 + mb * 5120 + pslot;
#pragma unroll
        for (int j = 0; j < 5; ++j) acc[j] = f32x4{0.f, 0.f, 0.f, 0.f};
        if (kh) {
#pragma unroll
            for (int j = 0; j < 5; ++j) acc[j] = *(const LDS_AS f32x4*)(pp + j * 1024 + ((r16 + 2 * j + (g >> 1)) & 15) * 16);
        }
        lds_ptr wp = lds + kh * 51200 + lane * 16;
        bf16x8 wf[2][10];
#pragma unroll
        for (int j = 0; j < 10; ++j) wf[0][j] = lds_b128(wp + j * 1024);
        auto bad = [&](int s) {
            const u32x4 a = __builtin_elementwise_max(of[s], of[s + 1]);
            return __any(max(max(a[0], a[1]), max(a[2], a[3])) == ~0u);
        };
#pragma unroll
        for (int s = 0; s < 10; s += 2) {
            if (s < 8) {
#pragma unroll
                for (int j = 0; j < 10; ++j) wf[(s / 2 + 1) & 1][j] = lds_b128(wp + (s + 2) * 5120 + j * 1024);
            }
            if (bad(s)) {
#pragma unroll 1
                for (unsigned spins = 0; spins < (1u << 18); ++spins) {
                    asm volatile("" ::: "memory");
#pragma unroll
                    for (int t = 8; t >= s; t -= 2)
                        if (t == s || bad(t)) { of[t] = LD16(lane * 16, osrc + t * 1024); of[t + 1] = LD16(lane * 16, osrc + (t + 1) * 1024); }
                    if (!bad(s)) break;
                }
            }
            if (s == 2) fin_addr();
#pragma unroll
            for (int j = 0; j < 10; ++j) acc[j % 5] = MFMA16(wf[(s / 2) & 1][j], BF8(of[s + j / 5]), acc[j % 5]);
        }
#pragma unroll
        for (int j = 0; j < 5; ++j) *(LDS_AS f32x4*)(pp + j * 1024 + ((r16 + 2 * j + (g >> 1)) & 15) * 16) = acc[j];
    }
    RAW_BAR();
    if (tid < 640) {
        f32x4 t[6];
#pragma unroll
        for (int p = 0; p < 6; ++p) t[p] = *(const LDS_AS f32x4*)(lds + fq + (p >> 1) * 20480 + (p & 1) * 256);
        SB();
        const f32x4 a = t[0] + t[2] + t[4], b = t[1] + t[3] + t[5];
        const u32x4 v = {pack2(a[0], a[1]), pack2(a[2], a[3]), pack2(b[0], b[1]), pack2(b[2], b[3])};
        __builtin_amdgcn_raw_buffer_store_b128(v, __builtin_amdgcn_make_buffer_rsrc(out, 0, SEQ * DM * 2, 0x00020000), fo, 0, SC1);
    }
}

extern "C" void kernel_launch(void* const* d_in, const int* in_sizes, int n_in, void* d_out, int out_size, void* d_ws, size_t ws_size,
                              hipStream_t stream) {
    gated_mix_kernel<<<256, 768, 0, stream>>>((const u16*)d_in[0], (const u16*)d_in[1], (const u16*)d_in[4], (const u16*)d_in[2],
                                              (const u16*)d_in[5], (const u16*)d_in[6], (char*)d_ws, (u16*)d_out);
}
```

```cpp
#include <hip/hip_runtime.h>
#include <hip/hip_bf16.h>

typedef __attribute__((ext_vector_type(8))) __bf16 bf16x8;
typedef __attribute__((ext_vector_type(16))) __bf16 bf16x16;
typedef __attribute__((ext_vector_type(2))) __bf16 bf16x2;
typedef __attribute__((ext_vector_type(4))) short s16x4;
typedef __attribute__((ext_vector_type(2))) float f32x2;
typedef __attribute__((ext_vector_type(4))) float f32x4;
typedef __attribute__((ext_vector_type(16))) float f32x16;
typedef __attribute__((ext_vector_type(2))) unsigned u32x2;
typedef __attribute__((ext_vector_type(4))) unsigned u32x4;
typedef unsigned short u16;
#define LDS_AS __attribute__((address_space(3)))
typedef LDS_AS char* lds_ptr;

constexpr int SEQ = 2048, DM = 640, NH = 32;
constexpr size_t KV_TILE = 2560, KV_HEAD = 64 * KV_TILE;
constexpr size_t Q_TILE = 1280, Q_HEAD = 64 * Q_TILE;
constexpr size_t WS_KV = 0;
constexpr size_t WS_Q = WS_KV + 2 * NH * KV_HEAD;
constexpr size_t O_BLK = 80 * 256;
constexpr size_t WS_O = WS_Q + 2 * NH * Q_HEAD;
constexpr size_t WS_WO = WS_O + 2 * 128 * O_BLK;
constexpr size_t WS_G = WS_WO + 40 * 40 * 1024;
constexpr size_t WS_F1 = WS_G + SEQ * 4;
constexpr int FS = 16;
constexpr size_t WS_FA = WS_F1 + 512 * 128;
constexpr size_t WS_EPOCH = WS_FA + 256 * 128;
constexpr size_t WS_END = WS_EPOCH + 128;
constexpr float QSCALE = 0.22360679774997896f * 1.4426950408889634f;

#define WAIT_V(n) asm volatile("s_waitcnt vmcnt(" #n ")" ::: "memory")
#define RAW_BAR() asm volatile("s_waitcnt lgkmcnt(0)\n\ts_barrier" ::: "memory")
#define MFMA16(a, b, c) __builtin_amdgcn_mfma_f32_16x16x32_bf16(a, b, c, 0, 0, 0)
#define MFMA32(a, b, c) __builtin_amdgcn_mfma_f32_32x32x16_bf16(a, b, c, 0, 0, 0)
#define MFMA32H(a, b, c) __builtin_amdgcn_mfma_f32_32x32x8bf16_1k(__builtin_bit_cast(s16x4, a), __builtin_bit_cast(s16x4, b), c, 0, 0, 0)

__device__ __forceinline__ unsigned pack2(float lo, float hi) {
    f32x2 v = {lo, hi};
    return __builtin_bit_cast(unsigned, __builtin_convertvector(v, bf16x2));
}
__device__ __forceinline__ void glds16(const char* sbase, unsigned voff, lds_ptr l) {
    unsigned keep;
    asm volatile("s_mov_b32 %0, m0\n\ts_mov_b32 m0, %3\n\ts_nop 0\n\tglobal_load_lds_dwordx4 %1, %2\n\ts_mov_b32 m0, %0"
                 : "=&s"(keep) : "v"(voff), "s"(sbase), "s"((unsigned)(size_t)l) : "memory");
}
__device__ __forceinline__ void glds16x3(const char* s0, unsigned v0, unsigned l0, const char* s1, const char* s2, unsigned v1, unsigned l1, unsigned l2) {
    unsigned keep;
    asm volatile("s_mov_b32 %0, m0\n\ts_mov_b32 m0, %3\n\ts_nop 0\n\tglobal_load_lds_dwordx4 %1, %2\n\t"
                 "s_mov_b32 m0, %7\n\ts_nop 0\n\tglobal_load_lds_dwordx4 %4, %5\n\t"
                 "s_mov_b32 m0, %8\n\ts_nop 0\n\tglobal_load_lds_dwordx4 %4, %6\n\ts_mov_b32 m0, %0"
                 : "=&s"(keep) : "v"(v0), "s"(s0), "s"(l0), "v"(v1), "s"(s1), "s"(s2), "s"(l1), "s"(l2) : "memory");
}
__device__ __forceinline__ bf16x8 lds_b128(lds_ptr p) { return *(const LDS_AS bf16x8*)p; }
__device__ __forceinline__ s16x4 lds_tr(lds_ptr p) { return __builtin_amdgcn_ds_read_tr16_b64_v4i16((LDS_AS s16x4*)p); }
__device__ __forceinline__ bf16x8 cat8(s16x4 a, s16x4 b) {
    return __builtin_bit_cast(bf16x8, __builtin_shufflevector(a, b, 0, 1, 2, 3, 4, 5, 6, 7));
}
typedef __amdgpu_buffer_rsrc_t rsrc_t;
typedef unsigned long long u64;
typedef __attribute__((address_space(1))) u64 gu64;
#define RLX_AGENT __ATOMIC_RELAXED, __HIP_MEMORY_SCOPE_AGENT
constexpr int SC1 = 16;
#define LD16(voff, soff) __builtin_amdgcn_raw_buffer_load_b128(wrs, voff, soff, SC1)
#define LD8(voff, soff) __builtin_amdgcn_raw_buffer_load_b64(wrs, voff, soff, SC1)
#define LD4(voff, soff) __builtin_amdgcn_raw_buffer_load_b32(wrs, voff, soff, SC1)
#define ST16(v, voff, soff) __builtin_amdgcn_raw_buffer_store_b128(v, wrs, (voff) + (soff), 0, SC1)
#define ST8(v, voff, soff) __builtin_amdgcn_raw_buffer_store_b64(v, wrs, voff, soff, SC1)
#define ST4(v, voff, soff) __builtin_amdgcn_raw_buffer_store_b32(v, wrs, voff, soff, SC1)
#define BF8(x) __builtin_bit_cast(bf16x8, x)
__device__ __forceinline__ void glds16_dev(const char* sbase, unsigned voff, lds_ptr l) {
    unsigned keep;
    asm volatile("s_mov_b32 %0, m0\n\ts_mov_b32 m0, %3\n\ts_nop 0\n\tglobal_load_lds_dwordx4 %1, %2 sc1\n\ts_mov_b32 m0, %0"
                 : "=&s"(keep) : "v"(voff), "s"(sbase), "s"((unsigned)(size_t)l) : "memory");
}
#define GL_(v, s, o) "global_load_lds_dwordx4 " v ", " s " offset:" #o " sc1\n\t"
__device__ __forceinline__ void glds_first(unsigned voff, const char* s0, unsigned l0, const char* s1, unsigned l1, const char* s2, unsigned l2) {
    unsigned keep;
    asm volatile("s_mov_b32 %0, m0\n\ts_mov_b32 m0, %3\n\ts_nop 0\n\t" GL_("%1", "%2", 0) GL_("%1", "%2", 1024) GL_("%1", "%2", 2048)
                 "s_mov_b32 m0, %5\n\ts_nop 0\n\t" GL_("%1", "%4", 0) GL_("%1", "%4", 1024) GL_("%1", "%4", 2048) GL_("%1", "%4", 3072)
                 "s_mov_b32 m0, %7\n\ts_nop 0\n\t" GL_("%1", "%6", 0) GL_("%1", "%6", 1024) GL_("%1", "%6", 2048)
                 "s_mov_b32 m0, %0"
                 : "=&s"(keep) : "v"(voff), "s"(s0), "s"(l0), "s"(s1), "s"(l1), "s"(s2), "s"(l2) : "memory");
}
__device__ __forceinline__ void arrive(lds_ptr cnt, gu64* flag, u64 tag) {
    unsigned r, sr;
    asm volatile("s_waitcnt vmcnt(0)\n\ts_mov_b64 exec, 1\n\tds_add_rtn_u32 %0, %2, %3\n\ts_waitcnt lgkmcnt(0)\n\t"
                 "v_readfirstlane_b32 %1, %0\n\ts_cmp_lg_u32 %1, 3\n\ts_cbranch_scc1 1f\n\t"
                 "global_store_dwordx2 %4, %5, %6 sc1\n\t1:\n\ts_mov_b64 exec, -1"
                 : "=&v"(r), "=&s"(sr) : "v"((unsigned)(size_t)cnt), "v"(1u), "v"(0u), "v"(tag), "s"(flag) : "scc", "memory");
}
template <int SLEEP, int N>
__device__ __forceinline__ void wait_flags(gu64* const (&w)[N], u64 tag) {
#pragma unroll 1
    for (unsigned spins = 0; spins < (1u << 18); ++spins) {
        bool ok = true;
#pragma unroll
        for (int i = 0; i < N; ++i) ok &= __hip_atomic_load(w[i], RLX_AGENT) == tag;
        if (__all(ok)) break;
        __builtin_amdgcn_s_sleep(SLEEP);
    }
    __builtin_amdgcn_fence(__ATOMIC_ACQUIRE, "wavefront");
}
constexpr int K1_WP = 1024 + 32;
constexpr int K1_STAGE = 16384 + 32 * K1_WP;
constexpr int L_Q = 112 * 1024, L_STASH = 122 * 1024, L_CNT = 163824;
#define SB() __builtin_amdgcn_sched_barrier(0)
__device__ __forceinline__ void put_qkv(const u32x4 (&pv)[2], unsigned off0, unsigned off1, int ps, rsrc_t wrs) {
#pragma unroll
    for (int i = 0; i < 2; ++i) {
        ST8((u32x2{pv[i][0], pv[i][1]}), off0, i * ps);
        ST8((u32x2{pv[i][2], pv[i][3]}), off1, i * ps);
    }
}

template <bool VT, class M, class F>
__device__ __forceinline__ void k1_main(f32x4 (&acc)[4][4], lds_ptr lds, int xa, int wa, const char* tp, int tstep, M mid, F fin) {
    unsigned d0 = 0;
#define TOUCH() asm volatile("global_load_dword %0, %1, off" : "+v"(d0) : "v"(tp) : "memory")
    bf16x8 xf[2][4], wf[2][4];
    auto rd = [&](int so, int kk) {
#pragma unroll
        for (int j = 0; j < 4; ++j) {
            lds_ptr p = lds + so + wa + kk * 512 + j * 32;
            wf[kk][j] = cat8(lds_tr(p), lds_tr(p + 256));
        }
#pragma unroll
        for (int i = 0; i < 4; ++i) xf[kk][i] = lds_b128(lds + so + (xa ^ (kk * 64)) + i * 2048);
    };
    auto mm = [&](int kk) {
#pragma unroll
        for (int i = 0; i < 4; ++i)
#pragma unroll
            for (int j = 0; j < 4; ++j)
                acc[i][j] = VT ? MFMA16(xf[kk][i], wf[kk][j], acc[i][j]) : MFMA16(wf[kk][j], xf[kk][i], acc[i][j]);
    };
#define ILV() do { _Pragma("unroll") for (int q_ = 0; q_ < 12; ++q_) { __builtin_amdgcn_sched_group_barrier(0x100, 1, 0); __builtin_amdgcn_sched_group_barrier(0x8, 1, 0); } __builtin_amdgcn_sched_group_barrier(0x8, 4, 0); SB(); } while (0)
    if (tstep < 0) TOUCH();
    RAW_BAR();
    if (tstep == 0) TOUCH();
    rd(0, 0); SB();
    rd(0, 1);
    mm(0); ILV();
    int so = K1_STAGE;
#pragma unroll 1
    for (int t = 1; t < 10; ++t) {
        RAW_BAR();
        if (tstep == t) TOUCH();
        rd(so, 0);
        mm(1); ILV();
        rd(so, 1);
        mm(0); ILV();
        so = so == 2 * K1_STAGE ? 0 : so + K1_STAGE;
    }
    asm volatile("s_waitcnt vmcnt(0)" : "+v"(d0) :: "memory");
    mid();
#pragma unroll
    for (int j = 0; j < 5; ++j) {
        if (j < 4) {
#pragma unroll
            for (int i = 0; i < 4; ++i) acc[i][j] = VT ? MFMA16(xf[1][i], wf[1][j], acc[i][j]) : MFMA16(wf[1][j], xf[1][i], acc[i][j]);
        }
        SB();
        if (j) fin(j - 1);
        SB();
    }
}

#define SMFMAC(a, b8, c) __builtin_amdgcn_smfmac_f32_32x32x32_bf16(a, __builtin_bit_cast(bf16x16, b8), c, (int)0xDDDDDDDD, 0, 0)
#define LO8(s) __builtin_shufflevector(s, s, 0, 1, 2, 3, 4, 5, 6, 7)
#define HI8(s) __builtin_shufflevector(s, s, 8, 9, 10, 11, 12, 13, 14, 15)
constexpr unsigned ONE2 = 0x3F803F80u;

__device__ __forceinline__ float xhalf_max(float m) {
    auto r = __builtin_amdgcn_permlane32_swap(__float_as_uint(m), __float_as_uint(m), false, false);
    return fmaxf(__uint_as_float(r[0]), __uint_as_float(r[1]));
}
__device__ __forceinline__ float max3f(float a, float b, float c) { return __builtin_fmaxf(__builtin_fmaxf(a, b), c); }
__device__ __forceinline__ float max16(const f32x16& s, float m) {
#pragma unroll
    for (int r = 0; r < 16; r += 2) m = max3f(m, s[r], s[r + 1]);
    return m;
}
__device__ __forceinline__ float row_sum(const f32x16& o) {
    auto r = __builtin_amdgcn_permlane32_swap(__float_as_uint(o[8]), __float_as_uint(o[8]), false, false);
    return __uint_as_float(r[1]);
}
__device__ __forceinline__ u32x2 with_ref(u32x2 q, float m, int hh) {
    const unsigned h1 = __float_as_uint(-m) & 0xffff0000u;
    const float r1 = -m - __uint_as_float(h1);
    const unsigned h2 = __float_as_uint(r1) & 0xffff0000u;
    const unsigned h3 = __float_as_uint(r1 - __uint_as_float(h2)) >> 16;
    return hh ? u32x2{h1 | (h2 >> 16), h3} : q;
}
__device__ __forceinline__ void put_o(u32x4 v, u32x2 t, rsrc_t wrs, unsigned voff, int soff, int head, int hh) {
    const int c0 = (head >> 1) * 5;
    ST16(v, voff + hh * 256, soff + (c0 + (head & 1) * 2) * 256);
    if (!hh) ST8(t, voff, soff + (c0 + 4) * 256 + (head & 1) * 8);
}
__device__ __forceinline__ unsigned pack_o(float a, float b) { return min(pack2(a, b), 0xFFFFFFFEu); }
__device__ __forceinline__ void store_o(const f32x16& o, float f, rsrc_t wrs, unsigned voff, int soff, int head, int hh) {
    put_o(u32x4{pack_o(o[0] * f, o[1] * f), pack_o(o[2] * f, o[3] * f), pack_o(o[4] * f, o[5] * f), pack_o(o[6] * f, o[7] * f)},
          u32x2{pack_o(o[8] * f, o[9] * f), pack_o(o[10] * f, o[11] * f)}, wrs, voff, soff, head, hh);
}

#define EXP4(C, a) "v_exp_f32 v[" #C "+" #a "], v[" #C "+" #a "]\n\tv_exp_f32 v[" #C "+" #a "+1], v[" #C "+" #a "+1]\n\t"         \
                   "v_exp_f32 v[" #C "+" #a "+2], v[" #C "+" #a "+2]\n\tv_exp_f32 v[" #C "+" #a "+3], v[" #C "+" #a "+3]\n\t"
#define PV(O, V, P) "v_smfmac_f32_32x32x32_bf16 v[" #O ":" #O "+15], v[" #V ":" #V "+3], v[" #P ":" #P "+7], v40\n\t"
#define QK0(S, Q) "v_mfma_f32_32x32x16_bf16 v[" #S ":" #S "+15], v[24:27], v[" #Q ":" #Q "+3], 0\n\t"
#define QK1(S, Q, K) "v_mfma_f32_32x32x8bf16_1k v[" #S ":" #S "+15], v[" #K ":" #K "+1], v[" #Q "+4:" #Q "+5], v[" #S ":" #S "+15]\n\t"
#define VLOAD(T) "s_mov_b64 exec, %[vm]\n\tds_read_b128 v[32:35], v43 offset:" #T "*5120+1280\n\t"                                \
                 "ds_read_b128 v[36:39], v43 offset:" #T "*5120+1920\n\t"
#define KBLOAD(T, K) "s_mov_b64 exec, %[km]\n\tds_read_b64 v[" #K ":" #K "+1], v42 offset:(" #T ")*5120+1024\n\ts_mov_b64 exec, -1\n\t"
#define PAIR(T, KC, KN, MID)                                                                       \
    PV(96, 36, 72) VLOAD(T) KBLOAD(T + 1, KN) EXP4(48, 0)                                          \
    QK0(64, 16) "ds_read_b128 v[24:27], v41 offset:(" #T "+1)*5120\n\t" EXP4(48, 4)                \
    QK1(64, 16, KC) EXP4(48, 8)                                                                    \
    "s_waitcnt lgkmcnt(2)\n\t" PV(80, 32, 48) EXP4(48, 12) MID "s_nop 1\n\t"                       \
    PV(80, 36, 56) EXP4(64, 0)                                                                     \
    "s_waitcnt lgkmcnt(0)\n\t" QK0(48, 8) EXP4(64, 4)                                              \
    QK1(48, 8, KN) EXP4(64, 8)                                                                     \
    PV(96, 32, 64) EXP4(64, 12) "s_nop 1\n\t"
#define PAIR_LAST(T, KC)                                                                           \
    PV(96, 36, 72) VLOAD(T) "s_mov_b64 exec, -1\n\t" EXP4(48, 0)                                   \
    QK0(64, 16) EXP4(48, 4)                                                                        \
    QK1(64, 16, KC) EXP4(48, 8)                                                                    \
    "s_waitcnt lgkmcnt(0)\n\t" PV(80, 32, 48) EXP4(48, 12) "s_nop 1\n\t"                           \
    PV(80, 36, 56) EXP4(64, 0) EXP4(64, 4) EXP4(64, 8)                                             \
    PV(96, 32, 64) EXP4(64, 12) "s_nop 1\n\t"                                                      \
    PV(96, 36, 72) "s_nop 15\n\t"
#define ZERO4(R) "v_mov_b64 v[" #R ":" #R "+1], 0\n\tv_mov_b64 v[" #R "+2:" #R "+3], 0\n\t"
#define MAX2(i) "v_max3_f32 v14, v14, v[116+" #i "], v[117+" #i "]\n\tv_max3_f32 v22, v22, v[132+" #i "], v[133+" #i "]\n\t"
#define PRIO(n, L) "s_cmp_ge_u32 %[psw], 4\n\ts_cbranch_scc1 " #L "f\n\ts_setprio " #n "\n\t" #L ":\n\t"

__global__ __launch_bounds__(768, 3) void gated_mix_kernel(const u16* __restrict__ x, const u16* __restrict__ wA, const u16* __restrict__ wB,
                                                            const u16* __restrict__ woA, const u16* __restrict__ woB,
                                                            const u16* __restrict__ gate_w, char* __restrict__ ws, u16* out) {
    __shared__ __attribute__((aligned(16))) char smem[KV_HEAD];
    lds_ptr lds = (lds_ptr)smem;
    const int tid = threadIdx.x, lane = tid & 63, wid = __builtin_amdgcn_readfirstlane(tid >> 6);
    const int g = lane >> 4, r16 = lane & 15;
    const rsrc_t wrs = __builtin_amdgcn_make_buffer_rsrc(ws, 0, (int)WS_END, 0x00020000);
    const int xcd = blockIdx.x & 7, idx = blockIdx.x >> 3;
    const int mt = (idx & 7) * 2 + (xcd & 1), nt = (xcd >> 1) + (idx >> 3) * 4, m0 = mt * 128;
    if (wid == 8) *(LDS_AS u32x2*)(lds + L_CNT) = u32x2{0u, 0u};
    if (wid >= 8 && nt < 15) {
        const int lw = wid - 8;
        const unsigned xoff = (lw * 8 + (lane >> 3)) * (DM * 2) + (((lane & 7) ^ ((lane >> 3) & 7)) << 4);
        const unsigned woff = (32 * (g >> 1) + 4 * (g & 1) + lw) * 3840 + r16 * 16;
        const char* xs = (const char*)(x + (size_t)m0 * DM);
        const char* bs = (const char*)(wB + nt * 128);
        const char* as = (const char*)(wA + nt * 128);
        unsigned so = (unsigned)(size_t)lds;
#pragma unroll 1
        for (int t = -2; t < 9; ++t) {
            if (t >= 0) {
                WAIT_V(12);
                RAW_BAR();
            }
            if (t < 8) {
#pragma unroll 1
                for (int i = 0; i < 4; ++i) {
                    const unsigned lx = so + (lw + 4 * i) * 1024, lb = so + 16384 + (lw + 4 * i) * K1_WP;
                    glds16x3(xs + i * (32 * DM * 2), xoff, lx, bs + i * (8 * 3840), as + i * (8 * 3840), woff, lb, lb + 16 * K1_WP);
                }
                xs += 128; bs += 64 * 3840; as += 64 * 3840;
            }
            so = so == (unsigned)(size_t)lds + 2 * K1_STAGE ? (unsigned)(size_t)lds : so + K1_STAGE;
        }
        WAIT_V(0);
        RAW_BAR();
    }
    const int head = (blockIdx.x & 7) * 4 + ((blockIdx.x >> 3) & 3), qb = blockIdx.x >> 5;
    const int omt = head, ont = qb;
    const int r32 = lane & 31, hh = lane >> 5;
    const u32x4 vfill = r32 == 20 ? u32x4{ONE2, ONE2, ONE2, ONE2} : u32x4{0u, 0u, 0u, 0u};
    const int voff = hh * 320 + (r32 < 20 ? r32 : 0) * 16;
    const unsigned olane = (r32 >> 4) * (int)O_BLK + (r32 & 15) * 16;
    const int ps = nt < 5 ? (int)Q_TILE : (int)KV_TILE;
    const int wimg = (int)WS_WO + ont * 5120;
    gu64* const f1 = (gu64*)(ws + WS_F1);
    constexpr int P_OFF = 102400;
    const int pslot = g * 256;
    asm volatile("" : "+s"(out));
    unsigned fq, fo;
    auto fin_addr = [&]() {
        const int row = tid / 10, c = tid - row * 10;
        fq = P_OFF + ((row >> 4) * 5 + (c >> 1)) * 1024 + (c & 1) * 512 + (((row & 15) + c) & 15) * 16;
        fo = ((omt * 64 + row) * DM + ont * 80 + c * 8) * 2;
        asm volatile("" : "+v"(fq), "+v"(fo));
    };
    if (nt == 15) {
        const int task = mt * 10 + wid, ks = task >> 2, cg = task & 3;
        lds_ptr st = lds + wid * 10240;
        if (wid < 10) {
            const char* wsrc = (const char*)((ks >= 20 ? woB : woA) + cg * 160);
#pragma unroll
            for (int i = 0; i < 10; ++i) {
                const int ci = i * 64 + lane, slot = ci / 20, cc = ci - slot * 20;
                const int c = (ks >= 20 ? ks - 20 : ks) * 4 + (slot >> 3), e = slot & 7, p = c / 5, r = c - p * 5;
                const int k = r < 4 ? (2 * p + (r >> 1)) * 20 + 4 * (r & 1) + (e & 3) + 8 * (e >> 2) : (2 * p + (e >> 2)) * 20 + 16 + (e & 3);
                glds16(wsrc, k * (DM * 2) + cc * 16, st + i * 1024);
            }
        }
        u32x4 gv[2];
        gv[0] = *(const u32x4*)(gate_w + lane * 8);
        gv[1] = lane < 16 ? *(const u32x4*)(gate_w + 512 + lane * 8) : u32x4{0u, 0u, 0u, 0u};
        float s[11];
#pragma unroll
        for (int u = 0; u < 11; ++u) {
            const int row = wid + 12 * u < 128 ? wid + 12 * u : wid;
            const u16* xr = x + (size_t)(m0 + row) * DM;
            const u32x4 xv[2] = {*(const u32x4*)(xr + lane * 8), *(const u32x4*)(xr + 512 + r16 * 8)};
            float a = 0.f;
#pragma unroll
            for (int c = 0; c < 2; ++c)
#pragma unroll
                for (int e = 0; e < 4; ++e) {
                    a = fmaf(__uint_as_float(xv[c][e] << 16), __uint_as_float(gv[c][e] << 16), a);
                    a = fmaf(__uint_as_float(xv[c][e] & 0xffff0000u), __uint_as_float(gv[c][e] & 0xffff0000u), a);
                }
            s[u] = a;
        }
#pragma unroll
        for (int o = 32; o; o >>= 1)
#pragma unroll
            for (int u = 0; u < 11; ++u) s[u] += __shfl_xor(s[u], o);
        float z = s[0];
#pragma unroll
        for (int u = 1; u < 11; ++u) z = lane == u ? s[u] : z;
        if (lane < 11 && wid + 12 * lane < 128) ST4(__float_as_uint(1.f / (1.f + __expf(-z))), (wid + 12 * lane) * 4, (int)WS_G + m0 * 4);
        WAIT_V(0);
        if (wid < 10) {
            lds_ptr rp = st + (8 * g + (r16 >> 2)) * 320 + (lane & 3) * 8;
#pragma unroll
            for (int j = 0; j < 10; ++j)
                ST16(__builtin_bit_cast(u32x4, cat8(lds_tr(rp + j * 32), lds_tr(rp + j * 32 + 4 * 320))), lane * 16, (int)WS_WO + (ks * 40 + cg * 10 + j) * 1024);
        }
    }
    if (wid >= 8) {
        u64 tag = __hip_atomic_load((gu64*)(ws + WS_EPOCH), RLX_AGENT) + 1;
        const int hw = wid - 8;
        __builtin_amdgcn_s_setprio(3);
        RAW_BAR();
        f32x16 c0, c1;
        {
            const unsigned long long msk = ~(0x15554ull << r32) >> (4 * hh);
            const unsigned mlo = (unsigned)msk, mhi = (unsigned)(msk >> 32);
#pragma unroll
            for (int r = 0; r < 16; ++r) {
                const int u = (r & 3) + 8 * (r >> 2);
                c0[r] = r < 8 ? 0.f : __uint_as_float(__builtin_amdgcn_sbfe(mlo, (u - 16) & 31, 1) & 0xff800000u);
                c1[r] = __uint_as_float(__builtin_amdgcn_sbfe(u < 16 ? mlo : mhi, (u + 16) & 31, 1) & 0xff800000u);
            }
        }
        asm volatile("" : "+v"(c0), "+v"(c1));
        const int fc = head * 20 + (g & 1) * 19;
        gu64* const fkv = f1 + (((g < 2 ? 640 : 1280) + fc) >> 7) * 16 * FS;
        auto go = [&](int n, gu64* w0) {
            volatile LDS_AS int* gw = (volatile LDS_AS int*)(lds + L_CNT + 4);
            if (hw == 3) {
                gu64* const w[1] = {w0};
                wait_flags<2>(w, tag);
                *gw = n;
            } else {
#pragma unroll 1
                while (__builtin_amdgcn_readfirstlane(*gw) < n) __builtin_amdgcn_s_sleep(1);
                asm volatile("" ::: "memory");
            }
        };
        const int qp = (hw < 3 ? hw * 3 : 7) * 1024;
        const char* q0s = ws + WS_Q + (size_t)((NH + head) * 64 + qb * 8) * Q_TILE + qp;
        const char* k0s = ws + WS_KV + (size_t)(NH + head) * KV_HEAD + hw * 7168;
        const char* k1s = k0s + 4096;
        unsigned q0l = (unsigned)(size_t)lds + L_Q + qp, k0l = (unsigned)(size_t)lds + hw * 7168, k1l = k0l + 4096;
        asm volatile("" : "+s"(q0s), "+s"(k0s), "+s"(k1s), "+s"(q0l), "+s"(k0l), "+s"(k1l));
        go(1, r16 < 3 ? fkv + r16 * FS : f1 + ((fc >> 7) * 16 + qb * 2 + (lane & 1)) * FS);
        glds_first(lane * 16, q0s, q0l, k0s, k0l, k1s, k1l);
        const char* src = ws + WS_KV + (size_t)(NH + head) * KV_HEAD + hw * 1024;
#define LOADS(a, b) _Pragma("unroll 1") for (int i = a; i < b; ++i) glds16_dev(src + i * 4096, lane * 16, lds + hw * 1024 + i * 4096)
        WAIT_V(0); RAW_BAR();
        go(2, fkv + r16 * FS);
        LOADS(7, 28);
        WAIT_V(16); RAW_BAR();
        *(LDS_AS int*)lds = 0;
        if (nt < 15) {
            lds_ptr sp = lds + L_STASH + hw * 9216 + lane * 16;
            const u32x4 po = *(const LDS_AS u32x4*)(sp + 8192);
#pragma unroll
            for (int j = 0; j < 4; ++j) {
                const u32x4 pv[2] = {*(const LDS_AS u32x4*)(sp + j * 2048), *(const LDS_AS u32x4*)(sp + j * 2048 + 1024)};
                put_qkv(pv, po[j] & ~7u, (po[j] & ~7u) + (po[j] & 7u) * 128, ps, wrs);
            }
        }
        WAIT_V(0);
        RAW_BAR();
        if (hw == 0 && lane == 0) __hip_atomic_store(f1 + (256 + nt * 16 + mt) * FS, tag, RLX_AGENT);
        LOADS(28, 40);
#pragma unroll
        for (int i = 0; i < 2; ++i)
            put_o(u32x4{~0u, ~0u, ~0u, ~0u}, u32x2{~0u, ~0u}, wrs, olane, (int)WS_O + (128 + (qb * 8 + hw * 2 + i) * 2) * (int)O_BLK, head, hh);
        {
            const int sec = r16 >> 2, fm = max(qb * 2 - 1 + (r16 & 3), 0);
            gu64* const w[1] = {f1 + (256 + (g < 2 && sec < 3 && (r16 & 3) < 3 ? ((sec * 640 + fc) >> 7) * 16 + fm : 240 + r16)) * FS};
            wait_flags<8>(w, tag);
        }
        {
            const int q0 = qb * 8 + hw * 2;
            const int qA = (int)WS_Q + (head * 64 + q0) * (int)Q_TILE, k1 = (int)WS_KV + (head * 64 + q0) * (int)KV_TILE;
            u32x4 aqa[2], ka[3], vf[5];
            u32x2 aqb[2] = {{0u, 0u}, {0u, 0u}}, kb[3];
            float gv[2];
#pragma unroll
            for (int i = 0; i < 3; ++i) {
                const int kt = k1 + (i == 0 ? (q0 ? -(int)KV_TILE : 0) : (i - 1) * (int)KV_TILE);
                ka[i] = LD16(lane * 16, kt);
                kb[i] = LD8(r32 * 8, kt + 1024);
            }
#pragma unroll
            for (int i = 0; i < 2; ++i) aqa[i] = LD16(lane * 16, qA + i * (int)Q_TILE);
            if (!hh) {
#pragma unroll
                for (int i = 0; i < 2; ++i) aqb[i] = LD8(r32 * 8, qA + i * (int)Q_TILE + 1024);
            }
#pragma unroll
            for (int i = 0; i < 5; ++i) vf[i] = vfill;
            if (r32 < 20) {
                vf[0] = LD16(voff, k1 + (q0 ? -(int)KV_TILE : 0) + 1920);
#pragma unroll
                for (int i = 1; i < 5; ++i) vf[i] = LD16(voff, k1 + ((i - 1) >> 1) * (int)KV_TILE + 1280 + ((i - 1) & 1) * 640);
            }
#pragma unroll
            for (int i = 0; i < 2; ++i) gv[i] = __uint_as_float(LD4(r32 * 4, (int)WS_G + (q0 + i) * 128));
#pragma unroll
            for (int i = 1; i >= 0; --i) {
                if (i == 0 && q0 == 0) {
#pragma unroll
                    for (int r = 8; r < 16; ++r) c0[r] = -INFINITY;
                }
                f32x16 s0 = MFMA32(BF8(ka[i]), BF8(aqa[i]), c0);
                f32x16 s1 = MFMA32(BF8(ka[i + 1]), BF8(aqa[i]), c1);
                s0 = MFMA32H(kb[i], aqb[i], s0);
                s1 = MFMA32H(kb[i + 1], aqb[i], s1);
                float m = max3f(s0[8], s0[9], s0[10]);
                m = max3f(m, s0[11], s0[12]); m = max3f(m, s0[13], s0[14]);
                m = xhalf_max(max16(s1, fmaxf(m, s0[15])));
                const u32x2 ref = with_ref(u32x2{0u, 0u}, m, hh);
                s0 = MFMA32H((u32x2{ONE2, ONE2}), ref, s0);
                s1 = MFMA32H((u32x2{ONE2, ONE2}), ref, s1);
#pragma unroll
                for (int r = 8; r < 16; ++r) s0[r] = __builtin_amdgcn_exp2f(s0[r]);
#pragma unroll
                for (int r = 0; r < 16; ++r) s1[r] = __builtin_amdgcn_exp2f(s1[r]);
                f32x16 o = SMFMAC(BF8(vf[2 * i]), HI8(s0), f32x16{});
                o = SMFMAC(BF8(vf[2 * i + 1]), LO8(s1), o);
                o = SMFMAC(BF8(vf[2 * i + 2]), HI8(s1), o);
                const float f = gv[i] * __builtin_amdgcn_rcpf(row_sum(o));
                put_o(u32x4{pack2(o[0] * f, o[1] * f), pack2(o[2] * f, o[3] * f), pack2(o[4] * f, o[5] * f), pack2(o[6] * f, o[7] * f)},
                      u32x2{pack2(o[8] * f, o[9] * f), pack2(o[10] * f, o[11] * f)}, wrs, olane, (int)WS_O + (q0 + i) * 2 * (int)O_BLK, head, hh);
            }
        }
        {
            unsigned d;
            asm volatile("global_load_dword %0, %1, %2 sc1\n\ts_waitcnt vmcnt(0)"
                         : "=v"(d) : "v"(((omt * 64 + hw * 16 + r16) * DM + ont * 80 + (g & 1) * 78) * 2), "s"(out) : "memory");
        }
        arrive(lds, (gu64*)(ws + WS_FA) + (qb * 32 + head) * FS, tag);
        f32x4 acc[2][5];
        bf16x8 of[3][2], wf[3][5];
        const int osrc = (int)WS_O + (omt * 4 + (hw & 1) * 2) * (int)O_BLK + (hw >> 1) * 10240, wsrc = wimg + (hw >> 1) * 409600;
        auto ldo = [&](int ks) {
#pragma unroll
            for (int i = 0; i < 2; ++i) of[ks % 3][i] = BF8(LD16(lane * 16, osrc + i * (int)O_BLK + ks * 1024));
        };
        auto ldw = [&](int ks) {
#pragma unroll
            for (int j = 0; j < 5; ++j) wf[ks % 3][j] = BF8(LD16(lane * 16, wsrc + ks * 40960 + j * 1024));
        };
        ldw(0); ldw(1); ldw(2);
        SB();
        RAW_BAR();
        {
            gu64* const w[1] = {(gu64*)(ws + WS_FA) + ((omt >> 2) * 32 + r32) * FS};
            wait_flags<8>(w, tag);
        }
        ldo(0); ldo(1); ldo(2);
        SB();
#pragma unroll
        for (int ks = 0; ks < 10; ++ks) {
#pragma unroll
            for (int j = 0; j < 5; ++j) {
#pragma unroll
                for (int i = 0; i < 2; ++i) acc[i][j] = MFMA16(wf[ks % 3][j], of[ks % 3][i], ks ? acc[i][j] : (f32x4{0.f, 0.f, 0.f, 0.f}));
                SB();
                __builtin_amdgcn_s_sleep(1);
                SB();
            }
            if (ks < 7) { ldo(ks + 3); ldw(ks + 3); SB(); }
        }
#pragma unroll 1
        for (int ks = hw * 5; ks < hw * 5 + 5; ++ks)
#pragma unroll
            for (int j = 0; j < 5; ++j) glds16_dev(ws + wimg + (20 + ks) * 40960 + j * 1024, lane * 16, lds + ks * 5120 + j * 1024);
        RAW_BAR();
        if (blockIdx.x == 0 && hw == 0) {
            gu64* const w[4] = {f1 + (256 + lane) * FS, f1 + (320 + lane) * FS, f1 + (384 + lane) * FS, f1 + (448 + lane) * FS};
            wait_flags<8>(w, tag);
            if (lane == 0) __hip_atomic_store((gu64*)(ws + WS_EPOCH), tag, RLX_AGENT);
        }
        WAIT_V(0);
        auto put = [&]() {
#pragma unroll
            for (int i = 0; i < 2; ++i)
#pragma unroll
                for (int j = 0; j < 5; ++j)
                    *(LDS_AS f32x4*)(lds + P_OFF + (1 + (hw >> 1)) * 20480 + (((hw & 1) * 2 + i) * 5 + j) * 1024 + pslot + ((r16 + 2 * j + (g >> 1)) & 15) * 16) = acc[i][j];
        };
        fin_addr();
        RAW_BAR();
        if (hw >= 2) put();
        RAW_BAR();
        if (hw < 2) put();
    } else {
        if (nt < 15) {
            const u64 tv = __hip_atomic_load((gu64*)(ws + WS_EPOCH), RLX_AGENT) + 1;
            const char* tp;
            if (lane < 16) {
                tp = (const char*)((lane & 2 ? wA : wB) + nt * 128 + (lane & 1) * 64) + (size_t)((wid + 2) * 64 + mt * 4 + (lane >> 2)) * 3840;
            } else {
                const int r = nt * 9 + (lane < 25 ? lane - 16 : 0);
                tp = (const char*)(x + (size_t)(m0 + (r < 127 ? r : 127)) * DM) + (wid + 2) * 128;
            }
            const int br = wid < 4, wm = (wid >> 1) & 1, wn = wid & 1;
            const int xa = (wm * 64 + r16) * 128 + ((g ^ (lane & 7)) << 4);
            const int wa = 16384 + ((wid >> 2) * 16 + 4 * g + (r16 >> 2)) * K1_WP + (wn * 64 + 4 * (lane & 3)) * 2;
            const int tile0 = mt * 4 + wm * 2, cw = nt * 128 + wn * 64;
            unsigned poff[4], poff1[4];
#pragma unroll
            for (int j = 0; j < 4; ++j) {
                if (nt < 10) {
                    const int cp = (cw - (nt < 5 ? 0 : 640)) / 8 + j * 2 + (g >> 1), p = cp / 5, r = cp - p * 5;
                    const unsigned hoff = (int)(nt < 5 ? WS_Q : WS_KV) + ((br * NH + 2 * p) * 64 + tile0) * ps;
                    poff[j] = r == 2 ? hoff + (g & 1) * 64 * ps + 1024 + r16 * 8 : hoff + (r > 2 ? 64 * ps + (r - 3) * 512 : r * 512) + r16 * 16 + (g & 1) * 8;
                    poff1[j] = poff[j] + (r == 2 ? 128 : 256);
                } else {
                    const int c = cw - 1280 + j * 16 + r16, h = c / 20, d = c - h * 20;
                    poff[j] = (int)WS_KV + ((br * NH + h) * 64 + tile0) * (int)KV_TILE + 1280 + (g >> 1) * 320 + d * 16 + (g & 1) * 8;
                    poff1[j] = poff[j] + 640;
                }
            }
            asm volatile("" : "+v"(poff[0]), "+v"(poff[1]), "+v"(poff[2]), "+v"(poff[3]), "+v"(poff1[0]), "+v"(poff1[1]), "+v"(poff1[2]), "+v"(poff1[3]));
            const u64 tag = (u64)__builtin_amdgcn_readfirstlane((unsigned)(tv >> 32)) << 32 | (unsigned)__builtin_amdgcn_readfirstlane((unsigned)tv);
            f32x4 acc[4][4];
#pragma unroll
            for (int i = 0; i < 4; ++i)
#pragma unroll
                for (int j = 0; j < 4; ++j) acc[i][j] = f32x4{0.f, 0.f, 0.f, 0.f};
            if (br) __builtin_amdgcn_s_setprio(1);
            const bool urgent = br && (nt < 5 || mt < 3);
            gu64* bflag = f1 + (nt * 16 + mt) * FS;
            asm volatile("" : "+s"(bflag));
            auto mid = [&]() {
                if (!urgent) RAW_BAR();
                if (br && !urgent) __builtin_amdgcn_s_sleep(16);
            };
            auto fin = [&](int j) {
                if (nt < 5) {
#pragma unroll
                    for (int i = 0; i < 4; ++i) acc[i][j] *= QSCALE;
                }
                u32x4 pv[2];
#pragma unroll
                for (int i = 0; i < 2; ++i) {
                    const f32x4 a = acc[2 * i][j], b = acc[2 * i + 1][j];
                    pv[i] = u32x4{pack2(a[0], a[1]), pack2(a[2], a[3]), pack2(b[0], b[1]), pack2(b[2], b[3])};
                }
                if (br) {
                    put_qkv(pv, poff[j], poff1[j], ps, wrs);
                } else {
#pragma unroll
                    for (int i = 0; i < 2; ++i) *(LDS_AS u32x4*)(lds + L_STASH + (wid - 4) * 9216 + j * 2048 + i * 1024 + lane * 16) = pv[i];
                }
            };
            if (nt < 10) k1_main<false>(acc, lds, xa, wa, tp, wid - 1, mid, fin);
            else k1_main<true>(acc, lds, xa, wa, tp, wid - 1, mid, fin);
            if (urgent) RAW_BAR();
            if (br) {
                arrive(lds + L_CNT, bflag, tag);
            } else {
                *(LDS_AS u32x4*)(lds + L_STASH + (wid - 4) * 9216 + 8192 + lane * 16) =
                    u32x4{poff[0] + ((poff1[0] - poff[0]) >> 7), poff[1] + ((poff1[1] - poff[1]) >> 7), poff[2] + ((poff1[2] - poff[2]) >> 7), poff[3] + ((poff1[3] - poff[3]) >> 7)};
            }
        } else {
            RAW_BAR();
            WAIT_V(0);
        }
        const int qt = qb * 8 + wid;
        const unsigned long long vmask = 0x000fffff000fffffull;
        f32x16 o, o2;
        float gate;
        u32x4 qa0, vlo = vfill, vhi = vfill;
        u32x2 qb0;
        const unsigned l0 = (unsigned)(size_t)lds, kap = (wid >> 2) * (int)KV_TILE;
        unsigned a_ka = l0 + kap + lane * 16, a_kb = l0 + kap + r32 * 8, a_v = l0 + kap + voff;
        const unsigned qs0 = L_Q + wid * (int)Q_TILE - kap, qs1 = L_Q + (wid ^ 4) * (int)Q_TILE - kap;
        int nbar = 0x53, it = 7;
        __builtin_amdgcn_s_setprio(1);
        asm volatile(
            "v_mov_b32 v28, %[one]\n\tv_mov_b32 v29, %[one]\n\tv_mov_b32 v30, %[one]\n\tv_mov_b32 v31, %[one]\n\tv_mov_b32 v40, %[sidx]\n\t"
            ZERO4(72) ZERO4(76) ZERO4(80) ZERO4(84) ZERO4(88) ZERO4(92) ZERO4(96) ZERO4(100) ZERO4(104) ZERO4(108)
            "v_mov_b64 v[12:13], 0\n\tv_mov_b64 v[20:21], 0\n\t"
            "v_add_u32 v48, %[qs0], v41\n\tv_add_u32 v49, %[qs1], v41\n\tv_subrev_u32 v50, %[kap], v41\n\t"
            "v_add_u32 v51, %[qs0], v42\n\tv_add_u32 v52, %[qs1], v42\n\tv_subrev_u32 v53, %[kap], v42\n\t"
            "s_waitcnt lgkmcnt(0)\n\ts_barrier\n\t"
            "ds_read_b128 v[112:115], v50\n\tds_read_b128 v[8:11], v48\n\tds_read_b128 v[16:19], v49\n\t"
            "s_mov_b64 exec, %[km]\n\t"
            "ds_read_b64 v[30:31], v53 offset:1024\n\tds_read_b64 v[12:13], v51 offset:1024\n\tds_read_b64 v[20:21], v52 offset:1024\n\t"
            "ds_read_b64 v[28:29], v42 offset:1024\n\t"
            "s_mov_b64 exec, -1\n\t"
            "ds_read_b128 v[24:27], v41\n\t"
            "s_waitcnt lgkmcnt(5)\n\t"
            "v_mfma_f32_32x32x16_bf16 v[116:131], v[112:115], v[8:11], 0\n\t"
            "v_mfma_f32_32x32x16_bf16 v[132:147], v[112:115], v[16:19], 0\n\t"
            "s_waitcnt lgkmcnt(0)\n\t"
            QK1(116, 8, 30) QK1(132, 16, 30) QK0(48, 8)
            "s_nop 10\n\t"
            "v_max_f32 v14, v116, v117\n\tv_max_f32 v22, v132, v133\n\t"
            MAX2(2) MAX2(4) MAX2(6) MAX2(8) MAX2(10) MAX2(12) MAX2(14)
            "v_mov_b32 v15, v14\n\tv_mov_b32 v23, v22\n\ts_nop 1\n\t"
            "v_permlane32_swap_b32 v14, v15\n\tv_permlane32_swap_b32 v22, v23\n\t"
            "s_not_b64 exec, %[km]\n\t"
            "v_min_f32 v14, -v14, -v15\n\tv_min_f32 v22, -v22, -v23\n\t"
            "v_and_b32 v12, 0xffff0000, v14\n\tv_and_b32 v20, 0xffff0000, v22\n\t"
            "s_mov_b64 exec, -1\n\ts_nop 1\n\t"
            QK1(48, 8, 28)
            "s_nop 5\n\t"
            "1:\n\t"
            PRIO(2, 3) PAIR(0, 28, 30, "") PAIR(1, 30, 28, "") PAIR(2, 28, 30, PRIO(0, 4)) PAIR(3, 30, 28, "")
            "v_add_u32 v41, 20480, v41\n\tv_add_u32 v42, 20480, v42\n\tv_add_u32 v43, 20480, v43\n\t"
            "s_bitcmp0_b32 %[nbar], 0\n\t"
            "s_cbranch_scc1 2f\n\t"
            "s_barrier\n\t"
            "2:\n\t"
            "s_lshr_b32 %[nbar], %[nbar], 1\n\t"
            "s_sub_i32 %[it], %[it], 1\n\t"
            "s_cmp_lg_u32 %[it], 0\n\t"
            "s_cbranch_scc1 1b\n\t"
            "global_load_dword v44, %[goff], %[gb] sc1\n\t"
            PRIO(2, 3) PAIR(0, 28, 30, "") PAIR(1, 30, 28, "") PAIR(2, 28, 30, PRIO(0, 4)) PAIR_LAST(3, 30)
            "s_waitcnt vmcnt(0)"
            : "=&{v44}"(gate), "=&{v[80:95]}"(o), "=&{v[96:111]}"(o2), "=&{v[8:11]}"(qa0), "=&{v[12:13]}"(qb0), "+{v[32:35]}"(vlo),
              "+{v[36:39]}"(vhi), "+{v41}"(a_ka), "+{v42}"(a_kb), "+{v43}"(a_v), [nbar] "+s"(nbar), [it] "+s"(it)
            : [vm] "s"(vmask), [km] "s"(0xffffffffull), [one] "s"(ONE2), [sidx] "s"(0xDDDDDDDDu), [psw] "s"(wid), [goff] "v"(r32 * 4),
              [gb] "s"(ws + WS_G + qt * 128), [qs0] "s"(qs0), [qs1] "s"(qs1), [kap] "s"(kap)
            : "v14", "v15", "v16", "v17", "v18", "v19", "v20", "v21", "v22", "v23", "v24", "v25", "v26", "v27", "v28", "v29", "v30",
              "v31", "v40", "v48", "v49", "v50", "v51", "v52", "v53", "v54", "v55", "v56", "v57", "v58", "v59", "v60", "v61", "v62",
              "v63", "v64", "v65", "v66", "v67", "v68", "v69", "v70", "v71", "v72", "v73", "v74", "v75", "v76", "v77", "v78", "v79",
              "v112", "v113", "v114", "v115", "v116", "v117", "v118", "v119", "v120", "v121", "v122", "v123", "v124", "v125", "v126",
              "v127", "v128", "v129", "v130", "v131", "v132", "v133", "v134", "v135", "v136", "v137", "v138", "v139", "v140", "v141",
              "v142", "v143", "v144", "v145", "v146", "v147", "scc", "memory");
        __builtin_amdgcn_s_setprio(3);
#pragma unroll
        for (int i = 0; i < 3; ++i)
            *(LDS_AS f32x4*)(lds + P_OFF + (wid ^ 4) * 3072 + i * 1024 + lane * 16) = f32x4{o2[4 * i], o2[4 * i + 1], o2[4 * i + 2], o2[4 * i + 3]};
        RAW_BAR();
#pragma unroll
        for (int i = 0; i < 3; ++i) {
            const f32x4 t = *(const LDS_AS f32x4*)(lds + P_OFF + wid * 3072 + i * 1024 + lane * 16);
#pragma unroll
            for (int e = 0; e < 4; ++e) o[4 * i + e] += t[e];
        }
        const float gB = 1.f - gate;
        float l = row_sum(o);
        float asum = 0.f;
#pragma unroll
        for (int i = 0; i < 12; ++i) asum += fabsf(o[i]);
        if (__builtin_expect(__ballot(l >= 0x1p-64f && l <= 0x1p64f && asum <= 0x1p126f) == ~0ull, 1)) {
            store_o(o, gB * __builtin_amdgcn_rcpf(l), wrs, olane, (int)WS_O + (128 + qt * 2) * (int)O_BLK, head, hh);
        } else {
            const int kv = (int)WS_KV + (NH + head) * (int)KV_HEAD;
            u32x2 qr = hh ? u32x2{0u, 0u} : qb0;
            float mt2 = -INFINITY;
            o = f32x16{};
#pragma unroll 1
            for (int t = 0; t < 128; ++t) {
                const int kt = kv + (t & 63) * (int)KV_TILE;
                u32x2 kb = {ONE2, ONE2};
                if (!hh) kb = LD8(r32 * 8, kt + 1024);
                f32x16 sc = MFMA32(BF8(LD16(lane * 16, kt)), BF8(qa0), f32x16{});
                sc = MFMA32H(kb, qr, sc);
                if (t < 64) {
                    mt2 = max16(sc, mt2);
                    if (t == 63) qr = with_ref(qb0, xhalf_max(mt2), hh);
                } else {
                    u32x4 v0 = vfill, v1 = vfill;
                    if (r32 < 20) { v0 = LD16(voff, kt + 1280); v1 = LD16(voff, kt + 1920); }
#pragma unroll
                    for (int r = 0; r < 16; ++r) sc[r] = __builtin_amdgcn_exp2f(sc[r]);
                    o = SMFMAC(BF8(v0), LO8(sc), o);
                    o = SMFMAC(BF8(v1), HI8(sc), o);
                }
            }
            l = row_sum(o);
            asm volatile("" : "+v"(l));
            store_o(o, gB * __builtin_amdgcn_rcpf(l), wrs, olane, (int)WS_O + (128 + qt * 2) * (int)O_BLK, head, hh);
        }
        RAW_BAR();
        const int mb = wid & 3, kh = wid >> 2;
        const int osrc = (int)WS_O + (128 + omt * 4 + mb) * (int)O_BLK + kh * 10240;
        u32x4 of[10];
#pragma unroll
        for (int s = 0; s < 10; ++s) of[s] = LD16(lane * 16, osrc + s * 1024);
        SB();
        f32x4 acc[5];
        lds_ptr pp = lds + P_OFF + kh * 40960 + mb * 5120 + pslot;
#pragma unroll
        for (int j = 0; j < 5; ++j) acc[j] = f32x4{0.f, 0.f, 0.f, 0.f};
        if (kh) {
#pragma unroll
            for (int j = 0; j < 5; ++j) acc[j] = *(const LDS_AS f32x4*)(pp + j * 1024 + ((r16 + 2 * j + (g >> 1)) & 15) * 16);
        }
        lds_ptr wp = lds + kh * 51200 + lane * 16;
        bf16x8 wf[2][10];
#pragma unroll
        for (int j = 0; j < 10; ++j) wf[0][j] = lds_b128(wp + j * 1024);
        auto bad = [&](int s) {
            const u32x4 a = __builtin_elementwise_max(of[s], of[s + 1]);
            return __any(max(max(a[0], a[1]), max(a[2], a[3])) == ~0u);
        };
#pragma unroll
        for (int s = 0; s < 10; s += 2) {
            if (s < 8) {
#pragma unroll
                for (int j = 0; j < 10; ++j) wf[(s / 2 + 1) & 1][j] = lds_b128(wp + (s + 2) * 5120 + j * 1024);
            }
            if (bad(s)) {
#pragma unroll 1
                for (unsigned spins = 0; spins < (1u << 18); ++spins) {
                    asm volatile("" ::: "memory");
#pragma unroll
                    for (int t = 8; t >= s; t -= 2)
                        if (t == s || bad(t)) { of[t] = LD16(lane * 16, osrc + t * 1024); of[t + 1] = LD16(lane * 16, osrc + (t + 1) * 1024); }
                    if (!bad(s)) break;
                }
            }
            if (s == 2) fin_addr();
#pragma unroll
            for (int j = 0; j < 10; ++j) acc[j % 5] = MFMA16(wf[(s / 2) & 1][j], BF8(of[s + j / 5]), acc[j % 5]);
        }
#pragma unroll
        for (int j = 0; j < 5; ++j) *(LDS_AS f32x4*)(pp + j * 1024 + ((r16 + 2 * j + (g >> 1)) & 15) * 16) = acc[j];
    }
    RAW_BAR();
    if (tid < 640) {
        f32x4 t[6];
#pragma unroll
        for (int p = 0; p < 6; ++p) t[p] = *(const LDS_AS f32x4*)(lds + fq + (p >> 1) * 20480 + (p & 1) * 256);
        SB();
        const f32x4 a = t[0] + t[2] + t[4], b = t[1] + t[3] + t[5];
        const u32x4 v = {pack2(a[0], a[1]), pack2(a[2], a[3]), pack2(b[0], b[1]), pack2(b[2], b[3])};
        __builtin_amdgcn_raw_buffer_store_b128(v, __builtin_amdgcn_make_buffer_rsrc(out, 0, SEQ * DM * 2, 0x00020000), fo, 0, SC1);
    }
}

extern "C" void kernel_launch(void* const* d_in, const int* in_sizes, int n_in, void* d_out, int out_size, void* d_ws, size_t ws_size,
                              hipStream_t stream) {
    gated_mix_kernel<<<256, 768, 0, stream>>>((const u16*)d_in[0], (const u16*)d_in[1], (const u16*)d_in[4], (const u16*)d_in[2],
                                              (const u16*)d_in[5], (const u16*)d_in[6], (char*)d_ws, (u16*)d_out);
}
```

```cpp
#include <hip/hip_runtime.h>
#include <hip/hip_bf16.h>

typedef __attribute__((ext_vector_type(8))) __bf16 bf16x8;
typedef __attribute__((ext_vector_type(16))) __bf16 bf16x16;
typedef __attribute__((ext_vector_type(2))) __bf16 bf16x2;
typedef __attribute__((ext_vector_type(4))) short s16x4;
typedef __attribute__((ext_vector_type(2))) float f32x2;
typedef __attribute__((ext_vector_type(4))) float f32x4;
typedef __attribute__((ext_vector_type(16))) float f32x16;
typedef __attribute__((ext_vector_type(2))) unsigned u32x2;
typedef __attribute__((ext_vector_type(4))) unsigned u32x4;
typedef unsigned short u16;
#define LDS_AS __attribute__((address_space(3)))
typedef LDS_AS char* lds_ptr;

constexpr int SEQ = 2048, DM = 640, NH = 32;
constexpr size_t KV_TILE = 2560, KV_HEAD = 64 * KV_TILE;
constexpr size_t Q_TILE = 1280, Q_HEAD = 64 * Q_TILE;
constexpr size_t WS_KV = 0;
constexpr size_t WS_Q = WS_KV + 2 * NH * KV_HEAD;
constexpr size_t O_BLK = 80 * 256;
constexpr size_t WS_O = WS_Q + 2 * NH * Q_HEAD;
constexpr size_t WS_WO = WS_O + 2 * 128 * O_BLK;
constexpr size_t WS_G = WS_WO + 40 * 40 * 1024;
constexpr size_t WS_F1 = WS_G + SEQ * 4;
constexpr int FS = 16;
constexpr size_t WS_FA = WS_F1 + 512 * 128;
constexpr size_t WS_EPOCH = WS_FA + 256 * 128;
constexpr size_t WS_END = WS_EPOCH + 128;
constexpr float QSCALE = 0.22360679774997896f * 1.4426950408889634f;

#define WAIT_V(n) asm volatile("s_waitcnt vmcnt(" #n ")" ::: "memory")
#define RAW_BAR() asm volatile("s_waitcnt lgkmcnt(0)\n\ts_barrier" ::: "memory")
#define MFMA16(a, b, c) __builtin_amdgcn_mfma_f32_16x16x32_bf16(a, b, c, 0, 0, 0)
#define MFMA32(a, b, c) __builtin_amdgcn_mfma_f32_32x32x16_bf16(a, b, c, 0, 0, 0)
#define MFMA32H(a, b, c) __builtin_amdgcn_mfma_f32_32x32x8bf16_1k(__builtin_bit_cast(s16x4, a), __builtin_bit_cast(s16x4, b), c, 0, 0, 0)

__device__ __forceinline__ unsigned pack2(float lo, float hi) {
    f32x2 v = {lo, hi};
    return __builtin_bit_cast(unsigned, __builtin_convertvector(v, bf16x2));
}
__device__ __forceinline__ void glds16(const char* sbase, unsigned voff, lds_ptr l) {
    unsigned keep;
    asm volatile("s_mov_b32 %0, m0\n\ts_mov_b32 m0, %3\n\ts_nop 0\n\tglobal_load_lds_dwordx4 %1, %2\n\ts_mov_b32 m0, %0"
                 : "=&s"(keep) : "v"(voff), "s"(sbase), "s"((unsigned)(size_t)l) : "memory");
}
__device__ __forceinline__ void glds16x3(const char* s0, unsigned v0, unsigned l0, const char* s1, const char* s2, unsigned v1, unsigned l1, unsigned l2) {
    unsigned keep;
    asm volatile("s_mov_b32 %0, m0\n\ts_mov_b32 m0, %3\n\ts_nop 0\n\tglobal_load_lds_dwordx4 %1, %2\n\t"
                 "s_mov_b32 m0, %7\n\ts_nop 0\n\tglobal_load_lds_dwordx4 %4, %5\n\t"
                 "s_mov_b32 m0, %8\n\ts_nop 0\n\tglobal_load_lds_dwordx4 %4, %6\n\ts_mov_b32 m0, %0"
                 : "=&s"(keep) : "v"(v0), "s"(s0), "s"(l0), "v"(v1), "s"(s1), "s"(s2), "s"(l1), "s"(l2) : "memory");
}
__device__ __forceinline__ bf16x8 lds_b128(lds_ptr p) { return *(const LDS_AS bf16x8*)p; }
__device__ __forceinline__ s16x4 lds_tr(lds_ptr p) { return __builtin_amdgcn_ds_read_tr16_b64_v4i16((LDS_AS s16x4*)p); }
__device__ __forceinline__ bf16x8 cat8(s16x4 a, s16x4 b) {
    return __builtin_bit_cast(bf16x8, __builtin_shufflevector(a, b, 0, 1, 2, 3, 4, 5, 6, 7));
}
typedef __amdgpu_buffer_rsrc_t rsrc_t;
typedef unsigned long long u64;
typedef __attribute__((address_space(1))) u64 gu64;
#define RLX_AGENT __ATOMIC_RELAXED, __HIP_MEMORY_SCOPE_AGENT
constexpr int SC1 = 16;
#define LD16(voff, soff) __builtin_amdgcn_raw_buffer_load_b128(wrs, voff, soff, SC1)
#define LD8(voff, soff) __builtin_amdgcn_raw_buffer_load_b64(wrs, voff, soff, SC1)
#define LD4(voff, soff) __builtin_amdgcn_raw_buffer_load_b32(wrs, voff, soff, SC1)
#define ST16(v, voff, soff) __builtin_amdgcn_raw_buffer_store_b128(v, wrs, (voff) + (soff), 0, SC1)
#define ST8(v, voff, soff) __builtin_amdgcn_raw_buffer_store_b64(v, wrs, voff, soff, SC1)
#define ST4(v, voff, soff) __builtin_amdgcn_raw_buffer_store_b32(v, wrs, voff, soff, SC1)
#define BF8(x) __builtin_bit_cast(bf16x8, x)
__device__ __forceinline__ void glds16_dev(const char* sbase, unsigned voff, lds_ptr l) {
    unsigned keep;
    asm volatile("s_mov_b32 %0, m0\n\ts_mov_b32 m0, %3\n\ts_nop 0\n\tglobal_load_lds_dwordx4 %1, %2 sc1\n\ts_mov_b32 m0, %0"
                 : "=&s"(keep) : "v"(voff), "s"(sbase), "s"((unsigned)(size_t)l) : "memory");
}
#define GL_(v, s, o) "global_load_lds_dwordx4 " v ", " s " offset:" #o " sc1\n\t"
__device__ __forceinline__ void glds_first(unsigned voff, const char* s0, unsigned l0, const char* s1, unsigned l1, const char* s2, unsigned l2) {
    unsigned keep;
    asm volatile("s_mov_b32 %0, m0\n\ts_mov_b32 m0, %3\n\ts_nop 0\n\t" GL_("%1", "%2", 0) GL_("%1", "%2", 1024) GL_("%1", "%2", 2048)
                 "s_mov_b32 m0, %5\n\ts_nop 0\n\t" GL_("%1", "%4", 0) GL_("%1", "%4", 1024) GL_("%1", "%4", 2048) GL_("%1", "%4", 3072)
                 "s_mov_b32 m0, %7\n\ts_nop 0\n\t" GL_("%1", "%6", 0) GL_("%1", "%6", 1024) GL_("%1", "%6", 2048)
                 "s_mov_b32 m0, %0"
                 : "=&s"(keep) : "v"(voff), "s"(s0), "s"(l0), "s"(s1), "s"(l1), "s"(s2), "s"(l2) : "memory");
}
__device__ __forceinline__ void glds_five(unsigned voff, const char* s0, unsigned l0) {
    unsigned keep;
    asm volatile("s_mov_b32 %0, m0\n\ts_mov_b32 m0, %3\n\ts_nop 0\n\t" GL_("%1", "%2", 0) GL_("%1", "%2", 1024) GL_("%1", "%2", 2048)
                 GL_("%1", "%2", 3072) "s_mov_b32 m0, %5\n\ts_nop 0\n\t" GL_("%1", "%4", 0) "s_mov_b32 m0, %0"
                 : "=&s"(keep) : "v"(voff), "s"(s0), "s"(l0), "s"(s0 + 4096), "s"(l0 + 4096) : "memory");
}
__device__ __forceinline__ void arrive(lds_ptr cnt, gu64* flag, u64 tag) {
    unsigned r, sr;
    asm volatile("s_waitcnt vmcnt(0)\n\ts_mov_b64 exec, 1\n\tds_add_rtn_u32 %0, %2, %3\n\ts_waitcnt lgkmcnt(0)\n\t"
                 "v_readfirstlane_b32 %1, %0\n\ts_cmp_lg_u32 %1, 3\n\ts_cbranch_scc1 1f\n\t"
                 "global_store_dwordx2 %4, %5, %6 sc1\n\t1:\n\ts_mov_b64 exec, -1"
                 : "=&v"(r), "=&s"(sr) : "v"((unsigned)(size_t)cnt), "v"(1u), "v"(0u), "v"(tag), "s"(flag) : "scc", "memory");
}
template <int SLEEP, int N>
__device__ __forceinline__ void wait_flags(gu64* const (&w)[N], u64 tag) {
#pragma unroll 1
    for (unsigned spins = 0; spins < (1u << 18); ++spins) {
        bool ok = true;
#pragma unroll
        for (int i = 0; i < N; ++i) ok &= __hip_atomic_load(w[i], RLX_AGENT) == tag;
        if (__all(ok)) break;
        __builtin_amdgcn_s_sleep(SLEEP);
    }
    __builtin_amdgcn_fence(__ATOMIC_ACQUIRE, "wavefront");
}
constexpr int K1_WP = 1024 + 32;
constexpr int K1_STAGE = 16384 + 32 * K1_WP;
constexpr int L_Q = 112 * 1024, L_STASH = 122 * 1024, L_CNT = 163824;
#define SB() __builtin_amdgcn_sched_barrier(0)
__device__ __forceinline__ void put_qkv(const u32x4 (&pv)[2], unsigned off0, unsigned off1, int ps, rsrc_t wrs) {
#pragma unroll
    for (int i = 0; i < 2; ++i) {
        ST8((u32x2{pv[i][0], pv[i][1]}), off0, i * ps);
        ST8((u32x2{pv[i][2], pv[i][3]}), off1, i * ps);
    }
}

template <bool VT, class M, class F>
__device__ __forceinline__ void k1_main(f32x4 (&acc)[4][4], lds_ptr lds, int xa, int wa, const char* tp, int tstep, M mid, F fin) {
    unsigned d0 = 0;
#define TOUCH() asm volatile("global_load_dword %0, %1, off" : "+v"(d0) : "v"(tp) : "memory")
    bf16x8 xf[2][4], wf[2][4];
    auto rd = [&](int so, int kk) {
#pragma unroll
        for (int j = 0; j < 4; ++j) {
            lds_ptr p = lds + so + wa + kk * 512 + j * 32;
            wf[kk][j] = cat8(lds_tr(p), lds_tr(p + 256));
        }
#pragma unroll
        for (int i = 0; i < 4; ++i) xf[kk][i] = lds_b128(lds + so + (xa ^ (kk * 64)) + i * 2048);
    };
    auto mm = [&](int kk) {
#pragma unroll
        for (int i = 0; i < 4; ++i)
#pragma unroll
            for (int j = 0; j < 4; ++j)
                acc[i][j] = VT ? MFMA16(xf[kk][i], wf[kk][j], acc[i][j]) : MFMA16(wf[kk][j], xf[kk][i], acc[i][j]);
    };
#define SG(m, n) __builtin_amdgcn_sched_group_barrier(m, n, 0)
#define ILV() do { _Pragma("unroll") for (int q_ = 0; q_ < 4; ++q_) { SG(0x100, 1); SG(0x8, 1); SG(0x100, 1); SG(0x8, 1); SG(0x100, 1); SG(0x8, 2); } SB(); } while (0)
    if (tstep < 0) TOUCH();
    RAW_BAR();
    if (tstep == 0) TOUCH();
    rd(0, 0); SB();
    rd(0, 1);
    mm(0); ILV();
    int so = K1_STAGE;
#pragma unroll 1
    for (int t = 1; t < 10; ++t) {
        RAW_BAR();
        if (tstep == t) TOUCH();
        rd(so, 0);
        mm(1); ILV();
        rd(so, 1);
        mm(0); ILV();
        so = so == 2 * K1_STAGE ? 0 : so + K1_STAGE;
    }
    asm volatile("s_waitcnt vmcnt(0)" : "+v"(d0) :: "memory");
    mid();
#pragma unroll
    for (int j = 0; j < 5; ++j) {
        if (j < 4) {
#pragma unroll
            for (int i = 0; i < 4; ++i) acc[i][j] = VT ? MFMA16(xf[1][i], wf[1][j], acc[i][j]) : MFMA16(wf[1][j], xf[1][i], acc[i][j]);
        }
        SB();
        if (j) fin(j - 1);
        SB();
    }
}

#define SMFMAC(a, b8, c) __builtin_amdgcn_smfmac_f32_32x32x32_bf16(a, __builtin_bit_cast(bf16x16, b8), c, (int)0xDDDDDDDD, 0, 0)
#define LO8(s) __builtin_shufflevector(s, s, 0, 1, 2, 3, 4, 5, 6, 7)
#define HI8(s) __builtin_shufflevector(s, s, 8, 9, 10, 11, 12, 13, 14, 15)
constexpr unsigned ONE2 = 0x3F803F80u;

__device__ __forceinline__ float xhalf_max(float m) {
    auto r = __builtin_amdgcn_permlane32_swap(__float_as_uint(m), __float_as_uint(m), false, false);
    return fmaxf(__uint_as_float(r[0]), __uint_as_float(r[1]));
}
__device__ __forceinline__ float max3f(float a, float b, float c) { return __builtin_fmaxf(__builtin_fmaxf(a, b), c); }
__device__ __forceinline__ float max16(const f32x16& s, float m) {
#pragma unroll
    for (int r = 0; r < 16; r += 2) m = max3f(m, s[r], s[r + 1]);
    return m;
}
__device__ __forceinline__ float row_sum(const f32x16& o) {
    auto r = __builtin_amdgcn_permlane32_swap(__float_as_uint(o[8]), __float_as_uint(o[8]), false, false);
    return __uint_as_float(r[1]);
}
__device__ __forceinline__ u32x2 with_ref(u32x2 q, float m, int hh) {
    const unsigned h1 = __float_as_uint(-m) & 0xffff0000u;
    const float r1 = -m - __uint_as_float(h1);
    const unsigned h2 = __float_as_uint(r1) & 0xffff0000u;
    const unsigned h3 = __float_as_uint(r1 - __uint_as_float(h2)) >> 16;
    return hh ? u32x2{h1 | (h2 >> 16), h3} : q;
}
__device__ __forceinline__ void put_o(u32x4 v, u32x2 t, rsrc_t wrs, unsigned voff, int soff, int head, int hh) {
    const int c0 = (head >> 1) * 5;
    ST16(v, voff + hh * 256, soff + (c0 + (head & 1) * 2) * 256);
    if (!hh) ST8(t, voff, soff + (c0 + 4) * 256 + (head & 1) * 8);
}
__device__ __forceinline__ unsigned pack_o(float a, float b) { return min(pack2(a, b), 0xFFFFFFFEu); }
__device__ __forceinline__ void store_o(const f32x16& o, float f, rsrc_t wrs, unsigned voff, int soff, int head, int hh) {
    put_o(u32x4{pack_o(o[0] * f, o[1] * f), pack_o(o[2] * f, o[3] * f), pack_o(o[4] * f, o[5] * f), pack_o(o[6] * f, o[7] * f)},
          u32x2{pack_o(o[8] * f, o[9] * f), pack_o(o[10] * f, o[11] * f)}, wrs, voff, soff, head, hh);
}

#define EXP4(C, a) "v_exp_f32 v[" #C "+" #a "], v[" #C "+" #a "]\n\tv_exp_f32 v[" #C "+" #a "+1], v[" #C "+" #a "+1]\n\t"         \
                   "v_exp_f32 v[" #C "+" #a "+2], v[" #C "+" #a "+2]\n\tv_exp_f32 v[" #C "+" #a "+3], v[" #C "+" #a "+3]\n\t"
#define PV(O, V, P) "v_smfmac_f32_32x32x32_bf16 v[" #O ":" #O "+15], v[" #V ":" #V "+3], v[" #P ":" #P "+7], v40\n\t"
#define QK0(S, Q) "v_mfma_f32_32x32x16_bf16 v[" #S ":" #S "+15], v[24:27], v[" #Q ":" #Q "+3], 0\n\t"
#define QK1(S, Q, K) "v_mfma_f32_32x32x8bf16_1k v[" #S ":" #S "+15], v[" #K ":" #K "+1], v[" #Q "+4:" #Q "+5], v[" #S ":" #S "+15]\n\t"
#define VLOAD(T) "s_mov_b64 exec, %[vm]\n\tds_read_b128 v[32:35], v43 offset:" #T "*5120+1280\n\t"                                \
                 "ds_read_b128 v[36:39], v43 offset:" #T "*5120+1920\n\ts_mov_b64 exec, -1\n\t"
#define KBLOAD(T, K) "s_mov_b64 exec, %[km]\n\tds_read_b64 v[" #K ":" #K "+1], v42 offset:(" #T ")*5120+1024\n\ts_mov_b64 exec, -1\n\t"
#define PAIR(T, KC, KN, MID)                                                                       \
    PV(96, 36, 72) VLOAD(T) EXP4(48, 0)                                                            \
    QK0(64, 16) "ds_read_b128 v[24:27], v41 offset:(" #T "+1)*5120\n\t" EXP4(48, 4)                \
    QK1(64, 16, KC) KBLOAD(T + 1, KN) EXP4(48, 8)                                                  \
    "s_waitcnt lgkmcnt(2)\n\t" PV(80, 32, 48) EXP4(48, 12) MID "s_nop 1\n\t"                       \
    PV(80, 36, 56) EXP4(64, 0)                                                                     \
    "s_waitcnt lgkmcnt(0)\n\t" QK0(48, 8) EXP4(64, 4)                                              \
    QK1(48, 8, KN) EXP4(64, 8)                                                                     \
    PV(96, 32, 64) EXP4(64, 12) "s_nop 1\n\t"
#define PAIR_LAST(T, KC)                                                                           \
    PV(96, 36, 72) VLOAD(T) EXP4(48, 0)                                                            \
    QK0(64, 16) EXP4(48, 4)                                                                        \
    QK1(64, 16, KC) EXP4(48, 8)                                                                    \
    "s_waitcnt lgkmcnt(0)\n\t" PV(80, 32, 48) EXP4(48, 12) "s_nop 1\n\t"                           \
    PV(80, 36, 56) EXP4(64, 0) EXP4(64, 4) EXP4(64, 8)                                             \
    PV(96, 32, 64) EXP4(64, 12) "s_nop 1\n\t"                                                      \
    PV(96, 36, 72) "s_nop 15\n\t"
#define ZERO4(R) "v_mov_b64 v[" #R ":" #R "+1], 0\n\tv_mov_b64 v[" #R "+2:" #R "+3], 0\n\t"
#define MAX2(i) "v_max3_f32 v14, v14, v[116+" #i "], v[117+" #i "]\n\tv_max3_f32 v22, v22, v[132+" #i "], v[133+" #i "]\n\t"
#define PRIO(n, L) "s_cmp_ge_u32 %[psw], 4\n\ts_cbranch_scc1 " #L "f\n\ts_setprio " #n "\n\t" #L ":\n\t"

__global__ __launch_bounds__(768, 3) void gated_mix_kernel(const u16* __restrict__ x, const u16* __restrict__ wA, const u16* __restrict__ wB,
                                                            const u16* __restrict__ woA, const u16* __restrict__ woB,
                                                            const u16* __restrict__ gate_w, char* __restrict__ ws, u16* out) {
    __shared__ __attribute__((aligned(16))) char smem[KV_HEAD];
    lds_ptr lds = (lds_ptr)smem;
    const int tid = threadIdx.x, lane = tid & 63, wid = __builtin_amdgcn_readfirstlane(tid >> 6);
    const int g = lane >> 4, r16 = lane & 15;
    const rsrc_t wrs = __builtin_amdgcn_make_buffer_rsrc(ws, 0, (int)WS_END, 0x00020000);
    const int xcd = blockIdx.x & 7, idx = blockIdx.x >> 3;
    const int mt = (idx & 7) * 2 + (xcd & 1), nt = (xcd >> 1) + (idx >> 3) * 4, m0 = mt * 128;
    if (wid == 8) *(LDS_AS u32x2*)(lds + L_CNT) = u32x2{0u, 0u};
    if (wid >= 8 && nt < 15) {
        const int lw = wid - 8;
        const unsigned xoff = (lw * 8 + (lane >> 3)) * (DM * 2) + (((lane & 7) ^ ((lane >> 3) & 7)) << 4);
        const unsigned woff = (32 * (g >> 1) + 4 * (g & 1) + lw) * 3840 + r16 * 16;
        const char* xs = (const char*)(x + (size_t)m0 * DM);
        const char* bs = (const char*)(wB + nt * 128);
        const char* as = (const char*)(wA + nt * 128);
        unsigned so = (unsigned)(size_t)lds;
#pragma unroll 1
        for (int t = -2; t < 9; ++t) {
            if (t >= 0) {
                WAIT_V(12);
                RAW_BAR();
            }
            if (t < 8) {
#pragma unroll 1
                for (int i = 0; i < 4; ++i) {
                    const unsigned lx = so + (lw + 4 * i) * 1024, lb = so + 16384 + (lw + 4 * i) * K1_WP;
                    glds16x3(xs + i * (32 * DM * 2), xoff, lx, bs + i * (8 * 3840), as + i * (8 * 3840), woff, lb, lb + 16 * K1_WP);
                }
                xs += 128; bs += 64 * 3840; as += 64 * 3840;
            }
            so = so == (unsigned)(size_t)lds + 2 * K1_STAGE ? (unsigned)(size_t)lds : so + K1_STAGE;
        }
        WAIT_V(0);
        RAW_BAR();
    }
    const int head = (blockIdx.x & 7) * 4 + ((blockIdx.x >> 3) & 3), qb = blockIdx.x >> 5;
    const int omt = head, ont = qb;
    const int r32 = lane & 31, hh = lane >> 5;
    const u32x4 vfill = r32 == 20 ? u32x4{ONE2, ONE2, ONE2, ONE2} : u32x4{0u, 0u, 0u, 0u};
    const int voff = hh * 320 + (r32 < 20 ? r32 : 0) * 16;
    const unsigned olane = (r32 >> 4) * (int)O_BLK + (r32 & 15) * 16;
    const int ps = nt < 5 ? (int)Q_TILE : (int)KV_TILE;
    const int wimg = (int)WS_WO + ont * 5120;
    gu64* const f1 = (gu64*)(ws + WS_F1);
    constexpr int P_OFF = 102400;
    const int pslot = g * 256;
    asm volatile("" : "+s"(out));
    unsigned fq, fo;
    auto fin_addr = [&]() {
        const int row = tid / 10, c = tid - row * 10;
        fq = P_OFF + ((row >> 4) * 5 + (c >> 1)) * 1024 + (c & 1) * 512 + (((row & 15) + c) & 15) * 16;
        fo = ((omt * 64 + row) * DM + ont * 80 + c * 8) * 2;
        asm volatile("" : "+v"(fq), "+v"(fo));
    };
    if (nt == 15) {
        const int task = mt * 10 + wid, ks = task >> 2, cg = task & 3;
        lds_ptr st = lds + wid * 10240;
        if (wid < 10) {
            const char* wsrc = (const char*)((ks >= 20 ? woB : woA) + cg * 160);
#pragma unroll
            for (int i = 0; i < 10; ++i) {
                const int ci = i * 64 + lane, slot = ci / 20, cc = ci - slot * 20;
                const int c = (ks >= 20 ? ks - 20 : ks) * 4 + (slot >> 3), e = slot & 7, p = c / 5, r = c - p * 5;
                const int k = r < 4 ? (2 * p + (r >> 1)) * 20 + 4 * (r & 1) + (e & 3) + 8 * (e >> 2) : (2 * p + (e >> 2)) * 20 + 16 + (e & 3);
                glds16(wsrc, k * (DM * 2) + cc * 16, st + i * 1024);
            }
        }
        u32x4 gv[2];
        gv[0] = *(const u32x4*)(gate_w + lane * 8);
        gv[1] = lane < 16 ? *(const u32x4*)(gate_w + 512 + lane * 8) : u32x4{0u, 0u, 0u, 0u};
        float s[11];
#pragma unroll
        for (int u = 0; u < 11; ++u) {
            const int row = wid + 12 * u < 128 ? wid + 12 * u : wid;
            const u16* xr = x + (size_t)(m0 + row) * DM;
            const u32x4 xv[2] = {*(const u32x4*)(xr + lane * 8), *(const u32x4*)(xr + 512 + r16 * 8)};
            float a = 0.f;
#pragma unroll
            for (int c = 0; c < 2; ++c)
#pragma unroll
                for (int e = 0; e < 4; ++e) {
                    a = fmaf(__uint_as_float(xv[c][e] << 16), __uint_as_float(gv[c][e] << 16), a);
                    a = fmaf(__uint_as_float(xv[c][e] & 0xffff0000u), __uint_as_float(gv[c][e] & 0xffff0000u), a);
                }
            s[u] = a;
        }
#pragma unroll
        for (int o = 32; o; o >>= 1)
#pragma unroll
            for (int u = 0; u < 11; ++u) s[u] += __shfl_xor(s[u], o);
        float z = s[0];
#pragma unroll
        for (int u = 1; u < 11; ++u) z = lane == u ? s[u] : z;
        if (lane < 11 && wid + 12 * lane < 128) ST4(__float_as_uint(1.f / (1.f + __expf(-z))), (wid + 12 * lane) * 4, (int)WS_G + m0 * 4);
        WAIT_V(0);
        if (wid < 10) {
            lds_ptr rp = st + (8 * g + (r16 >> 2)) * 320 + (lane & 3) * 8;
#pragma unroll
            for (int j = 0; j < 10; ++j)
                ST16(__builtin_bit_cast(u32x4, cat8(lds_tr(rp + j * 32), lds_tr(rp + j * 32 + 4 * 320))), lane * 16, (int)WS_WO + (ks * 40 + cg * 10 + j) * 1024);
        }
    }
    if (wid >= 8) {
        u64 tag = __hip_atomic_load((gu64*)(ws + WS_EPOCH), RLX_AGENT) + 1;
        const int hw = wid - 8;
        __builtin_amdgcn_s_setprio(3);
        RAW_BAR();
        f32x16 c0, c1;
        {
            const unsigned long long msk = ~(0x15554ull << r32) >> (4 * hh);
            const unsigned mlo = (unsigned)msk, mhi = (unsigned)(msk >> 32);
#pragma unroll
            for (int r = 0; r < 16; ++r) {
                const int u = (r & 3) + 8 * (r >> 2);
                c0[r] = r < 8 ? 0.f : __uint_as_float(__builtin_amdgcn_sbfe(mlo, (u - 16) & 31, 1) & 0xff800000u);
                c1[r] = __uint_as_float(__builtin_amdgcn_sbfe(u < 16 ? mlo : mhi, (u + 16) & 31, 1) & 0xff800000u);
            }
        }
        asm volatile("" : "+v"(c0), "+v"(c1));
        const int fc = head * 20 + (g & 1) * 19;
        gu64* const fkv = f1 + (((g < 2 ? 640 : 1280) + fc) >> 7) * 16 * FS;
        auto go = [&](int n, gu64* w0) {
            volatile LDS_AS int* gw = (volatile LDS_AS int*)(lds + L_CNT + 4);
            if (hw == 3) {
                gu64* const w[1] = {w0};
                wait_flags<2>(w, tag);
                *gw = n;
            } else {
#pragma unroll 1
                while (__builtin_amdgcn_readfirstlane(*gw) < n) __builtin_amdgcn_s_sleep(1);
                asm volatile("" ::: "memory");
            }
        };
        const int qp = (hw < 3 ? hw * 3 : 7) * 1024;
        const char* q0s = ws + WS_Q + (size_t)((NH + head) * 64 + qb * 8) * Q_TILE + qp;
        const char* k0s = ws + WS_KV + (size_t)(NH + head) * KV_HEAD + hw * 7168;
        const char* k1s = k0s + 4096;
        unsigned q0l = (unsigned)(size_t)lds + L_Q + qp, k0l = (unsigned)(size_t)lds + hw * 7168, k1l = k0l + 4096;
        asm volatile("" : "+s"(q0s), "+s"(k0s), "+s"(k1s), "+s"(q0l), "+s"(k0l), "+s"(k1l));
        go(1, r16 < 3 ? fkv + r16 * FS : f1 + ((fc >> 7) * 16 + qb * 2 + (lane & 1)) * FS);
        glds_first(lane * 16, q0s, q0l, k0s, k0l, k1s, k1l);
        const char* src = ws + WS_KV + (size_t)(NH + head) * KV_HEAD + hw * 1024;
#define LOADS(a, b) _Pragma("unroll 1") for (int i = a; i < b; ++i) glds16_dev(src + i * 4096, lane * 16, lds + hw * 1024 + i * 4096)
        WAIT_V(0); RAW_BAR();
        go(2, fkv + r16 * FS);
        LOADS(7, 28);
        WAIT_V(16); RAW_BAR();
        *(LDS_AS int*)lds = 0;
        if (nt < 15) {
            lds_ptr sp = lds + L_STASH + hw * 9216 + lane * 16;
            const u32x4 po = *(const LDS_AS u32x4*)(sp + 8192);
#pragma unroll
            for (int j = 0; j < 4; ++j) {
                const u32x4 pv[2] = {*(const LDS_AS u32x4*)(sp + j * 2048), *(const LDS_AS u32x4*)(sp + j * 2048 + 1024)};
                put_qkv(pv, po[j] & ~7u, (po[j] & ~7u) + (po[j] & 7u) * 128, ps, wrs);
            }
        }
        WAIT_V(0);
        RAW_BAR();
        if (hw == 0 && lane == 0) __hip_atomic_store(f1 + (256 + nt * 16 + mt) * FS, tag, RLX_AGENT);
        LOADS(28, 40);
#pragma unroll
        for (int i = 0; i < 2; ++i)
            put_o(u32x4{~0u, ~0u, ~0u, ~0u}, u32x2{~0u, ~0u}, wrs, olane, (int)WS_O + (128 + (qb * 8 + hw * 2 + i) * 2) * (int)O_BLK, head, hh);
        {
            const int sec = r16 >> 2, fm = max(qb * 2 - 1 + (r16 & 3), 0);
            gu64* const w[1] = {f1 + (256 + (g < 2 && sec < 3 && (r16 & 3) < 3 ? ((sec * 640 + fc) >> 7) * 16 + fm : 240 + r16)) * FS};
            wait_flags<8>(w, tag);
        }
        {
            const int q0 = qb * 8 + hw * 2;
            const int qA = (int)WS_Q + (head * 64 + q0) * (int)Q_TILE, k1 = (int)WS_KV + (head * 64 + q0) * (int)KV_TILE;
            u32x4 aqa[2], ka[3], vf[5];
            u32x2 aqb[2] = {{0u, 0u}, {0u, 0u}}, kb[3];
            float gv[2];
#pragma unroll
            for (int i = 0; i < 3; ++i) {
                const int kt = k1 + (i == 0 ? (q0 ? -(int)KV_TILE : 0) : (i - 1) * (int)KV_TILE);
                ka[i] = LD16(lane * 16, kt);
                kb[i] = LD8(r32 * 8, kt + 1024);
            }
#pragma unroll
            for (int i = 0; i < 2; ++i) aqa[i] = LD16(lane * 16, qA + i * (int)Q_TILE);
            if (!hh) {
#pragma unroll
                for (int i = 0; i < 2; ++i) aqb[i] = LD8(r32 * 8, qA + i * (int)Q_TILE + 1024);
            }
#pragma unroll
            for (int i = 0; i < 5; ++i) vf[i] = vfill;
            if (r32 < 20) {
                vf[0] = LD16(voff, k1 + (q0 ? -(int)KV_TILE : 0) + 1920);
#pragma unroll
                for (int i = 1; i < 5; ++i) vf[i] = LD16(voff, k1 + ((i - 1) >> 1) * (int)KV_TILE + 1280 + ((i - 1) & 1) * 640);
            }
#pragma unroll
            for (int i = 0; i < 2; ++i) gv[i] = __uint_as_float(LD4(r32 * 4, (int)WS_G + (q0 + i) * 128));
#pragma unroll
            for (int i = 1; i >= 0; --i) {
                if (i == 0 && q0 == 0) {
#pragma unroll
                    for (int r = 8; r < 16; ++r) c0[r] = -INFINITY;
                }
                f32x16 s0 = MFMA32(BF8(ka[i]), BF8(aqa[i]), c0);
                f32x16 s1 = MFMA32(BF8(ka[i + 1]), BF8(aqa[i]), c1);
                s0 = MFMA32H(kb[i], aqb[i], s0);
                s1 = MFMA32H(kb[i + 1], aqb[i], s1);
                float m = max3f(s0[8], s0[9], s0[10]);
                m = max3f(m, s0[11], s0[12]); m = max3f(m, s0[13], s0[14]);
                m = xhalf_max(max16(s1, fmaxf(m, s0[15])));
                const u32x2 ref = with_ref(u32x2{0u, 0u}, m, hh);
                s0 = MFMA32H((u32x2{ONE2, ONE2}), ref, s0);
                s1 = MFMA32H((u32x2{ONE2, ONE2}), ref, s1);
#pragma unroll
                for (int r = 8; r < 16; ++r) s0[r] = __builtin_amdgcn_exp2f(s0[r]);
#pragma unroll
                for (int r = 0; r < 16; ++r) s1[r] = __builtin_amdgcn_exp2f(s1[r]);
                f32x16 o = SMFMAC(BF8(vf[2 * i]), HI8(s0), f32x16{});
                o = SMFMAC(BF8(vf[2 * i + 1]), LO8(s1), o);
                o = SMFMAC(BF8(vf[2 * i + 2]), HI8(s1), o);
                const float f = gv[i] * __builtin_amdgcn_rcpf(row_sum(o));
                put_o(u32x4{pack2(o[0] * f, o[1] * f), pack2(o[2] * f, o[3] * f), pack2(o[4] * f, o[5] * f), pack2(o[6] * f, o[7] * f)},
                      u32x2{pack2(o[8] * f, o[9] * f), pack2(o[10] * f, o[11] * f)}, wrs, olane, (int)WS_O + (q0 + i) * 2 * (int)O_BLK, head, hh);
            }
        }
        arrive(lds, (gu64*)(ws + WS_FA) + (qb * 32 + head) * FS, tag);
        f32x4 acc[2][5];
        bf16x8 of[3][2], wf[3][5];
        const int osrc = (int)WS_O + (omt * 4 + (hw & 1) * 2) * (int)O_BLK + (hw >> 1) * 10240, wsrc = wimg + (hw >> 1) * 409600;
        auto ldo = [&](int ks) {
#pragma unroll
            for (int i = 0; i < 2; ++i) of[ks % 3][i] = BF8(LD16(lane * 16, osrc + i * (int)O_BLK + ks * 1024));
        };
        auto ldw = [&](int ks) {
#pragma unroll
            for (int j = 0; j < 5; ++j) wf[ks % 3][j] = BF8(LD16(lane * 16, wsrc + ks * 40960 + j * 1024));
        };
#define WLOAD(ks) glds_five(lane * 16, ws + wimg + (20 + (ks)) * 40960, (unsigned)(size_t)lds + (ks) * 5120)
        if (hw) WLOAD(hw);
        WLOAD(4 + hw);
        ldw(0); ldw(1); ldw(2);
        SB();
        RAW_BAR();
        {
            gu64* const w[1] = {(gu64*)(ws + WS_FA) + ((omt >> 2) * 32 + r32) * FS};
            wait_flags<8>(w, tag);
        }
        ldo(0); ldo(1); ldo(2);
        SB();
#pragma unroll
        for (int ks = 0; ks < 10; ++ks) {
#pragma unroll
            for (int j = 0; j < 5; ++j) {
#pragma unroll
                for (int i = 0; i < 2; ++i) acc[i][j] = MFMA16(wf[ks % 3][j], of[ks % 3][i], ks ? acc[i][j] : (f32x4{0.f, 0.f, 0.f, 0.f}));
                SB();
                __builtin_amdgcn_s_sleep(1);
                SB();
            }
            if (ks < 7) { ldo(ks + 3); ldw(ks + 3); SB(); }
            if (ks == 7) RAW_BAR();
        }
        if (!hw) WLOAD(0);
        WLOAD(8 + hw); WLOAD(12 + hw); WLOAD(16 + hw);
        if (blockIdx.x == 0 && hw == 0) {
            gu64* const w[4] = {f1 + (256 + lane) * FS, f1 + (320 + lane) * FS, f1 + (384 + lane) * FS, f1 + (448 + lane) * FS};
            wait_flags<8>(w, tag);
            if (lane == 0) __hip_atomic_store((gu64*)(ws + WS_EPOCH), tag, RLX_AGENT);
        }
        WAIT_V(0);
        auto put = [&]() {
#pragma unroll
            for (int i = 0; i < 2; ++i)
#pragma unroll
                for (int j = 0; j < 5; ++j)
                    *(LDS_AS f32x4*)(lds + P_OFF + (1 + (hw >> 1)) * 20480 + (((hw & 1) * 2 + i) * 5 + j) * 1024 + pslot + ((r16 + 2 * j + (g >> 1)) & 15) * 16) = acc[i][j];
        };
        fin_addr();
        RAW_BAR();
        if (hw >= 2) put();
        RAW_BAR();
        if (hw < 2) put();
    } else {
        if (nt < 15) {
            const u64 tv = __hip_atomic_load((gu64*)(ws + WS_EPOCH), RLX_AGENT) + 1;
            const char* tp;
            if (lane < 16) {
                tp = (const char*)((lane & 2 ? wA : wB) + nt * 128 + (lane & 1) * 64) + (size_t)((wid + 2) * 64 + mt * 4 + (lane >> 2)) * 3840;
            } else {
                const int r = nt * 9 + (lane < 25 ? lane - 16 : 0);
                tp = (const char*)(x + (size_t)(m0 + (r < 127 ? r : 127)) * DM) + (wid + 2) * 128;
            }
            const int br = wid < 4, wm = (wid >> 1) & 1, wn = wid & 1;
            const int xa = (wm * 64 + r16) * 128 + ((g ^ (lane & 7)) << 4);
            const int wa = 16384 + ((wid >> 2) * 16 + 4 * g + (r16 >> 2)) * K1_WP + (wn * 64 + 4 * (lane & 3)) * 2;
            const int tile0 = mt * 4 + wm * 2, cw = nt * 128 + wn * 64;
            unsigned poff[4], poff1[4];
#pragma unroll
            for (int j = 0; j < 4; ++j) {
                if (nt < 10) {
                    const int cp = (cw - (nt < 5 ? 0 : 640)) / 8 + j * 2 + (g >> 1), p = cp / 5, r = cp - p * 5;
                    const unsigned hoff = (int)(nt < 5 ? WS_Q : WS_KV) + ((br * NH + 2 * p) * 64 + tile0) * ps;
                    poff[j] = r == 2 ? hoff + (g & 1) * 64 * ps + 1024 + r16 * 8 : hoff + (r > 2 ? 64 * ps + (r - 3) * 512 : r * 512) + r16 * 16 + (g & 1) * 8;
                    poff1[j] = poff[j] + (r == 2 ? 128 : 256);
                } else {
                    const int c = cw - 1280 + j * 16 + r16, h = c / 20, d = c - h * 20;
                    poff[j] = (int)WS_KV + ((br * NH + h) * 64 + tile0) * (int)KV_TILE + 1280 + (g >> 1) * 320 + d * 16 + (g & 1) * 8;
                    poff1[j] = poff[j] + 640;
                }
            }
            asm volatile("" : "+v"(poff[0]), "+v"(poff[1]), "+v"(poff[2]), "+v"(poff[3]), "+v"(poff1[0]), "+v"(poff1[1]), "+v"(poff1[2]), "+v"(poff1[3]));
            const u64 tag = (u64)__builtin_amdgcn_readfirstlane((unsigned)(tv >> 32)) << 32 | (unsigned)__builtin_amdgcn_readfirstlane((unsigned)tv);
            f32x4 acc[4][4];
#pragma unroll
            for (int i = 0; i < 4; ++i)
#pragma unroll
                for (int j = 0; j < 4; ++j) acc[i][j] = f32x4{0.f, 0.f, 0.f, 0.f};
            if (br) __builtin_amdgcn_s_setprio(1);
            const bool urgent = br && (nt < 5 || mt < 3);
            gu64* bflag = f1 + (nt * 16 + mt) * FS;
            asm volatile("" : "+s"(bflag));
            auto mid = [&]() {
                if (!urgent) RAW_BAR();
                if (br && !urgent) __builtin_amdgcn_s_sleep(16);
            };
            auto fin = [&](int j) {
                if (nt < 5) {
#pragma unroll
                    for (int i = 0; i < 4; ++i) acc[i][j] *= QSCALE;
                }
                u32x4 pv[2];
#pragma unroll
                for (int i = 0; i < 2; ++i) {
                    const f32x4 a = acc[2 * i][j], b = acc[2 * i + 1][j];
                    pv[i] = u32x4{pack2(a[0], a[1]), pack2(a[2], a[3]), pack2(b[0], b[1]), pack2(b[2], b[3])};
                }
                if (br) {
                    put_qkv(pv, poff[j], poff1[j], ps, wrs);
                } else {
#pragma unroll
                    for (int i = 0; i < 2; ++i) *(LDS_AS u32x4*)(lds + L_STASH + (wid - 4) * 9216 + j * 2048 + i * 1024 + lane * 16) = pv[i];
                }
            };
            if (nt < 10) k1_main<false>(acc, lds, xa, wa, tp, wid - 1, mid, fin);
            else k1_main<true>(acc, lds, xa, wa, tp, wid - 1, mid, fin);
            if (urgent) RAW_BAR();
            if (br) {
                arrive(lds + L_CNT, bflag, tag);
            } else {
                *(LDS_AS u32x4*)(lds + L_STASH + (wid - 4) * 9216 + 8192 + lane * 16) =
                    u32x4{poff[0] + ((poff1[0] - poff[0]) >> 7), poff[1] + ((poff1[1] - poff[1]) >> 7), poff[2] + ((poff1[2] - poff[2]) >> 7), poff[3] + ((poff1[3] - poff[3]) >> 7)};
            }
        } else {
            RAW_BAR();
            WAIT_V(0);
        }
        const int qt = qb * 8 + wid;
        const unsigned long long vmask = 0x000fffff000fffffull;
        f32x16 o, o2;
        float gate;
        u32x4 qa0, vlo = vfill, vhi = vfill;
        u32x2 qb0;
        const unsigned l0 = (unsigned)(size_t)lds, kap = (wid >> 2) * (int)KV_TILE;
        unsigned a_ka = l0 + kap + lane * 16, a_kb = l0 + kap + r32 * 8, a_v = l0 + kap + voff;
        const unsigned qs0 = L_Q + wid * (int)Q_TILE - kap, qs1 = L_Q + (wid ^ 4) * (int)Q_TILE - kap;
        int nbar = 0x53, it = 7;
        __builtin_amdgcn_s_setprio(1);
        asm volatile(
            "v_mov_b32 v28, %[one]\n\tv_mov_b32 v29, %[one]\n\tv_mov_b32 v30, %[one]\n\tv_mov_b32 v31, %[one]\n\tv_mov_b32 v40, %[sidx]\n\t"
            ZERO4(72) ZERO4(76) ZERO4(80) ZERO4(84) ZERO4(88) ZERO4(92) ZERO4(96) ZERO4(100) ZERO4(104) ZERO4(108)
            "v_mov_b64 v[12:13], 0\n\tv_mov_b64 v[20:21], 0\n\t"
            "v_add_u32 v48, %[qs0], v41\n\tv_add_u32 v49, %[qs1], v41\n\tv_subrev_u32 v50, %[kap], v41\n\t"
            "v_add_u32 v51, %[qs0], v42\n\tv_add_u32 v52, %[qs1], v42\n\tv_subrev_u32 v53, %[kap], v42\n\t"
            "s_waitcnt lgkmcnt(0)\n\ts_barrier\n\t"
            "ds_read_b128 v[112:115], v50\n\tds_read_b128 v[8:11], v48\n\tds_read_b128 v[16:19], v49\n\t"
            "s_mov_b64 exec, %[km]\n\t"
            "ds_read_b64 v[30:31], v53 offset:1024\n\tds_read_b64 v[12:13], v51 offset:1024\n\tds_read_b64 v[20:21], v52 offset:1024\n\t"
            "ds_read_b64 v[28:29], v42 offset:1024\n\t"
            "s_mov_b64 exec, -1\n\t"
            "ds_read_b128 v[24:27], v41\n\t"
            "s_waitcnt lgkmcnt(5)\n\t"
            "v_mfma_f32_32x32x16_bf16 v[116:131], v[112:115], v[8:11], 0\n\t"
            "v_mfma_f32_32x32x16_bf16 v[132:147], v[112:115], v[16:19], 0\n\t"
            "s_waitcnt lgkmcnt(0)\n\t"
            QK1(116, 8, 30) QK1(132, 16, 30) QK0(48, 8)
            "s_nop 10\n\t"
            "v_max_f32 v14, v116, v117\n\tv_max_f32 v22, v132, v133\n\t"
            MAX2(2) MAX2(4) MAX2(6) MAX2(8) MAX2(10) MAX2(12) MAX2(14)
            "v_mov_b32 v15, v14\n\tv_mov_b32 v23, v22\n\ts_nop 1\n\t"
            "v_permlane32_swap_b32 v14, v15\n\tv_permlane32_swap_b32 v22, v23\n\t"
            "s_not_b64 exec, %[km]\n\t"
            "v_min_f32 v14, -v14, -v15\n\tv_min_f32 v22, -v22, -v23\n\t"
            "v_and_b32 v12, 0xffff0000, v14\n\tv_and_b32 v20, 0xffff0000, v22\n\t"
            "s_mov_b64 exec, -1\n\ts_nop 1\n\t"
            QK1(48, 8, 28)
            "s_nop 5\n\t"
            "1:\n\t"
            PRIO(2, 3) PAIR(0, 28, 30, "") PAIR(1, 30, 28, "") PAIR(2, 28, 30, PRIO(0, 4)) PAIR(3, 30, 28, "")
            "v_add_u32 v41, 20480, v41\n\tv_add_u32 v42, 20480, v42\n\tv_add_u32 v43, 20480, v43\n\t"
            "s_bitcmp0_b32 %[nbar], 0\n\t"
            "s_cbranch_scc1 2f\n\t"
            "s_barrier\n\t"
            "2:\n\t"
            "s_lshr_b32 %[nbar], %[nbar], 1\n\t"
            "s_sub_i32 %[it], %[it], 1\n\t"
            "s_cmp_lg_u32 %[it], 0\n\t"
            "s_cbranch_scc1 1b\n\t"
            "global_load_dword v44, %[goff], %[gb] sc1\n\t"
            PRIO(2, 3) PAIR(0, 28, 30, "") PAIR(1, 30, 28, "") PAIR(2, 28, 30, PRIO(0, 4)) PAIR_LAST(3, 30)
            "s_waitcnt vmcnt(0)"
            : "=&{v44}"(gate), "=&{v[80:95]}"(o), "=&{v[96:111]}"(o2), "=&{v[8:11]}"(qa0), "=&{v[12:13]}"(qb0), "+{v[32:35]}"(vlo),
              "+{v[36:39]}"(vhi), "+{v41}"(a_ka), "+{v42}"(a_kb), "+{v43}"(a_v), [nbar] "+s"(nbar), [it] "+s"(it)
            : [vm] "s"(vmask), [km] "s"(0xffffffffull), [one] "s"(ONE2), [sidx] "s"(0xDDDDDDDDu), [psw] "s"(wid), [goff] "v"(r32 * 4),
              [gb] "s"(ws + WS_G + qt * 128), [qs0] "s"(qs0), [qs1] "s"(qs1), [kap] "s"(kap)
            : "v14", "v15", "v16", "v17", "v18", "v19", "v20", "v21", "v22", "v23", "v24", "v25", "v26", "v27", "v28", "v29", "v30",
              "v31", "v40", "v48", "v49", "v50", "v51", "v52", "v53", "v54", "v55", "v56", "v57", "v58", "v59", "v60", "v61", "v62",
              "v63", "v64", "v65", "v66", "v67", "v68", "v69", "v70", "v71", "v72", "v73", "v74", "v75", "v76", "v77", "v78", "v79",
              "v112", "v113", "v114", "v115", "v116", "v117", "v118", "v119", "v120", "v121", "v122", "v123", "v124", "v125", "v126",
              "v127", "v128", "v129", "v130", "v131", "v132", "v133", "v134", "v135", "v136", "v137", "v138", "v139", "v140", "v141",
              "v142", "v143", "v144", "v145", "v146", "v147", "scc", "memory");
        __builtin_amdgcn_s_setprio(3);
#pragma unroll
        for (int i = 0; i < 3; ++i)
            *(LDS_AS f32x4*)(lds + P_OFF + (wid ^ 4) * 3072 + i * 1024 + lane * 16) = f32x4{o2[4 * i], o2[4 * i + 1], o2[4 * i + 2], o2[4 * i + 3]};
        RAW_BAR();
#pragma unroll
        for (int i = 0; i < 3; ++i) {
            const f32x4 t = *(const LDS_AS f32x4*)(lds + P_OFF + wid * 3072 + i * 1024 + lane * 16);
#pragma unroll
            for (int e = 0; e < 4; ++e) o[4 * i + e] += t[e];
        }
        const float gB = 1.f - gate;
        float l = row_sum(o);
        float asum = 0.f;
#pragma unroll
        for (int i = 0; i < 12; ++i) asum += fabsf(o[i]);
        if (__builtin_expect(__ballot(l >= 0x1p-64f && l <= 0x1p100f && asum <= 0x1p126f) == ~0ull, 1)) {
            store_o(o, gB * __builtin_amdgcn_rcpf(l), wrs, olane, (int)WS_O + (128 + qt * 2) * (int)O_BLK, head, hh);
        } else {
            const int kv = (int)WS_KV + (NH + head) * (int)KV_HEAD;
            u32x2 qr = hh ? u32x2{0u, 0u} : qb0;
            float mt2 = -INFINITY;
            o = f32x16{};
#pragma unroll 1
            for (int t = 0; t < 128; ++t) {
                const int kt = kv + (t & 63) * (int)KV_TILE;
                u32x2 kb = {ONE2, ONE2};
                if (!hh) kb = LD8(r32 * 8, kt + 1024);
                f32x16 sc = MFMA32(BF8(LD16(lane * 16, kt)), BF8(qa0), f32x16{});
                sc = MFMA32H(kb, qr, sc);
                if (t < 64) {
                    mt2 = max16(sc, mt2);
                    if (t == 63) qr = with_ref(qb0, xhalf_max(mt2), hh);
                } else {
                    u32x4 v0 = vfill, v1 = vfill;
                    if (r32 < 20) { v0 = LD16(voff, kt + 1280); v1 = LD16(voff, kt + 1920); }
#pragma unroll
                    for (int r = 0; r < 16; ++r) sc[r] = __builtin_amdgcn_exp2f(sc[r]);
                    o = SMFMAC(BF8(v0), LO8(sc), o);
                    o = SMFMAC(BF8(v1), HI8(sc), o);
                }
            }
            l = row_sum(o);
            asm volatile("" : "+v"(l));
            store_o(o, gB * __builtin_amdgcn_rcpf(l), wrs, olane, (int)WS_O + (128 + qt * 2) * (int)O_BLK, head, hh);
        }
        WAIT_V(0);
        RAW_BAR();
        __builtin_amdgcn_s_sleep(6);
        const int mb = wid & 3, kh = wid >> 2;
        const int osrc = (int)WS_O + (128 + omt * 4 + mb) * (int)O_BLK + kh * 10240;
        u32x4 of[10];
#pragma unroll
        for (int s = 0; s < 10; ++s) of[s] = LD16(lane * 16, osrc + s * 1024);
        SB();
        f32x4 acc[5];
        lds_ptr pp = lds + P_OFF + kh * 40960 + mb * 5120 + pslot;
#pragma unroll
        for (int j = 0; j < 5; ++j) acc[j] = f32x4{0.f, 0.f, 0.f, 0.f};
        if (kh) {
#pragma unroll
            for (int j = 0; j < 5; ++j) acc[j] = *(const LDS_AS f32x4*)(pp + j * 1024 + ((r16 + 2 * j + (g >> 1)) & 15) * 16);
        }
        lds_ptr wp = lds + kh * 51200 + lane * 16;
        bf16x8 wf[2][10];
#pragma unroll
        for (int j = 0; j < 10; ++j) wf[0][j] = lds_b128(wp + j * 1024);
        auto bad = [&](int s) {
            const u32x4 a = __builtin_elementwise_max(of[s], of[s + 1]);
            return __any(max(max(a[0], a[1]), max(a[2], a[3])) == ~0u);
        };
#pragma unroll
        for (int s = 0; s < 10; s += 2) {
            if (s < 8) {
#pragma unroll
                for (int j = 0; j < 10; ++j) wf[(s / 2 + 1) & 1][j] = lds_b128(wp + (s + 2) * 5120 + j * 1024);
            }
            if (bad(s)) {
#pragma unroll 1
                for (unsigned spins = 0; spins < (1u << 18); ++spins) {
                    asm volatile("" ::: "memory");
#pragma unroll
                    for (int t = 8; t >= s; t -= 2)
                        if (t == s || bad(t)) { of[t] = LD16(lane * 16, osrc + t * 1024); of[t + 1] = LD16(lane * 16, osrc + (t + 1) * 1024); }
                    if (!bad(s)) break;
                }
            }
            if (s == 2) fin_addr();
#pragma unroll
            for (int j = 0; j < 10; ++j) acc[j % 5] = MFMA16(wf[(s / 2) & 1][j], BF8(of[s + j / 5]), acc[j % 5]);
        }
#pragma unroll
        for (int j = 0; j < 5; ++j) *(LDS_AS f32x4*)(pp + j * 1024 + ((r16 + 2 * j + (g >> 1)) & 15) * 16) = acc[j];
    }
    RAW_BAR();
    if (tid < 640) {
        f32x4 t[6];
#pragma unroll
        for (int p = 0; p < 6; ++p) t[p] = *(const LDS_AS f32x4*)(lds + fq + (p >> 1) * 20480 + (p & 1) * 256);
        SB();
        const f32x4 a = t[0] + t[2] + t[4], b = t[1] + t[3] + t[5];
        const u32x4 v = {pack2(a[0], a[1]), pack2(a[2], a[3]), pack2(b[0], b[1]), pack2(b[2], b[3])};
        __builtin_amdgcn_raw_buffer_store_b128(v, __builtin_amdgcn_make_buffer_rsrc(out, 0, SEQ * DM * 2, 0x00020000), fo, 0, SC1);
    }
}

extern "C" void kernel_launch(void* const* d_in, const int* in_sizes, int n_in, void* d_out, int out_size, void* d_ws, size_t ws_size,
                              hipStream_t stream) {
    gated_mix_kernel<<<256, 768, 0, stream>>>((const u16*)d_in[0], (const u16*)d_in[1], (const u16*)d_in[4], (const u16*)d_in[2],
                                              (const u16*)d_in[5], (const u16*)d_in[6], (char*)d_ws, (u16*)d_out);
}
```
